```python
import torch

def _softmax(x, axis=-1):
    return torch.softmax(x, dim=axis)
def _gelu(x):
    return torch.nn.functional.gelu(x, approximate="tanh")
def _silu(x):
    return torch.nn.functional.silu(x)
def _sigmoid(x):
    return torch.sigmoid(x)
def _layernorm(x, axis=-1, eps=1e-5):
    m = x.mean(dim=axis, keepdim=True)
    v = x.var(dim=axis, keepdim=True, unbiased=False)
    return (x - m) / torch.sqrt(v + eps)
def _scatter_add(tgt, idx, src, axis=0):
    return tgt.clone().index_add_(axis, idx, src)
def _segment_sum(src, seg, n):
    out = torch.zeros((n,) + src.shape[1:], dtype=src.dtype)
    return out.index_add_(0, seg, src)
def _pad(x, pw):
    flat = tuple(v for pair in reversed(pw) for v in pair)
    return torch.nn.functional.pad(x, flat)

kernel_name = 'recurrent_scan__sliding_block_cached__glu_gated_geglu__prompt'

def setup_inputs():

    _w = (lambda a: (lambda t: t.to(torch.bfloat16) if t.is_floating_point() else t)(torch.as_tensor(a)))
    def _A_setup():
        B, Sq, D = 1, 2048, 640
        Hq, Hkv, Sk, Dh = 32, 32, 2048, 192
        assert Hq % Hkv == 0, (Hq, Hkv)
        return {
            "x": _w((lambda *s: torch.randn(*s, dtype=torch.bfloat16) * 0.05)(B, Sq, D)),
            "k_cache": _w((lambda *s: torch.randn(*s, dtype=torch.bfloat16) * 3.2)(B, Hkv, Sk, Dh)),
            "v_cache": _w((lambda *s: torch.randn(*s, dtype=torch.bfloat16) * 3.2)(B, Hkv, Sk, Dh)),
            "wq": _w((lambda *s: torch.randn(*s, dtype=torch.bfloat16) * 3.2)(D, Hq * Dh) * (D ** -0.5)),
            "wo": _w((lambda *s: torch.randn(*s, dtype=torch.bfloat16) * 3.2)(Hq * Dh, D) * ((Hq * Dh) ** -0.5)),
        }
    _A_in = _A_setup()

    def _B_setup():
        B, S, Dm, Df = 1, 2048, 640, 1536
        return {
            "x": _w((lambda *s: torch.randn(*s, dtype=torch.bfloat16) * 0.05)(B, S, Dm)),
            "w_gate": _w((lambda *s: torch.randn(*s, dtype=torch.bfloat16) * 0.4)(Dm, Df) * (Dm ** -0.5)),
            "w_up": _w((lambda *s: torch.randn(*s, dtype=torch.bfloat16) * 0.4)(Dm, Df) * (Dm ** -0.5)),
            "w_down": _w((lambda *s: torch.randn(*s, dtype=torch.bfloat16) * 0.4)(Df, Dm) * (Df ** -0.5)),
        }
    _B_in = _B_setup()

    return {
        "A_x": _A_in["x"],
        "A_k_cache": _A_in["k_cache"],
        "A_v_cache": _A_in["v_cache"],
        "A_wq": _A_in["wq"],
        "A_wo": _A_in["wo"],
        "B_x": _B_in["x"],
        "B_w_gate": _B_in["w_gate"],
        "B_w_up": _B_in["w_up"],
        "B_w_down": _B_in["w_down"],
    }

def reference(A_x, A_k_cache, A_v_cache, A_wq, A_wo, B_x, B_w_gate, B_w_up, B_w_down):
    def _A_ref(x, k_cache, v_cache, wq, wo):
        B, Sq, D = x.shape
        Hq, Dh = 32, 192
        Hkv, Sk = k_cache.shape[1], k_cache.shape[2]
        W = min(4096, max(256, Sk // 4))
        rep = Hq // Hkv
        q = (lambda x, *d: x.permute(*d))((x @ wq).reshape(B, Sq, Hq, Dh), 0, 2, 1, 3)
        k = (lambda x, n, axis: torch.repeat_interleave(x, n, dim=axis))(k_cache[:, :, Sk - W:, :], rep, axis=1)
        v = (lambda x, n, axis: torch.repeat_interleave(x, n, dim=axis))(v_cache[:, :, Sk - W:, :], rep, axis=1)
        scores = torch.einsum("bhqd,bhkd->bhqk", q, k) * (Dh ** -0.5)
        p = _softmax(scores, axis=-1)
        o = torch.einsum("bhqk,bhkd->bhqd", p, v)
        return (lambda x, *d: x.permute(*d))(o, 0, 2, 1, 3).reshape(B, Sq, Hq * Dh) @ wo

    def _B_ref(x, w_gate, w_up, w_down):
        g = torch.einsum("bsd,df->bsf", x, w_gate)
        u = torch.einsum("bsd,df->bsf", x, w_up)
        h = _gelu(g) * u
        return torch.einsum("bsf,fd->bsd", h, w_down)

    x = A_x
    xn = _layernorm(x, axis=-1)
    s = _A_ref(xn, A_k_cache, A_v_cache, A_wq, A_wo)
    g = _silu(_B_ref(xn, B_w_gate, B_w_up, B_w_down))
    return x + s * g
```

```cpp
#include <hip/hip_runtime.h>
#include <hip/hip_bf16.h>

typedef short bf16x8 __attribute__((ext_vector_type(8)));
typedef short s16x4 __attribute__((ext_vector_type(4)));
typedef float f32x4 __attribute__((ext_vector_type(4)));
typedef float f32x2 __attribute__((ext_vector_type(2)));
typedef unsigned u32x4 __attribute__((ext_vector_type(4)));
typedef unsigned u32x2 __attribute__((ext_vector_type(2)));
typedef __bf16 bf16x2_t __attribute__((ext_vector_type(2)));
typedef unsigned short u16;
#define DI __device__ __forceinline__
#define LDSP(T) __attribute__((address_space(3))) T*
#define MFMA16(a, b, c) __builtin_amdgcn_mfma_f32_16x16x32_bf16(a, b, c, 0, 0, 0)

constexpr int SEQ = 2048, DM = 640, NH = 32, DH = 192, SK = 2048, WIN = 512, DF = 1536, DQ = NH * DH;
constexpr int NKS = (DQ + DF) / 32;
constexpr size_t WS_XN = 0;
constexpr size_t WS_AP = 4u << 20;
constexpr size_t WS_WP = WS_AP + (size_t)128 * NKS * 1024;

DI unsigned cvtpk(float lo, float hi) { f32x2 v = {lo, hi}; bf16x2_t b = __builtin_convertvector(v, bf16x2_t); return __builtin_bit_cast(unsigned, b); }
DI float bf2f(u16 v) { return __uint_as_float((unsigned)v << 16); }
DI float rbf(float x) { return __uint_as_float(cvtpk(x, 0.f) << 16); }
DI unsigned rfl(unsigned x) { return __builtin_amdgcn_readfirstlane(x); }
DI void dma16(unsigned ldsdst, unsigned voff, __amdgpu_buffer_rsrc_t srd, unsigned soff) {
  asm volatile("s_mov_b32 m0, %0\n\ts_nop 0\n\tbuffer_load_dwordx4 %1, %2, %3 offen lds" :: "s"(ldsdst), "v"(voff), "s"(srd), "s"(soff) : "memory");
}
DI bf16x8 lds128(unsigned a) { return *(LDSP(const bf16x8))a; }
DI s16x4 ldstr(unsigned a) { return __builtin_amdgcn_ds_read_tr16_b64_v4i16((LDSP(s16x4))a); }
DI bf16x8 cat(s16x4 a, s16x4 b) { return __builtin_shufflevector(a, b, 0, 1, 2, 3, 4, 5, 6, 7); }
#define WAIT_BAR() asm volatile("s_waitcnt vmcnt(0) lgkmcnt(0)\n\ts_barrier" ::: "memory")

__global__ __launch_bounds__(256) void k_pre(const u16* __restrict__ x, const u16* __restrict__ wo, const u16* __restrict__ wd, u16* __restrict__ xn, u32x4* __restrict__ wp) {
  const int bid = blockIdx.x, tid = threadIdx.x;
  if (bid < 512) {
    const int row = bid * 4 + (tid >> 6), lane = tid & 63;
    const unsigned* xr = (const unsigned*)(x + (size_t)row * DM);
    float v[10]; float s = 0.f;
#pragma unroll
    for (int i = 0; i < 5; ++i) { unsigned w = xr[lane + 64 * i]; v[2 * i] = __uint_as_float(w << 16); v[2 * i + 1] = __uint_as_float(w & 0xffff0000u); s += v[2 * i] + v[2 * i + 1]; }
#pragma unroll
    for (int o = 32; o; o >>= 1) s += __shfl_xor(s, o);
    const float mean = s * (1.f / DM);
    float q = 0.f;
#pragma unroll
    for (int i = 0; i < 10; ++i) { float d = v[i] - mean; q += d * d; }
#pragma unroll
    for (int o = 32; o; o >>= 1) q += __shfl_xor(q, o);
    const float mb = rbf(mean), vb = rbf(q * (1.f / DM));
    const float den = rbf(sqrtf(rbf(vb + 1e-5f)));
    unsigned* xo = (unsigned*)(xn + (size_t)row * DM);
#pragma unroll
    for (int i = 0; i < 5; ++i) xo[lane + 64 * i] = cvtpk(rbf(v[2 * i] - mb) / den, rbf(v[2 * i + 1] - mb) / den);
  } else {
    const int idx = (bid - 512) * 256 + tid;
    const int lane = idx & 63, r = idx >> 6, nt = r % 40, ks = r / 40;
    const int n = nt * 16 + (lane & 15), g = lane >> 4;
    const u16* w = ks < 192 ? wo + (size_t)ks * 32 * DM : wd + (size_t)(ks - 192) * 32 * DM;
    u16 e[8];
#pragma unroll
    for (int j = 0; j < 8; ++j) e[j] = w[(16 * (j >> 2) + 4 * g + (j & 3)) * DM + n];
    u32x4 o;
#pragma unroll
    for (int j = 0; j < 4; ++j) o[j] = (unsigned)e[2 * j] | ((unsigned)e[2 * j + 1] << 16);
    wp[((size_t)nt * NKS + ks) * 64 + lane] = o;
  }
}

constexpr unsigned XS = 32768, WB = 18464, KGS = 3080, GOFF = 12320, STAGE = XS + 2 * WB;
constexpr unsigned SLOT0 = 0, SLOT1 = 163840 - STAGE;
constexpr unsigned KT = 24576, K_OFF = 0, V_OFF = 2 * KT, EX_OFF = 4 * KT;
constexpr int LDS_A = 163840;
constexpr float THR = 40.f;

__global__ __launch_bounds__(512, 2) void k_attn(const u16* __restrict__ xn, const u16* __restrict__ kc, const u16* __restrict__ vc, const u16* __restrict__ wq,
                                                 const u16* __restrict__ wg, const u16* __restrict__ wu, u16* __restrict__ ap) {
  extern __shared__ __attribute__((aligned(16))) char lds[];
  const unsigned tid = threadIdx.x, lane = tid & 63, w = rfl(tid >> 6);
  const unsigned bid = blockIdx.x, h = (bid & 7) * 4 + ((bid >> 3) & 3), qb = bid >> 5;
  const unsigned L0 = rfl((unsigned)(size_t)lds);
  const unsigned qg = w & 3, fh = w >> 2;
  const unsigned g = lane >> 4, l15 = lane & 15, qp = (lane >> 2) & 3, p = lane & 3;

  const __amdgpu_buffer_rsrc_t srdX = __builtin_amdgcn_make_buffer_rsrc((void*)xn, (short)0, SEQ * DM * 2, 0x00020000);
  const __amdgpu_buffer_rsrc_t srdQ = __builtin_amdgcn_make_buffer_rsrc((void*)wq, (short)0, DM * DQ * 2, 0x00020000);
  const unsigned e0 = w, e1 = 8 + (w & 3);
  const __amdgpu_buffer_rsrc_t srdG0 = __builtin_amdgcn_make_buffer_rsrc((void*)(((e0 / 3) & 1) ? wu : wg), (short)0, DM * DF * 2, 0x00020000);
  const __amdgpu_buffer_rsrc_t srdG1 = __builtin_amdgcn_make_buffer_rsrc((void*)(((e1 / 3) & 1) ? wu : wg), (short)0, DM * DF * 2, 0x00020000);
  const __amdgpu_buffer_rsrc_t srdK = __builtin_amdgcn_make_buffer_rsrc((void*)(kc + ((size_t)h * SK + (SK - WIN)) * DH), (short)0, WIN * DH * 2, 0x00020000);
  const __amdgpu_buffer_rsrc_t srdV = __builtin_amdgcn_make_buffer_rsrc((void*)(vc + ((size_t)h * SK + (SK - WIN)) * DH), (short)0, WIN * DH * 2, 0x00020000);

  const unsigned fsw = (4 * (w & 1) + (lane >> 4)) & 7;
  const unsigned voffX = (lane >> 3) * (DM * 2) + ((lane & 7) ^ fsw) * 16;
  const unsigned voffW = ((lane >> 2) & 7) * (DQ * 2) + (lane >> 5) * 64 + (lane & 3) * 16;
  auto guoff = [&](unsigned e) { const unsigned pp = e % 3, u = 2 * pp + (lane >> 5), kgp = u / 3, j = u % 3;
    const unsigned row = 16 * kgp + 8 * ((lane >> 1) & 1) + 4 * ((lane >> 4) & 1) + ((lane >> 2) & 3); return row * (DF * 2) + (16 * j + 8 * (lane & 1)) * 2; };
  const unsigned voffG0 = guoff(e0), voffG1 = guoff(e1);
  const unsigned gdst0 = XS + (e0 / 6) * WB + GOFF + ((e0 / 3) & 1) * 3072 + (e0 % 3) * 1024, gsrc0 = (e0 / 6) * 32 * (DF * 2) + h * 96;
  const unsigned gdst1 = XS + (e1 / 6) * WB + GOFF + ((e1 / 3) & 1) * 3072 + (e1 % 3) * 1024, gsrc1 = (e1 / 6) * 32 * (DF * 2) + h * 96;
  const unsigned voffK = (lane >> 3) * (DH * 2) + ((lane & 7) ^ fsw) * 16;
  const unsigned vrow = (lane >> 2) & 7;
  const unsigned voffV = vrow * (DH * 2) + (lane >> 5) * 64 + ((lane & 3) ^ ((vrow >> 2) << 1)) * 16;

  auto stage_dma = [&](unsigned s) {
    const unsigned sl = L0 + ((s & 1) ? SLOT1 : SLOT0), k0 = s * 64;
#pragma unroll
    for (int i = 0; i < 4; ++i) dma16(sl + (w + 8 * i) * 1024, voffX, srdX, (qb * 256 + 8 * (w + 8 * i)) * (DM * 2) + k0 * 2);
#pragma unroll
    for (int m = 0; m < 3; ++m) dma16(sl + XS + (w >> 2) * WB + (w & 3) * KGS + m * 1024, voffW, srdQ, (k0 + 8 * w) * (DQ * 2) + h * 384 + m * 128);
    dma16(sl + gdst0, voffG0, srdG0, k0 * (DF * 2) + gsrc0);
    if (w < 4) dma16(sl + gdst1, voffG1, srdG1, k0 * (DF * 2) + gsrc1);
  };
  auto k_dma = [&](unsigned t) {
    const unsigned sl = L0 + K_OFF + (t & 1) * KT;
#pragma unroll
    for (int m = 0; m < 3; ++m) dma16(sl + m * 8192 + w * 1024, voffK, srdK, (t * 64 + 8 * w) * (DH * 2) + m * 128);
  };
  auto v_dma = [&](unsigned t, unsigned vs) {
    const unsigned sl = L0 + V_OFF + vs * KT;
#pragma unroll
    for (int m = 0; m < 3; ++m) dma16(sl + w * 3072 + m * 1024, voffV, srdV, (t * 64 + 8 * w) * (DH * 2) + m * 128);
  };

  stage_dma(0);

  unsigned wa[9];
#pragma unroll
  for (int tt = 0; tt < 9; ++tt) {
    const unsigned T = fh ? 9 + tt : tt;
    const unsigned aq = g * KGS + (T >> 1) * 512 + qp * 64 + 16 * p + 8 * (T & 1);
    const unsigned j = (tt + 3) % 3, isU = tt >= 6;
    const unsigned ag = GOFF + isU * 3072 + ((g >> 1) * 3 + j) * 512 + qp * 64 + (g & 1) * 32 + 8 * p;
    wa[tt] = L0 + XS + ((fh && tt >= 3) ? ag : aq);
  }
  unsigned xa[2];
#pragma unroll
  for (int s = 0; s < 2; ++s) xa[s] = L0 + (64 * qg + l15) * 128 + (((4 * s + g) ^ ((l15 >> 1) & 7)) * 16);

  f32x4 acc[9][4];
#pragma unroll
  for (int tt = 0; tt < 9; ++tt)
#pragma unroll
    for (int qs = 0; qs < 4; ++qs) acc[tt][qs] = f32x4{0.f, 0.f, 0.f, 0.f};

  for (unsigned s = 0; s < 10; ++s) {
    WAIT_BAR();
    if (s < 9) stage_dma(s + 1); else { k_dma(0); v_dma(0, 0); }
    const unsigned so = (s & 1) ? SLOT1 : SLOT0;
#pragma unroll
    for (int sub = 0; sub < 2; ++sub) {
      bf16x8 xb[4];
#pragma unroll
      for (int qs = 0; qs < 4; ++qs) xb[qs] = lds128(xa[sub] + so + qs * 2048);
#pragma unroll
      for (int tt = 0; tt < 9; ++tt) {
        const bf16x8 wf = cat(ldstr(wa[tt] + so + sub * WB), ldstr(wa[tt] + so + sub * WB + 256));
#pragma unroll
        for (int qs = 0; qs < 4; ++qs) acc[tt][qs] = MFMA16(wf, xb[qs], acc[tt][qs]);
      }
    }
  }
  constexpr float QS = 0.07216878364870322f * 1.4426950408889634f;
  asm volatile("s_waitcnt lgkmcnt(0)\n\ts_barrier" ::: "memory");
  u32x2 qh[2][12];
  const unsigned ex = L0 + EX_OFF + qg * 12288 + lane * 8;
  auto pk = [&](f32x4 v) { return u32x2{cvtpk(v[0] * QS, v[1] * QS), cvtpk(v[2] * QS, v[3] * QS)}; };
  if (fh == 0) {
#pragma unroll
    for (int T = 0; T < 9; ++T)
#pragma unroll
      for (int a = 0; a < 2; ++a) { qh[a][T] = pk(acc[T][a]); *(LDSP(u32x2))(ex + (T * 2 + a) * 512) = pk(acc[T][2 + a]); }
  } else {
#pragma unroll
    for (int T = 9; T < 12; ++T)
#pragma unroll
      for (int a = 0; a < 2; ++a) { qh[a][T] = pk(acc[T - 9][2 + a]); *(LDSP(u32x2))(ex + (T * 2 + a) * 512) = pk(acc[T - 9][a]); }
  }
  WAIT_BAR();
  k_dma(1); v_dma(1, 1);
  if (fh == 0) {
#pragma unroll
    for (int T = 9; T < 12; ++T)
#pragma unroll
      for (int a = 0; a < 2; ++a) qh[a][T] = *(LDSP(const u32x2))(ex + (T * 2 + a) * 512);
  } else {
#pragma unroll
    for (int T = 0; T < 9; ++T)
#pragma unroll
      for (int a = 0; a < 2; ++a) qh[a][T] = *(LDSP(const u32x2))(ex + (T * 2 + a) * 512);
#pragma unroll
    for (int j = 0; j < 3; ++j) {
      const unsigned Tg = 3 * h + j;
#pragma unroll
      for (int qs = 0; qs < 4; ++qs) {
        float hv[4];
#pragma unroll
        for (int e = 0; e < 4; ++e) {
          const float gv = acc[3 + j][qs][e], uv = acc[6 + j][qs][e];
          const float z = gv * (-2.302208198144325f + -0.1029432397435085f * gv * gv);
          hv[e] = gv * __builtin_amdgcn_rcpf(1.f + __builtin_amdgcn_exp2f(z)) * uv;
        }
        const unsigned qt = qb * 16 + qg * 4 + qs;
        *(u32x2*)(ap + (((size_t)qt * NKS + 192 + (Tg >> 1)) * 64 + lane) * 8 + (Tg & 1) * 4) = u32x2{cvtpk(hv[0], hv[1]), cvtpk(hv[2], hv[3])};
      }
    }
  }
  bf16x8 qf[2][6];
#pragma unroll
  for (int a = 0; a < 2; ++a)
#pragma unroll
    for (int i = 0; i < 6; ++i) qf[a][i] = __builtin_bit_cast(bf16x8, u32x4{qh[a][2 * i][0], qh[a][2 * i][1], qh[a][2 * i + 1][0], qh[a][2 * i + 1][1]});

  unsigned kb[2], vb[2];
#pragma unroll
  for (int s = 0; s < 2; ++s) {
    kb[s] = L0 + K_OFF + l15 * 128 + (((4 * s + g) ^ ((l15 >> 1) & 7)) * 16);
    vb[s] = L0 + V_OFF + (g >> 1) * 3072 + (4 * (g & 1) + qp) * 64 + ((s ^ (g & 1)) * 32) + 8 * p;
  }
  f32x4 o[12][2];
#pragma unroll
  for (int dt = 0; dt < 12; ++dt) { o[dt][0] = f32x4{0.f, 0.f, 0.f, 0.f}; o[dt][1] = f32x4{0.f, 0.f, 0.f, 0.f}; }
  f32x4 negm[2] = {f32x4{0.f, 0.f, 0.f, 0.f}, f32x4{0.f, 0.f, 0.f, 0.f}};
  float lsum[2] = {0.f, 0.f};
  unsigned vs_cur = 0;
  for (unsigned t = 0; t < 8; ++t) {
    const unsigned vs_n1 = vs_cur == 2 ? 0 : vs_cur + 1, vs_n2 = vs_n1 == 2 ? 0 : vs_n1 + 1;
    if (fh == 0 && t > 0) { WAIT_BAR(); if (t < 7) { k_dma(t + 1); v_dma(t + 1, vs_n1); } }
    const unsigned ko = (t & 1) * KT;
    f32x4 sc[4][2];
#pragma unroll
    for (int kt = 0; kt < 4; ++kt)
#pragma unroll
      for (int i = 0; i < 6; ++i) {
        const bf16x8 kf = lds128(kb[i & 1] + ko + (i >> 1) * 8192 + kt * 2048);
#pragma unroll
        for (int a = 0; a < 2; ++a) sc[kt][a] = MFMA16(kf, qf[a][i], i == 0 ? negm[a] : sc[kt][a]);
      }
    if (fh == 1 && t < 7) { WAIT_BAR(); if (t < 6) { k_dma(t + 2); v_dma(t + 2, vs_n2); } }
    float mx[2];
#pragma unroll
    for (int a = 0; a < 2; ++a) {
      float m = fmaxf(fmaxf(sc[0][a][0], sc[0][a][1]), fmaxf(sc[0][a][2], sc[0][a][3]));
#pragma unroll
      for (int kt = 1; kt < 4; ++kt) m = fmaxf(fmaxf(fmaxf(m, sc[kt][a][0]), sc[kt][a][1]), fmaxf(sc[kt][a][2], sc[kt][a][3]));
      mx[a] = m;
    }
    if (t == 0 || __any(fmaxf(mx[0], mx[1]) > THR)) {
#pragma unroll
      for (int a = 0; a < 2; ++a) {
        float m = mx[a]; m = fmaxf(m, __shfl_xor(m, 16)); m = fmaxf(m, __shfl_xor(m, 32));
        const float dl = t == 0 ? m : fmaxf(m, 0.f);
        const float f = t == 0 ? 1.f : __builtin_amdgcn_exp2f(-dl);
#pragma unroll
        for (int kt = 0; kt < 4; ++kt) sc[kt][a] -= dl;
        negm[a] -= dl; lsum[a] *= f;
#pragma unroll
        for (int dt = 0; dt < 12; ++dt) o[dt][a] *= f;
      }
    }
    bf16x8 pf[2][2];
#pragma unroll
    for (int a = 0; a < 2; ++a) {
#pragma unroll
      for (int kt = 0; kt < 4; ++kt)
#pragma unroll
        for (int e = 0; e < 4; ++e) { sc[kt][a][e] = __builtin_amdgcn_exp2f(sc[kt][a][e]); lsum[a] += sc[kt][a][e]; }
#pragma unroll
      for (int u = 0; u < 2; ++u)
        pf[u][a] = __builtin_bit_cast(bf16x8, u32x4{cvtpk(sc[2 * u][a][0], sc[2 * u][a][1]), cvtpk(sc[2 * u][a][2], sc[2 * u][a][3]),
                                                    cvtpk(sc[2 * u + 1][a][0], sc[2 * u + 1][a][1]), cvtpk(sc[2 * u + 1][a][2], sc[2 * u + 1][a][3])});
    }
    const unsigned vo = vs_cur * KT;
#pragma unroll
    for (int u = 0; u < 2; ++u)
#pragma unroll
      for (int dt = 0; dt < 12; ++dt) {
        const unsigned va = vb[dt & 1] + vo + u * 12288 + (dt >> 1) * 512;
        const bf16x8 vf = cat(ldstr(va), ldstr(va + 6144));
#pragma unroll
        for (int a = 0; a < 2; ++a) o[dt][a] = MFMA16(vf, pf[u][a], o[dt][a]);
      }
    vs_cur = vs_n1;
  }
#pragma unroll
  for (int a = 0; a < 2; ++a) {
    float l = lsum[a]; l += __shfl_xor(l, 16); l += __shfl_xor(l, 32);
    const float il = 1.f / l;
    const unsigned qt = qb * 16 + qg * 4 + fh * 2 + a;
#pragma unroll
    for (int i = 0; i < 6; ++i) {
      const f32x4 x0 = o[2 * i][a] * il, x1 = o[2 * i + 1][a] * il;
      *(u32x4*)(ap + (((size_t)qt * NKS + h * 6 + i) * 64 + lane) * 8) = u32x4{cvtpk(x0[0], x0[1]), cvtpk(x0[2], x0[3]), cvtpk(x1[0], x1[1]), cvtpk(x1[2], x1[3])};
    }
  }
}

constexpr int PD = 6;
__global__ __launch_bounds__(256, 1) void k_out(const bf16x8* __restrict__ ap, const bf16x8* __restrict__ wp, const u16* __restrict__ x, u16* __restrict__ out) {
  extern __shared__ __attribute__((aligned(16))) char lds[];
  const unsigned tid = threadIdx.x, lane = tid & 63, w = rfl(tid >> 6);
  const unsigned bid = blockIdx.x, qblk = (bid & 7) * 4 + ((bid >> 3) & 3), nb = bid >> 5;
  const bf16x8* pa = ap + (size_t)qblk * 4 * NKS * 64 + lane;
  const bf16x8* pw = wp + (size_t)nb * 5 * NKS * 64 + lane;
  f32x4 acc[2][4][5];
#pragma unroll
  for (int z = 0; z < 2; ++z)
#pragma unroll
    for (int i = 0; i < 4; ++i)
#pragma unroll
      for (int j = 0; j < 5; ++j) acc[z][i][j] = f32x4{0.f, 0.f, 0.f, 0.f};
  bf16x8 fa[PD][4], fw[PD][5];
  auto ksof = [&](unsigned i) { return i < 12 ? 192 + w + 4 * i : w + 4 * (i - 12); };
  auto load = [&](int slot, unsigned i) {
    const unsigned ks = ksof(i);
#pragma unroll
    for (int a = 0; a < 4; ++a) fa[slot][a] = pa[((size_t)a * NKS + ks) * 64];
#pragma unroll
    for (int b = 0; b < 5; ++b) fw[slot][b] = pw[((size_t)b * NKS + ks) * 64];
  };
#pragma unroll
  for (int s = 0; s < PD; ++s) load(s, s);
#pragma unroll 1
  for (unsigned i0 = 0; i0 < 12; i0 += PD) {
#pragma unroll
    for (int s = 0; s < PD; ++s) {
#pragma unroll
      for (int a = 0; a < 4; ++a)
#pragma unroll
        for (int b = 0; b < 5; ++b) acc[0][a][b] = MFMA16(fw[s][b], fa[s][a], acc[0][a][b]);
      load(s, i0 + s + PD);
    }
  }
#pragma unroll 1
  for (unsigned i0 = 12; i0 < 60; i0 += PD) {
#pragma unroll
    for (int s = 0; s < PD; ++s) {
#pragma unroll
      for (int a = 0; a < 4; ++a)
#pragma unroll
        for (int b = 0; b < 5; ++b) acc[1][a][b] = MFMA16(fw[s][b], fa[s][a], acc[1][a][b]);
      const unsigned nx = i0 + s + PD;
      load(s, nx < 60 ? nx : 59);
    }
  }
  const unsigned L0 = (unsigned)(size_t)lds;
#pragma unroll
  for (int z = 0; z < 2; ++z)
#pragma unroll
    for (int a = 0; a < 4; ++a)
#pragma unroll
      for (int b = 0; b < 5; ++b) *(LDSP(f32x4))(L0 + ((((w * 2 + z) * 4 + a) * 5 + b) * 64 + lane) * 16) = acc[z][a][b];
  __syncthreads();
  const unsigned q = qblk * 64 + w * 16 + (lane & 15), g = lane >> 4;
#pragma unroll
  for (int b = 0; b < 5; ++b) {
    f32x4 d = {0.f, 0.f, 0.f, 0.f}, s = {0.f, 0.f, 0.f, 0.f};
#pragma unroll
    for (int v = 0; v < 4; ++v) {
      d += *(LDSP(const f32x4))(L0 + ((((v * 2 + 0) * 4 + w) * 5 + b) * 64 + lane) * 16);
      s += *(LDSP(const f32x4))(L0 + ((((v * 2 + 1) * 4 + w) * 5 + b) * 64 + lane) * 16);
    }
    const size_t off = (size_t)q * DM + nb * 80 + b * 16 + 4 * g;
    const u32x2 xv = *(const u32x2*)(x + off);
    float r[4];
#pragma unroll
    for (int e = 0; e < 4; ++e) {
      const float xe = __uint_as_float((e & 1) ? (xv[e >> 1] & 0xffff0000u) : (xv[e >> 1] << 16));
      const float gt = d[e] * __builtin_amdgcn_rcpf(1.f + __builtin_amdgcn_exp2f(-1.4426950408889634f * d[e]));
      r[e] = xe + s[e] * gt;
    }
    *(u32x2*)(out + off) = u32x2{cvtpk(r[0], r[1]), cvtpk(r[2], r[3])};
  }
}

extern "C" void kernel_launch(void* const* d_in, const int* in_sizes, int n_in, void* d_out, int out_size, void* d_ws, size_t ws_size, hipStream_t stream) {
  const u16* x = (const u16*)d_in[0]; const u16* kc = (const u16*)d_in[1]; const u16* vc = (const u16*)d_in[2];
  const u16* wq = (const u16*)d_in[3]; const u16* wo = (const u16*)d_in[4];
  const u16* wg = (const u16*)d_in[6]; const u16* wu = (const u16*)d_in[7]; const u16* wd = (const u16*)d_in[8];
  char* ws = (char*)d_ws;
  u16* xn = (u16*)(ws + WS_XN); u16* ap = (u16*)(ws + WS_AP); u16* wp = (u16*)(ws + WS_WP);
  (void)hipFuncSetAttribute((const void*)k_attn, hipFuncAttributeMaxDynamicSharedMemorySize, LDS_A);
  (void)hipFuncSetAttribute((const void*)k_out, hipFuncAttributeMaxDynamicSharedMemorySize, 163840);
  k_pre<<<512 + 40 * NKS * 64 / 256, 256, 0, stream>>>(x, wo, wd, xn, (u32x4*)wp);
  k_attn<<<256, 512, LDS_A, stream>>>(xn, kc, vc, wq, wg, wu, ap);
  k_out<<<256, 256, 163840, stream>>>((const bf16x8*)ap, (const bf16x8*)wp, x, (u16*)d_out);
}
```

```cpp
#include <hip/hip_runtime.h>
#include <hip/hip_bf16.h>

typedef short bf16x8 __attribute__((ext_vector_type(8)));
typedef float f32x4 __attribute__((ext_vector_type(4)));
typedef float f32x2 __attribute__((ext_vector_type(2)));
typedef unsigned u32x4 __attribute__((ext_vector_type(4)));
typedef unsigned u32x2 __attribute__((ext_vector_type(2)));
typedef unsigned u32x16 __attribute__((ext_vector_type(16)));
typedef __bf16 bf16x2_t __attribute__((ext_vector_type(2)));
typedef unsigned short u16;
#define DI __device__ __forceinline__
#define LDSP(T) __attribute__((address_space(3))) T*

constexpr int SEQ = 2048, DM = 640, NH = 32, DH = 192, SK = 2048, WIN = 512, DF = 1536, DQ = NH * DH;
constexpr int NKS = (DQ + DF) / 32;
constexpr size_t WS_XF = 0;
constexpr size_t WS_AP = 4u << 20;
constexpr size_t AP_BYTES = (size_t)128 * NKS * 1024;
constexpr size_t WS_WP = WS_AP + AP_BYTES;
constexpr size_t WS_GU = WS_WP + (size_t)40 * NKS * 1024;
constexpr size_t WS_PB = WS_GU + (size_t)32 * 20 * 6 * 1024;
constexpr size_t WS_CNT = WS_PB + (size_t)64 * 8 * 8 * 5 * 1024;
constexpr float QS = 0.07216878364870322f * 1.4426950408889634f;

DI unsigned cvtpk(float lo, float hi) { f32x2 v = {lo, hi}; bf16x2_t b = __builtin_convertvector(v, bf16x2_t); return __builtin_bit_cast(unsigned, b); }
DI float rbf(float x) { return __uint_as_float(cvtpk(x, 0.f) << 16); }
DI unsigned rfl(unsigned x) { return __builtin_amdgcn_readfirstlane(x); }
DI u32x4 mksrd(const void* p, unsigned bytes) { const size_t a = (size_t)p; return u32x4{rfl((unsigned)a), rfl((unsigned)(a >> 32) & 0xffffu), rfl(bytes), 0x00020000u}; }

template <int CTRL> DI float dpp_add(float x) { return x + __int_as_float(__builtin_amdgcn_update_dpp(0, __float_as_int(x), CTRL, 0xf, 0xf, false)); }
DI float row16_sum(float x) { x = dpp_add<0x128>(x); x = dpp_add<0x124>(x); x = dpp_add<0x122>(x); return dpp_add<0x121>(x); }
DI float wave_sum(float x) {
  x = row16_sum(x);
  return __int_as_float(__builtin_amdgcn_readlane(__float_as_int(x), 0)) + __int_as_float(__builtin_amdgcn_readlane(__float_as_int(x), 16)) + __int_as_float(__builtin_amdgcn_readlane(__float_as_int(x), 32));
}
typedef short s16x4 __attribute__((ext_vector_type(4)));
DI s16x4 ldstr(unsigned a) { return __builtin_amdgcn_ds_read_tr16_b64_v4i16((LDSP(s16x4))a); }
template <int NCH> DI void stage_rows(const u16* __restrict__ src, int ld, char* tile, int tid) {
  u32x4 r[NCH / 8];
#pragma unroll
  for (int i = 0; i < NCH / 8; ++i) { const int c = tid + 256 * i, row = c / NCH, ch = c % NCH; r[i] = *(const u32x4*)(src + (size_t)row * ld + ch * 8); }
#pragma unroll
  for (int i = 0; i < NCH / 8; ++i) { const int c = tid + 256 * i, row = c / NCH, ch = c % NCH; *(u32x4*)(tile + row * (NCH * 16 + 16) + ch * 16) = r[i]; }
  __syncthreads();
}
DI void st_wt(u32x4* p, u32x4 v) { asm volatile("global_store_dwordx4 %0, %1, off sc1\n\ts_nop 1" :: "v"(p), "v"(v) : "memory"); }
__global__ __launch_bounds__(512) void k_pre(const u16* __restrict__ x, const u16* __restrict__ wg, const u16* __restrict__ wu, u32x4* __restrict__ xf, u32x4* __restrict__ guf, unsigned* __restrict__ cnt) {
  __shared__ __attribute__((aligned(16))) char tiles[2 * 32 * (24 * 16 + 16)];
  const int bid = blockIdx.x, tid = threadIdx.x;
  asm volatile("" :: "s"(x), "s"(wg), "s"(wu), "s"(xf), "s"(guf), "s"(cnt));
  if (bid >= 160) {
    const int lane = tid & 63, wv = tid >> 6, rb = bid - 160;
    const bool on = lane < 40;
    const u32x4* xr = (const u32x4*)(x + (size_t)(rb * 8 + wv) * DM);
    u32x4 raw[2] = {u32x4{0u, 0u, 0u, 0u}, u32x4{0u, 0u, 0u, 0u}};
    if (on) { raw[0] = xr[lane]; raw[1] = xr[lane + 40]; }
    if (rb < 8) st_wt((u32x4*)cnt + rb * 512 + tid, u32x4{0u, 0u, 0u, 0u});
    float v[16]; float s = 0.f;
#pragma unroll
    for (int i = 0; i < 2; ++i)
#pragma unroll
      for (int j = 0; j < 4; ++j) { v[i * 8 + 2 * j] = __uint_as_float(raw[i][j] << 16); v[i * 8 + 2 * j + 1] = __uint_as_float(raw[i][j] & 0xffff0000u); s += v[i * 8 + 2 * j] + v[i * 8 + 2 * j + 1]; }
    const float mean = wave_sum(s) * (1.f / DM);
    float sq = 0.f;
#pragma unroll
    for (int i = 0; i < 16; ++i) { const float d = v[i] - mean; sq += d * d; }
    const float var = wave_sum(on ? sq : 0.f) * (1.f / DM);
    const float mb = rbf(mean);
    const float sc = QS * __builtin_amdgcn_rcpf(rbf(__builtin_amdgcn_sqrtf(rbf(rbf(var) + 1e-5f))));
    u32x4* stg = (u32x4*)tiles;
    if (on) {
#pragma unroll
      for (int i = 0; i < 2; ++i) {
        u32x4 o;
#pragma unroll
        for (int j = 0; j < 4; ++j) { const unsigned c = cvtpk(v[i * 8 + 2 * j] - mb, v[i * 8 + 2 * j + 1] - mb); o[j] = cvtpk(__uint_as_float(c << 16) * sc, __uint_as_float(c & 0xffff0000u) * sc); }
        stg[(lane + 40 * i) * 9 + wv] = o;
      }
    }
    __syncthreads();
    u32x4* dst = xf + (rb >> 1) * 1280 + (rb & 1) * 8 + (tid & 7);
    st_wt(dst + (tid >> 3) * 16, stg[(tid >> 3) * 9 + (tid & 7)]);
    if (tid < 128) st_wt(dst + (64 + (tid >> 3)) * 16, stg[(64 + (tid >> 3)) * 9 + (tid & 7)]);
  } else {
    const int u = tid >> 8, t2 = tid & 255, cb = bid & 7, ks = bid >> 3;
    char* tile = tiles + u * 32 * (24 * 16 + 16);
    const u16* src = (u ? wu : wg) + (size_t)ks * 32 * DF + cb * 192;
    stage_rows<24>(src, DF, tile, t2);
    const int lane = tid & 63, wv = t2 >> 6, g = lane >> 4;
    const unsigned base = (unsigned)(size_t)tile + (8 * g + ((lane >> 2) & 3)) * (24 * 16 + 16) + (lane & 3) * 8;
#pragma unroll
    for (int j = 0; j < 3; ++j) {
      const unsigned a0 = base + (wv * 3 + j) * 32;
      const s16x4 lo = ldstr(a0), hi = ldstr(a0 + 4 * (24 * 16 + 16));
      st_wt(&guf[(((cb * 4 + wv) * 20 + ks) * 6 + j * 2 + u) * 64 + lane], __builtin_bit_cast(u32x4, __builtin_shufflevector(lo, hi, 0, 1, 2, 3, 4, 5, 6, 7)));
    }
  }
}

struct Tables {
  unsigned s[2048 * 64];
  unsigned l[8 * 1024];
  constexpr Tables() : s(), l() {
    constexpr unsigned PBO = (unsigned)WS_PB, FLO = (unsigned)WS_CNT, APO = (unsigned)WS_AP, WPO = (unsigned)WS_WP;
    for (unsigned i = 0; i < 2048; ++i) {
      const unsigned b = i >> 3, w = i & 7, h = (b & 7) * 4 + ((b >> 3) & 3), qb = b >> 5;
      const unsigned kq = h >> 3, mt = 2 * qb + ((h >> 2) & 1), nb = h & 3, tl = mt * 4 + nb, qg = w & 3, par = w >> 2, q0 = 2 * qg + par, q1 = 2 * qg + 1 - par;
      unsigned* o = s + i * 64;
      o[0] = par * 40960 + qg * 10240; o[1] = (1 - par) * 40960 + qg * 10240; o[2] = par * 10240; o[3] = w < 2 ? 1u : 0u;
      o[4] = w * 5120; o[5] = WPO + ((192 + 12 * kq + (w >> 1)) * 40 + 10 * nb + 5 * (w & 1)) * 1024;
      o[6] = APO + ((mt * 8 + q0) * NKS + 192 + 12 * kq + par) * 1024; o[7] = APO + ((mt * 8 + q1) * NKS + 192 + 12 * kq + par) * 1024;
      o[8] = (qg == kq ? 0x10000000u : 0u) + PBO + ((tl * 8 + kq) * 8 + q0) * 5120; o[9] = qg ^ kq; o[10] = FLO + 32768 + (tl * 4 + qg) * 128; o[11] = FLO + 32768 + (tl * 4 + kq) * 128;
      o[12] = WPO + ((48 * kq + (w >> 1)) * 40 + 10 * nb + 5 * (w & 1)) * 1024;
      const unsigned rp = qb * 8 + w, rc = mt * 4 + qg;
      o[13] = FLO + 4096 + (rp >> 4) * 4096 + (rp & 15) * 128 + h * 4; o[14] = FLO + kq * 256; o[15] = FLO + 4096 + (rc >> 4) * 4096 + (rc & 15) * 128 + kq * 32;
      for (unsigned r = 0; r < 2; ++r) {
        const unsigned u = r ? 8 + (w & 1) : w, hq = u / 5, pp = u % 5, qs = 2 * kq + hq, bad = (r && w >= 2) ? 0x10000000u : 0u;
        o[16 + r] = PBO + (tl * 64 + qs) * 5120 + pp * 1024 + bad;
        o[21 + r] = ((mt * 128 + qs * 16) * DM + nb * 160 + pp * 32) * 2 + bad;
        o[23 + r] = 81920 + hq * 10240 + pp * 2048;
      }
      for (unsigned sl = 0; sl < 3; ++sl) o[18 + sl] = (sl + (sl >= kq ? 1 : 0)) * 40960;
      o[25] = (192 - 36 * kq) * 1024; o[26] = APO + ((qb * 16 + 2 * w) * NKS + 6 * h) * 1024;
      for (unsigned j = 0; j < 3; ++j) { const unsigned Tg = 3 * h + j; o[27 + j] = APO + ((qb * 16 + 2 * w) * NKS + 192 + (Tg >> 1)) * 1024 + (Tg & 1) * 512; }
      o[30] = FLO + ((w != 0 || b >= NKS) ? 0x10000000u : b < 192 ? (b / 48) * 256 + (b % 48) * 4 : ((b - 192) / 12) * 256 + (48 + (b - 192) % 12) * 4);
      o[31] = PBO + ((tl * 8 + 4 + kq) * 8 + q0) * 5120; o[41] = par ? 0u - 20480u : 20480u;
      const unsigned off = b < NKS ? 0u : 0x10000000u, kvh = (h * SK + (SK - WIN)) * (DH * 2);
      o[32] = off + (b < 192 ? b : b - 192) * (32 * DM * 2) + 8 * (w >> 1) * (DM * 2) + (w & 1) * 128; o[33] = w * 1024; o[34] = off + WPO + (b * 40 + w) * 1024;
      o[35] = b < 192 ? DQ * DM * 2 : DF * DM * 2; o[36] = b < 192 ? 0u : 1u;
      o[37] = kvh + 8 * w * 384; o[38] = par ^ 1u; o[39] = qg * 6144; o[40] = kvh + qg * 6144;
    }
    for (unsigned i = 0; i < 512; ++i) {
      const unsigned lw = i >> 6, lane = i & 63, g = lane >> 4, l15 = lane & 15, qp = (lane >> 2) & 3, p = lane & 3, vrow = (lane >> 2) & 7;
      const unsigned sw = ((lane & 3) ^ ((vrow >> 2) << 1)) * 16 + (lane >> 5) * 64, vo = vrow * (DH * 2) + sw, k0 = (lane >> 3) * (DH * 2);
      const unsigned kb = (l15 >> 3) * 3072 + (l15 & 7) * 128, vb = (g >> 1) * 3072 + (4 * (g & 1) + qp) * 64 + 8 * p;
      unsigned* t = l + lw * 1024;
      t[lane * 4] = vrow * (DM * 2) + sw; t[lane * 4 + 1] = 147840 + (g >> 1) * 2048 + (lw >> 1) * 512 + (4 * (g & 1) + qp) * 64 + ((p ^ (2 * (g & 1))) * 16) + 8 * (lw & 1);
      t[lane * 4 + 2] = lw < 4 ? k0 + ((lane & 7) ^ g) * 16 : vo; t[lane * 4 + 3] = k0 + ((lane & 7) ^ ((4 * (lw & 1) + g) & 7)) * 16;
      t[256 + lane * 4] = g * 3080 + qp * 64 + 16 * p; t[256 + lane * 4 + 1] = kb + ((g ^ ((l15 >> 1) & 7)) * 16);
      t[256 + lane * 4 + 2] = kb + (((4 + g) ^ ((l15 >> 1) & 7)) * 16); t[256 + lane * 4 + 3] = vb + (g & 1) * 32;
      t[512 + lane] = vo; t[576 + lane] = vb + ((g & 1) ^ 1) * 32; t[640 + lane] = lw < 4 ? k0 + ((lane & 7) ^ (4 + g)) * 16 : vo;
    }
  }
};
static __device__ const Tables g_tab = Tables();

constexpr int LDS_A = 163840;
typedef unsigned u32x8 __attribute__((ext_vector_type(8)));
typedef unsigned u32x32 __attribute__((ext_vector_type(32)));
constexpr unsigned WS_END = (unsigned)WS_CNT + 65536;
DI u32x2 ptr2(const void* p) { const size_t a = (size_t)p; return u32x2{rfl((unsigned)a), rfl((unsigned)(a >> 32))}; }
__global__ __launch_bounds__(512, 2) void k_main(char* __restrict__ ws, const u16* __restrict__ wq, const u16* __restrict__ kc, const u16* __restrict__ vc, const u16* __restrict__ wo,
                                                 const u16* __restrict__ wd, const u16* __restrict__ x, u16* __restrict__ out) {
  extern __shared__ __attribute__((aligned(16))) char lds[];
  const unsigned tid = threadIdx.x, lane = tid & 63, w = rfl(tid >> 6), bid = blockIdx.x, h = (bid & 7) * 4 + ((bid >> 3) & 3);
  const unsigned L0 = rfl((unsigned)(size_t)lds);
  const u32x4 srdA = mksrd(ws, WS_END), srdQ = mksrd(wq, DM * DQ * 2), srdT = mksrd(&g_tab, sizeof(Tables));
  u32x2 pK = ptr2(kc), pV = ptr2(vc), pO = ptr2(wo), pX = ptr2(x), pOut = ptr2(out), pD = ptr2(wd);
  u32x4 s36 = {pD[0], pD[1], rfl(2048 * 256 + w * 4096), 0u};
  u32x4 s60 = {L0, rfl(L0 + w * 3080), rfl(L0 + w * 3072), rfl((unsigned)WS_XF + ((bid >> 5) * 16 + 2 * w) * 20480)};
  u32x2 s64 = {rfl((unsigned)WS_GU + h * 122880 + w * 3072), rfl(8 * w * (DQ * 2) + h * 384)};
  unsigned s34 = rfl((bid * 8 + w) * 256);
  u32x2 v240 = {lane * 16, ((lane >> 2) & 7) * (DQ * 2) + (lane >> 5) * 64 + (lane & 3) * 16};
  unsigned v247 = L0 + lane * 16, v254 = 0;
  asm volatile("s_nop 4\n\t"
"s_add_u32 m0, s62, 0\n\t"
"s_add_u32 s76, s64, 0\n\t"
"buffer_load_dwordx4 v240, s[56:59], s76 offen lds\n\t"
"s_add_u32 m0, s62, 1024\n\t"
"s_add_u32 s76, s64, 1024\n\t"
"buffer_load_dwordx4 v240, s[56:59], s76 offen lds\n\t"
"s_add_u32 m0, s62, 2048\n\t"
"s_add_u32 s76, s64, 2048\n\t"
"buffer_load_dwordx4 v240, s[56:59], s76 offen lds\n\t"
"s_add_u32 s87, s63, 0\n\t"
"buffer_load_dwordx4 v[144:147], v240, s[56:59], s87 offen offset:0\n\t"
"s_add_u32 s87, s63, 20480\n\t"
"buffer_load_dwordx4 v[148:151], v240, s[56:59], s87 offen offset:0\n\t"
"s_add_u32 s87, s63, 1024\n\t"
"buffer_load_dwordx4 v[152:155], v240, s[56:59], s87 offen offset:0\n\t"
"s_add_u32 s87, s63, 21504\n\t"
"buffer_load_dwordx4 v[156:159], v240, s[56:59], s87 offen offset:0\n\t"
"s_add_u32 s87, s63, 2048\n\t"
"buffer_load_dwordx4 v[160:163], v240, s[56:59], s87 offen offset:0\n\t"
"s_add_u32 s87, s63, 22528\n\t"
"buffer_load_dwordx4 v[164:167], v240, s[56:59], s87 offen offset:0\n\t"
"s_add_u32 s87, s63, 3072\n\t"
"buffer_load_dwordx4 v[168:171], v240, s[56:59], s87 offen offset:0\n\t"
"s_add_u32 s87, s63, 23552\n\t"
"buffer_load_dwordx4 v[172:175], v240, s[56:59], s87 offen offset:0\n\t"
"v_add_u32 v232, 0, v247\n\t"
"s_waitcnt vmcnt(8)\n\t"
"s_barrier\n\t"
"ds_read_b128 v[192:195], v232 offset:0\n\t"
"ds_read_b128 v[196:199], v232 offset:6144\n\t"
"ds_read_b128 v[200:203], v232 offset:1024\n\t"
"ds_read_b128 v[204:207], v232 offset:7168\n\t"
"ds_read_b128 v[208:211], v232 offset:2048\n\t"
"ds_read_b128 v[212:215], v232 offset:8192\n\t"
"s_waitcnt vmcnt(4) lgkmcnt(4)\n\t"
"v_mfma_f32_16x16x32_bf16 v[96:99], v[192:195], v[144:147], 0\n\t"
"ds_read_b128 v[216:219], v232 offset:3072\n\t"
"s_add_u32 m0, s61, 24640\n\t"
"v_mfma_f32_16x16x32_bf16 v[96:99], v[196:199], v[152:155], v[96:99]\n\t"
"s_add_u32 s76, s65, 0\n\t"
"buffer_load_dwordx4 v241, s[40:43], s76 offen lds\n\t"
"v_mfma_f32_16x16x32_bf16 v[104:107], v[196:199], v[156:159], 0\n\t"
"ds_read_b128 v[220:223], v232 offset:9216\n\t"
"s_add_u32 m0, s61, 25664\n\t"
"v_mfma_f32_16x16x32_bf16 v[104:107], v[192:195], v[148:151], v[104:107]\n\t"
"s_add_u32 s76, s65, 128\n\t"
"buffer_load_dwordx4 v241, s[40:43], s76 offen lds\n\t"
"s_waitcnt lgkmcnt(4)\n\t"
"v_mfma_f32_16x16x32_bf16 v[100:103], v[200:203], v[144:147], 0\n\t"
"ds_read_b128 v[192:195], v232 offset:4096\n\t"
"s_add_u32 m0, s61, 26688\n\t"
"s_add_u32 s76, s65, 256\n\t"
"v_mfma_f32_16x16x32_bf16 v[100:103], v[204:207], v[152:155], v[100:103]\n\t"
"buffer_load_dwordx4 v241, s[40:43], s76 offen lds\n\t"
"ds_read_b128 v[196:199], v232 offset:10240\n\t"
"v_mfma_f32_16x16x32_bf16 v[108:111], v[204:207], v[156:159], 0\n\t"
"v_lshrrev_b32 v233, 2, v240\n\t"
"buffer_load_dword v246, v233, s[96:99], s38 offen offset:2048\n\t"
"buffer_load_dword v252, v233, s[96:99], s38 offen offset:2304\n\t"
"v_mfma_f32_16x16x32_bf16 v[108:111], v[200:203], v[148:151], v[108:111]\n\t"
"buffer_load_dword v255, v233, s[96:99], s38 offen offset:2560\n\t"
"buffer_load_dword v233, v233, s[96:99], s34 offen offset:0\n\t"
"s_waitcnt lgkmcnt(4)\n\t"
"v_mfma_f32_16x16x32_bf16 v[112:115], v[208:211], v[144:147], 0\n\t"
"ds_read_b128 v[200:203], v232 offset:5120\n\t"
"v_mfma_f32_16x16x32_bf16 v[112:115], v[212:215], v[152:155], v[112:115]\n\t"
"buffer_load_dwordx4 v[248:251], v240, s[96:99], s38 offen offset:1024\n\t"
"v_mfma_f32_16x16x32_bf16 v[120:123], v[212:215], v[156:159], 0\n\t"
"ds_read_b128 v[204:207], v232 offset:11264\n\t"
"v_mfma_f32_16x16x32_bf16 v[120:123], v[208:211], v[148:151], v[120:123]\n\t"
"buffer_load_dwordx4 v[242:245], v240, s[96:99], s38 offen offset:0\n\t"
"s_waitcnt lgkmcnt(4)\n\t"
"v_mfma_f32_16x16x32_bf16 v[116:119], v[216:219], v[144:147], 0\n\t"
"ds_read_b128 v[208:211], v232 offset:12288\n\t"
"s_add_u32 m0, s61, 49280\n\t"
"v_mfma_f32_16x16x32_bf16 v[116:119], v[220:223], v[152:155], v[116:119]\n\t"
"s_add_u32 s76, s65, 786432\n\t"
"buffer_load_dwordx4 v241, s[40:43], s76 offen lds\n\t"
"v_mfma_f32_16x16x32_bf16 v[124:127], v[220:223], v[156:159], 0\n\t"
"ds_read_b128 v[212:215], v232 offset:18432\n\t"
"s_add_u32 m0, s61, 50304\n\t"
"v_mfma_f32_16x16x32_bf16 v[124:127], v[216:219], v[148:151], v[124:127]\n\t"
"s_add_u32 s76, s65, 786560\n\t"
"buffer_load_dwordx4 v241, s[40:43], s76 offen lds\n\t"
"s_waitcnt lgkmcnt(4)\n\t"
"v_mfma_f32_16x16x32_bf16 v[128:131], v[192:195], v[144:147], 0\n\t"
"ds_read_b128 v[216:219], v232 offset:13312\n\t"
"s_add_u32 m0, s61, 51328\n\t"
"v_mfma_f32_16x16x32_bf16 v[128:131], v[196:199], v[152:155], v[128:131]\n\t"
"s_add_u32 s76, s65, 786688\n\t"
"v_mfma_f32_16x16x32_bf16 v[136:139], v[196:199], v[156:159], 0\n\t"
"buffer_load_dwordx4 v241, s[40:43], s76 offen lds\n\t"
"v_mfma_f32_16x16x32_bf16 v[136:139], v[192:195], v[148:151], v[136:139]\n\t"
"ds_read_b128 v[220:223], v232 offset:19456\n\t"
"s_waitcnt lgkmcnt(4)\n\t"
"v_mfma_f32_16x16x32_bf16 v[132:135], v[200:203], v[144:147], 0\n\t"
"ds_read_b128 v[192:195], v232 offset:14336\n\t"
"v_mfma_f32_16x16x32_bf16 v[132:135], v[204:207], v[152:155], v[132:135]\n\t"
"s_add_u32 s87, s63, 4096\n\t"
"v_mfma_f32_16x16x32_bf16 v[140:143], v[204:207], v[156:159], 0\n\t"
"buffer_load_dwordx4 v[176:179], v240, s[56:59], s87 offen offset:0\n\t"
"v_mfma_f32_16x16x32_bf16 v[140:143], v[200:203], v[148:151], v[140:143]\n\t"
"ds_read_b128 v[196:199], v232 offset:20480\n\t"
"s_waitcnt vmcnt(13) lgkmcnt(4)\n\t"
"v_mfma_f32_16x16x32_bf16 v[96:99], v[208:211], v[160:163], v[96:99]\n\t"
"ds_read_b128 v[200:203], v232 offset:15360\n\t"
"v_mfma_f32_16x16x32_bf16 v[96:99], v[212:215], v[168:171], v[96:99]\n\t"
"s_add_u32 s87, s63, 24576\n\t"
"v_mfma_f32_16x16x32_bf16 v[104:107], v[212:215], v[172:175], v[104:107]\n\t"
"buffer_load_dwordx4 v[180:183], v240, s[56:59], s87 offen offset:0\n\t"
"v_mfma_f32_16x16x32_bf16 v[104:107], v[208:211], v[164:167], v[104:107]\n\t"
"ds_read_b128 v[204:207], v232 offset:21504\n\t"
"s_waitcnt lgkmcnt(4)\n\t"
"v_mfma_f32_16x16x32_bf16 v[100:103], v[216:219], v[160:163], v[100:103]\n\t"
"ds_read_b128 v[208:211], v232 offset:16384\n\t"
"s_add_u32 m0, s62, 73920\n\t"
"v_mfma_f32_16x16x32_bf16 v[100:103], v[220:223], v[168:171], v[100:103]\n\t"
"s_add_u32 s76, s64, 24576\n\t"
"v_mfma_f32_16x16x32_bf16 v[108:111], v[220:223], v[172:175], v[108:111]\n\t"
"buffer_load_dwordx4 v240, s[56:59], s76 offen lds\n\t"
"v_mfma_f32_16x16x32_bf16 v[108:111], v[216:219], v[164:167], v[108:111]\n\t"
"ds_read_b128 v[212:215], v232 offset:22528\n\t"
"s_waitcnt lgkmcnt(4)\n\t"
"v_mfma_f32_16x16x32_bf16 v[112:115], v[192:195], v[160:163], v[112:115]\n\t"
"ds_read_b128 v[216:219], v232 offset:17408\n\t"
"s_add_u32 m0, s62, 74944\n\t"
"v_mfma_f32_16x16x32_bf16 v[112:115], v[196:199], v[168:171], v[112:115]\n\t"
"s_add_u32 s76, s64, 25600\n\t"
"v_mfma_f32_16x16x32_bf16 v[120:123], v[196:199], v[172:175], v[120:123]\n\t"
"buffer_load_dwordx4 v240, s[56:59], s76 offen lds\n\t"
"v_mfma_f32_16x16x32_bf16 v[120:123], v[192:195], v[164:167], v[120:123]\n\t"
"ds_read_b128 v[220:223], v232 offset:23552\n\t"
"s_waitcnt vmcnt(8)\n\t"
"v_add_u32 v248, s60, v248\n\t"
"s_barrier\n\t"
"v_add_u32 v232, 24640, v248\n\t"
"s_waitcnt lgkmcnt(4)\n\t"
"v_mfma_f32_16x16x32_bf16 v[116:119], v[200:203], v[160:163], v[116:119]\n\t"
"ds_read_b64_tr_b16 v[192:193], v232 offset:0\n\t"
"ds_read_b64_tr_b16 v[194:195], v232 offset:256\n\t"
"s_add_u32 m0, s62, 75968\n\t"
"s_add_u32 s76, s64, 26624\n\t"
"v_mfma_f32_16x16x32_bf16 v[116:119], v[204:207], v[168:171], v[116:119]\n\t"
"buffer_load_dwordx4 v240, s[56:59], s76 offen lds\n\t"
"ds_read_b64_tr_b16 v[196:197], v232 offset:12320\n\t"
"ds_read_b64_tr_b16 v[198:199], v232 offset:12576\n\t"
"v_mfma_f32_16x16x32_bf16 v[124:127], v[204:207], v[172:175], v[124:127]\n\t"
"s_waitcnt vmcnt(8)\n\t"
"v_readlane_b32 s72, v233, 32\n\t"
"v_readlane_b32 s73, v233, 33\n\t"
"v_mfma_f32_16x16x32_bf16 v[124:127], v[200:203], v[164:167], v[124:127]\n\t"
"v_readlane_b32 s74, v233, 34\n\t"
"v_readlane_b32 s94, v233, 35\n\t"
"v_readlane_b32 s83, v233, 36\n\t"
"s_waitcnt lgkmcnt(6)\n\t"
"v_mfma_f32_16x16x32_bf16 v[128:131], v[208:211], v[160:163], v[128:131]\n\t"
"ds_read_b64_tr_b16 v[200:201], v232 offset:8\n\t"
"ds_read_b64_tr_b16 v[202:203], v232 offset:264\n\t"
"s_add_u32 s87, s63, 5120\n\t"
"buffer_load_dwordx4 v[184:187], v240, s[56:59], s87 offen offset:0\n\t"
"v_mfma_f32_16x16x32_bf16 v[128:131], v[212:215], v[168:171], v[128:131]\n\t"
"s_cmp_lg_u32 s83, 0\n\t"
"s_cselect_b64 s[92:93], s[36:37], s[92:93]\n\t"
"s_and_b32 s93, s93, 0xffff\n\t"
"ds_read_b64_tr_b16 v[204:205], v232 offset:12328\n\t"
"v_mfma_f32_16x16x32_bf16 v[136:139], v[212:215], v[172:175], v[136:139]\n\t"
"ds_read_b64_tr_b16 v[206:207], v232 offset:12584\n\t"
"s_mov_b32 s95, 0x20000\n\t"
"s_add_u32 s73, s73, s60\n\t"
"v_add_u32 v243, s60, v243\n\t"
"v_mfma_f32_16x16x32_bf16 v[136:139], v[208:211], v[164:167], v[136:139]\n\t"
"v_add_u32 v249, s60, v249\n\t"
"v_add_u32 v250, s60, v250\n\t"
"v_add_u32 v251, s60, v251\n\t"
"v_add_u32 v252, s60, v252\n\t"
"s_waitcnt lgkmcnt(8)\n\t"
"v_mfma_f32_16x16x32_bf16 v[132:135], v[216:219], v[160:163], v[132:135]\n\t"
"ds_read_b64_tr_b16 v[208:209], v232 offset:512\n\t"
"ds_read_b64_tr_b16 v[210:211], v232 offset:768\n\t"
"s_add_u32 s87, s63, 25600\n\t"
"v_mfma_f32_16x16x32_bf16 v[132:135], v[220:223], v[168:171], v[132:135]\n\t"
"buffer_load_dwordx4 v[188:191], v240, s[56:59], s87 offen offset:0\n\t"
"ds_read_b64_tr_b16 v[212:213], v232 offset:12832\n\t"
"v_mfma_f32_16x16x32_bf16 v[140:143], v[220:223], v[172:175], v[140:143]\n\t"
"ds_read_b64_tr_b16 v[214:215], v232 offset:13088\n\t"
"s_add_u32 m0, s73, 147840\n\t"
"v_mfma_f32_16x16x32_bf16 v[140:143], v[216:219], v[164:167], v[140:143]\n\t"
"s_add_u32 s76, s72, 0\n\t"
"buffer_load_dwordx4 v242, s[92:95], s76 offen nt lds\n\t"
"s_waitcnt lgkmcnt(8)\n\t"
"v_mfma_f32_16x16x32_bf16 v[0:3], v[192:195], v[144:147], 0\n\t"
"ds_read_b64_tr_b16 v[216:217], v232 offset:520\n\t"
"ds_read_b64_tr_b16 v[218:219], v232 offset:776\n\t"
"v_mfma_f32_16x16x32_bf16 v[0:3], v[196:199], v[152:155], v[0:3]\n\t"
"s_add_u32 m0, s61, 98560\n\t"
"s_add_u32 s76, s65, 1572864\n\t"
"v_mfma_f32_16x16x32_bf16 v[4:7], v[196:199], v[156:159], 0\n\t"
"buffer_load_dwordx4 v241, s[40:43], s76 offen lds\n\t"
"ds_read_b64_tr_b16 v[220:221], v232 offset:12840\n\t"
"v_mfma_f32_16x16x32_bf16 v[4:7], v[192:195], v[148:151], v[4:7]\n\t"
"ds_read_b64_tr_b16 v[222:223], v232 offset:13096\n\t"
"s_waitcnt lgkmcnt(8)\n\t"
"v_mfma_f32_16x16x32_bf16 v[8:11], v[200:203], v[144:147], 0\n\t"
"ds_read_b64_tr_b16 v[192:193], v232 offset:1024\n\t"
"ds_read_b64_tr_b16 v[194:195], v232 offset:1280\n\t"
"v_mfma_f32_16x16x32_bf16 v[8:11], v[204:207], v[152:155], v[8:11]\n\t"
"s_add_u32 m0, s61, 99584\n\t"
"s_add_u32 s76, s65, 1572992\n\t"
"v_mfma_f32_16x16x32_bf16 v[12:15], v[204:207], v[156:159], 0\n\t"
"buffer_load_dwordx4 v241, s[40:43], s76 offen lds\n\t"
"ds_read_b64_tr_b16 v[196:197], v232 offset:13344\n\t"
"v_mfma_f32_16x16x32_bf16 v[12:15], v[200:203], v[148:151], v[12:15]\n\t"
"ds_read_b64_tr_b16 v[198:199], v232 offset:13600\n\t"
"s_waitcnt lgkmcnt(8)\n\t"
"v_mfma_f32_16x16x32_bf16 v[16:19], v[208:211], v[144:147], 0\n\t"
"ds_read_b64_tr_b16 v[200:201], v232 offset:1032\n\t"
"ds_read_b64_tr_b16 v[202:203], v232 offset:1288\n\t"
"v_mfma_f32_16x16x32_bf16 v[16:19], v[212:215], v[152:155], v[16:19]\n\t"
"s_add_u32 m0, s61, 100608\n\t"
"s_add_u32 s76, s65, 1573120\n\t"
"v_mfma_f32_16x16x32_bf16 v[20:23], v[212:215], v[156:159], 0\n\t"
"buffer_load_dwordx4 v241, s[40:43], s76 offen lds\n\t"
"ds_read_b64_tr_b16 v[204:205], v232 offset:13352\n\t"
"v_mfma_f32_16x16x32_bf16 v[20:23], v[208:211], v[148:151], v[20:23]\n\t"
"ds_read_b64_tr_b16 v[206:207], v232 offset:13608\n\t"
"s_waitcnt lgkmcnt(8)\n\t"
"v_mfma_f32_16x16x32_bf16 v[24:27], v[216:219], v[144:147], 0\n\t"
"ds_read_b64_tr_b16 v[208:209], v232 offset:1536\n\t"
"ds_read_b64_tr_b16 v[210:211], v232 offset:1792\n\t"
"v_mfma_f32_16x16x32_bf16 v[24:27], v[220:223], v[152:155], v[24:27]\n\t"
"s_add_u32 m0, s61, 123200\n\t"
"s_add_u32 s76, s65, 2359296\n\t"
"v_mfma_f32_16x16x32_bf16 v[28:31], v[220:223], v[156:159], 0\n\t"
"buffer_load_dwordx4 v241, s[40:43], s76 offen lds\n\t"
"ds_read_b64_tr_b16 v[212:213], v232 offset:13856\n\t"
"v_mfma_f32_16x16x32_bf16 v[28:31], v[216:219], v[148:151], v[28:31]\n\t"
"ds_read_b64_tr_b16 v[214:215], v232 offset:14112\n\t"
"s_waitcnt lgkmcnt(8)\n\t"
"v_mfma_f32_16x16x32_bf16 v[32:35], v[192:195], v[144:147], 0\n\t"
"ds_read_b64_tr_b16 v[216:217], v232 offset:1544\n\t"
"ds_read_b64_tr_b16 v[218:219], v232 offset:1800\n\t"
"v_mfma_f32_16x16x32_bf16 v[32:35], v[196:199], v[152:155], v[32:35]\n\t"
"s_add_u32 m0, s61, 124224\n\t"
"s_add_u32 s76, s65, 2359424\n\t"
"v_mfma_f32_16x16x32_bf16 v[36:39], v[196:199], v[156:159], 0\n\t"
"buffer_load_dwordx4 v241, s[40:43], s76 offen lds\n\t"
"ds_read_b64_tr_b16 v[220:221], v232 offset:13864\n\t"
"v_mfma_f32_16x16x32_bf16 v[36:39], v[192:195], v[148:151], v[36:39]\n\t"
"ds_read_b64_tr_b16 v[222:223], v232 offset:14120\n\t"
"s_waitcnt lgkmcnt(8)\n\t"
"v_mfma_f32_16x16x32_bf16 v[40:43], v[200:203], v[144:147], 0\n\t"
"ds_read_b64_tr_b16 v[192:193], v232 offset:2048\n\t"
"ds_read_b64_tr_b16 v[194:195], v232 offset:2304\n\t"
"v_mfma_f32_16x16x32_bf16 v[40:43], v[204:207], v[152:155], v[40:43]\n\t"
"s_add_u32 m0, s61, 125248\n\t"
"s_add_u32 s76, s65, 2359552\n\t"
"v_mfma_f32_16x16x32_bf16 v[44:47], v[204:207], v[156:159], 0\n\t"
"buffer_load_dwordx4 v241, s[40:43], s76 offen lds\n\t"
"ds_read_b64_tr_b16 v[196:197], v232 offset:14368\n\t"
"v_mfma_f32_16x16x32_bf16 v[44:47], v[200:203], v[148:151], v[44:47]\n\t"
"ds_read_b64_tr_b16 v[198:199], v232 offset:14624\n\t"
"s_waitcnt lgkmcnt(8)\n\t"
"v_mfma_f32_16x16x32_bf16 v[48:51], v[208:211], v[144:147], 0\n\t"
"ds_read_b64_tr_b16 v[200:201], v232 offset:2056\n\t"
"v_mfma_f32_16x16x32_bf16 v[48:51], v[212:215], v[152:155], v[48:51]\n\t"
"ds_read_b64_tr_b16 v[202:203], v232 offset:2312\n\t"
"v_mfma_f32_16x16x32_bf16 v[52:55], v[212:215], v[156:159], 0\n\t"
"ds_read_b64_tr_b16 v[204:205], v232 offset:14376\n\t"
"v_mfma_f32_16x16x32_bf16 v[52:55], v[208:211], v[148:151], v[52:55]\n\t"
"ds_read_b64_tr_b16 v[206:207], v232 offset:14632\n\t"
"s_waitcnt lgkmcnt(8)\n\t"
"v_mfma_f32_16x16x32_bf16 v[56:59], v[216:219], v[144:147], 0\n\t"
"ds_read_b64_tr_b16 v[208:209], v232 offset:2560\n\t"
"v_mfma_f32_16x16x32_bf16 v[56:59], v[220:223], v[152:155], v[56:59]\n\t"
"ds_read_b64_tr_b16 v[210:211], v232 offset:2816\n\t"
"v_mfma_f32_16x16x32_bf16 v[60:63], v[220:223], v[156:159], 0\n\t"
"ds_read_b64_tr_b16 v[212:213], v232 offset:14880\n\t"
"v_mfma_f32_16x16x32_bf16 v[60:63], v[216:219], v[148:151], v[60:63]\n\t"
"ds_read_b64_tr_b16 v[214:215], v232 offset:15136\n\t"
"s_waitcnt lgkmcnt(8)\n\t"
"v_mfma_f32_16x16x32_bf16 v[64:67], v[192:195], v[144:147], 0\n\t"
"ds_read_b64_tr_b16 v[216:217], v232 offset:2568\n\t"
"v_mfma_f32_16x16x32_bf16 v[64:67], v[196:199], v[152:155], v[64:67]\n\t"
"ds_read_b64_tr_b16 v[218:219], v232 offset:2824\n\t"
"v_mfma_f32_16x16x32_bf16 v[68:71], v[196:199], v[156:159], 0\n\t"
"ds_read_b64_tr_b16 v[220:221], v232 offset:14888\n\t"
"v_mfma_f32_16x16x32_bf16 v[68:71], v[192:195], v[148:151], v[68:71]\n\t"
"ds_read_b64_tr_b16 v[222:223], v232 offset:15144\n\t"
"s_waitcnt vmcnt(6)\n\t"
"s_barrier\n\t"
"v_add_u32 v232, 49280, v248\n\t"
"s_waitcnt lgkmcnt(8)\n\t"
"v_mfma_f32_16x16x32_bf16 v[72:75], v[200:203], v[144:147], 0\n\t"
"ds_read_b64_tr_b16 v[192:193], v232 offset:0\n\t"
"v_mfma_f32_16x16x32_bf16 v[72:75], v[204:207], v[152:155], v[72:75]\n\t"
"ds_read_b64_tr_b16 v[194:195], v232 offset:256\n\t"
"v_mfma_f32_16x16x32_bf16 v[76:79], v[204:207], v[156:159], 0\n\t"
"ds_read_b64_tr_b16 v[196:197], v232 offset:12320\n\t"
"v_mfma_f32_16x16x32_bf16 v[76:79], v[200:203], v[148:151], v[76:79]\n\t"
"ds_read_b64_tr_b16 v[198:199], v232 offset:12576\n\t"
"s_waitcnt lgkmcnt(8)\n\t"
"v_mfma_f32_16x16x32_bf16 v[80:83], v[208:211], v[144:147], 0\n\t"
"ds_read_b64_tr_b16 v[200:201], v232 offset:8\n\t"
"v_mfma_f32_16x16x32_bf16 v[80:83], v[212:215], v[152:155], v[80:83]\n\t"
"ds_read_b64_tr_b16 v[202:203], v232 offset:264\n\t"
"v_mfma_f32_16x16x32_bf16 v[84:87], v[212:215], v[156:159], 0\n\t"
"ds_read_b64_tr_b16 v[204:205], v232 offset:12328\n\t"
"v_mfma_f32_16x16x32_bf16 v[84:87], v[208:211], v[148:151], v[84:87]\n\t"
"ds_read_b64_tr_b16 v[206:207], v232 offset:12584\n\t"
"s_waitcnt lgkmcnt(8)\n\t"
"v_mfma_f32_16x16x32_bf16 v[88:91], v[216:219], v[144:147], 0\n\t"
"ds_read_b64_tr_b16 v[208:209], v232 offset:512\n\t"
"v_mfma_f32_16x16x32_bf16 v[88:91], v[220:223], v[152:155], v[88:91]\n\t"
"ds_read_b64_tr_b16 v[210:211], v232 offset:768\n\t"
"v_mfma_f32_16x16x32_bf16 v[92:95], v[220:223], v[156:159], 0\n\t"
"ds_read_b64_tr_b16 v[212:213], v232 offset:12832\n\t"
"v_mfma_f32_16x16x32_bf16 v[92:95], v[216:219], v[148:151], v[92:95]\n\t"
"ds_read_b64_tr_b16 v[214:215], v232 offset:13088\n\t"
"s_waitcnt lgkmcnt(8)\n\t"
"v_mfma_f32_16x16x32_bf16 v[0:3], v[192:195], v[160:163], v[0:3]\n\t"
"ds_read_b64_tr_b16 v[216:217], v232 offset:520\n\t"
"ds_read_b64_tr_b16 v[218:219], v232 offset:776\n\t"
"v_mfma_f32_16x16x32_bf16 v[0:3], v[196:199], v[168:171], v[0:3]\n\t"
"ds_read_b64_tr_b16 v[234:235], v243 offset:0\n\t"
"ds_read_b64_tr_b16 v[236:237], v243 offset:4096\n\t"
"v_mfma_f32_16x16x32_bf16 v[4:7], v[196:199], v[172:175], v[4:7]\n\t"
"ds_read_b64_tr_b16 v[220:221], v232 offset:12840\n\t"
"ds_read_b64_tr_b16 v[222:223], v232 offset:13096\n\t"
"v_mfma_f32_16x16x32_bf16 v[4:7], v[192:195], v[164:167], v[4:7]\n\t"
"s_add_u32 s87, s63, 6144\n\t"
"buffer_load_dwordx4 v[144:147], v240, s[56:59], s87 offen offset:0\n\t"
"s_waitcnt lgkmcnt(10)\n\t"
"v_mfma_f32_16x16x32_bf16 v[8:11], v[200:203], v[160:163], v[8:11]\n\t"
"ds_read_b64_tr_b16 v[192:193], v232 offset:1024\n\t"
"ds_read_b64_tr_b16 v[194:195], v232 offset:1280\n\t"
"v_mfma_f32_16x16x32_bf16 v[8:11], v[204:207], v[168:171], v[8:11]\n\t"
"ds_read_b64_tr_b16 v[196:197], v232 offset:13344\n\t"
"v_mfma_f32_16x16x32_bf16 v[12:15], v[204:207], v[172:175], v[12:15]\n\t"
"ds_read_b64_tr_b16 v[198:199], v232 offset:13600\n\t"
"s_add_u32 s87, s63, 26624\n\t"
"v_mfma_f32_16x16x32_bf16 v[12:15], v[200:203], v[164:167], v[12:15]\n\t"
"buffer_load_dwordx4 v[148:151], v240, s[56:59], s87 offen offset:0\n\t"
"s_waitcnt lgkmcnt(10)\n\t"
"v_mfma_f32_16x16x32_bf16 v[16:19], v[208:211], v[160:163], v[16:19]\n\t"
"ds_read_b64_tr_b16 v[200:201], v232 offset:1032\n\t"
"ds_read_b64_tr_b16 v[202:203], v232 offset:1288\n\t"
"v_mfma_f32_16x16x32_bf16 v[16:19], v[212:215], v[168:171], v[16:19]\n\t"
"ds_read_b64_tr_b16 v[204:205], v232 offset:13352\n\t"
"ds_read_b64_tr_b16 v[206:207], v232 offset:13608\n\t"
"v_mfma_f32_16x16x32_bf16 v[20:23], v[212:215], v[172:175], v[20:23]\n\t"
"s_add_u32 m0, s62, 0\n\t"
"s_add_u32 s76, s64, 49152\n\t"
"v_mfma_f32_16x16x32_bf16 v[20:23], v[208:211], v[164:167], v[20:23]\n\t"
"buffer_load_dwordx4 v240, s[56:59], s76 offen lds\n\t"
"s_waitcnt lgkmcnt(8)\n\t"
"v_mfma_f32_16x16x32_bf16 v[24:27], v[216:219], v[160:163], v[24:27]\n\t"
"ds_read_b64_tr_b16 v[208:209], v232 offset:1536\n\t"
"ds_read_b64_tr_b16 v[210:211], v232 offset:1792\n\t"
"v_mfma_f32_16x16x32_bf16 v[24:27], v[220:223], v[168:171], v[24:27]\n\t"
"ds_read_b64_tr_b16 v[212:213], v232 offset:13856\n\t"
"ds_read_b64_tr_b16 v[214:215], v232 offset:14112\n\t"
"v_mfma_f32_16x16x32_bf16 v[28:31], v[220:223], v[172:175], v[28:31]\n\t"
"s_add_u32 s76, s74, 0\n\t"
"v_mfma_f32_16x16x32_bf16 v[28:31], v[216:219], v[164:167], v[28:31]\n\t"
"buffer_store_dwordx4 v[234:237], v240, s[56:59], s76 offen offset:0 sc1\n\t"
"s_waitcnt lgkmcnt(8)\n\t"
"v_mfma_f32_16x16x32_bf16 v[32:35], v[192:195], v[160:163], v[32:35]\n\t"
"ds_read_b64_tr_b16 v[216:217], v232 offset:1544\n\t"
"ds_read_b64_tr_b16 v[218:219], v232 offset:1800\n\t"
"v_mfma_f32_16x16x32_bf16 v[32:35], v[196:199], v[168:171], v[32:35]\n\t"
"ds_read_b64_tr_b16 v[220:221], v232 offset:13864\n\t"
"v_mfma_f32_16x16x32_bf16 v[36:39], v[196:199], v[172:175], v[36:39]\n\t"
"ds_read_b64_tr_b16 v[222:223], v232 offset:14120\n\t"
"s_add_u32 s87, s63, 7168\n\t"
"v_mfma_f32_16x16x32_bf16 v[36:39], v[192:195], v[164:167], v[36:39]\n\t"
"buffer_load_dwordx4 v[152:155], v240, s[56:59], s87 offen offset:0\n\t"
"s_waitcnt lgkmcnt(8)\n\t"
"v_mfma_f32_16x16x32_bf16 v[40:43], v[200:203], v[160:163], v[40:43]\n\t"
"ds_read_b64_tr_b16 v[192:193], v232 offset:2048\n\t"
"ds_read_b64_tr_b16 v[194:195], v232 offset:2304\n\t"
"s_add_u32 m0, s62, 1024\n\t"
"v_mfma_f32_16x16x32_bf16 v[40:43], v[204:207], v[168:171], v[40:43]\n\t"
"s_add_u32 s76, s64, 50176\n\t"
"buffer_load_dwordx4 v240, s[56:59], s76 offen lds\n\t"
"v_mfma_f32_16x16x32_bf16 v[44:47], v[204:207], v[172:175], v[44:47]\n\t"
"ds_read_b64_tr_b16 v[196:197], v232 offset:14368\n\t"
"ds_read_b64_tr_b16 v[198:199], v232 offset:14624\n\t"
"v_mfma_f32_16x16x32_bf16 v[44:47], v[200:203], v[164:167], v[44:47]\n\t"
"s_add_u32 s87, s63, 27648\n\t"
"buffer_load_dwordx4 v[156:159], v240, s[56:59], s87 offen offset:0\n\t"
"s_waitcnt lgkmcnt(8)\n\t"
"v_mfma_f32_16x16x32_bf16 v[48:51], v[208:211], v[160:163], v[48:51]\n\t"
"ds_read_b64_tr_b16 v[200:201], v232 offset:2056\n\t"
"v_mfma_f32_16x16x32_bf16 v[48:51], v[212:215], v[168:171], v[48:51]\n\t"
"ds_read_b64_tr_b16 v[202:203], v232 offset:2312\n\t"
"v_mfma_f32_16x16x32_bf16 v[52:55], v[212:215], v[172:175], v[52:55]\n\t"
"ds_read_b64_tr_b16 v[204:205], v232 offset:14376\n\t"
"v_mfma_f32_16x16x32_bf16 v[52:55], v[208:211], v[164:167], v[52:55]\n\t"
"ds_read_b64_tr_b16 v[206:207], v232 offset:14632\n\t"
"s_waitcnt lgkmcnt(8)\n\t"
"v_mfma_f32_16x16x32_bf16 v[56:59], v[216:219], v[160:163], v[56:59]\n\t"
"ds_read_b64_tr_b16 v[208:209], v232 offset:2560\n\t"
"ds_read_b64_tr_b16 v[210:211], v232 offset:2816\n\t"
"v_mfma_f32_16x16x32_bf16 v[56:59], v[220:223], v[168:171], v[56:59]\n\t"
"ds_read_b64_tr_b16 v[212:213], v232 offset:14880\n\t"
"ds_read_b64_tr_b16 v[214:215], v232 offset:15136\n\t"
"v_mfma_f32_16x16x32_bf16 v[60:63], v[220:223], v[172:175], v[60:63]\n\t"
"s_add_u32 m0, s62, 2048\n\t"
"s_add_u32 s76, s64, 51200\n\t"
"v_mfma_f32_16x16x32_bf16 v[60:63], v[216:219], v[164:167], v[60:63]\n\t"
"buffer_load_dwordx4 v240, s[56:59], s76 offen lds\n\t"
"s_waitcnt lgkmcnt(8)\n\t"
"v_mfma_f32_16x16x32_bf16 v[64:67], v[192:195], v[160:163], v[64:67]\n\t"
"ds_read_b64_tr_b16 v[216:217], v232 offset:2568\n\t"
"v_mfma_f32_16x16x32_bf16 v[64:67], v[196:199], v[168:171], v[64:67]\n\t"
"ds_read_b64_tr_b16 v[218:219], v232 offset:2824\n\t"
"v_mfma_f32_16x16x32_bf16 v[68:71], v[196:199], v[172:175], v[68:71]\n\t"
"ds_read_b64_tr_b16 v[220:221], v232 offset:14888\n\t"
"v_mfma_f32_16x16x32_bf16 v[68:71], v[192:195], v[164:167], v[68:71]\n\t"
"ds_read_b64_tr_b16 v[222:223], v232 offset:15144\n\t"
"s_barrier\n\t"
"v_add_u32 v232, 73920, v247\n\t"
"s_waitcnt lgkmcnt(8)\n\t"
"v_mfma_f32_16x16x32_bf16 v[72:75], v[200:203], v[160:163], v[72:75]\n\t"
"ds_read_b128 v[192:195], v232 offset:0\n\t"
"v_mfma_f32_16x16x32_bf16 v[72:75], v[204:207], v[168:171], v[72:75]\n\t"
"v_mfma_f32_16x16x32_bf16 v[76:79], v[204:207], v[172:175], v[76:79]\n\t"
"ds_read_b128 v[196:199], v232 offset:6144\n\t"
"v_mfma_f32_16x16x32_bf16 v[76:79], v[200:203], v[164:167], v[76:79]\n\t"
"s_waitcnt lgkmcnt(6)\n\t"
"v_mfma_f32_16x16x32_bf16 v[80:83], v[208:211], v[160:163], v[80:83]\n\t"
"ds_read_b128 v[200:203], v232 offset:1024\n\t"
"v_mfma_f32_16x16x32_bf16 v[80:83], v[212:215], v[168:171], v[80:83]\n\t"
"v_mfma_f32_16x16x32_bf16 v[84:87], v[212:215], v[172:175], v[84:87]\n\t"
"ds_read_b128 v[204:207], v232 offset:7168\n\t"
"v_mfma_f32_16x16x32_bf16 v[84:87], v[208:211], v[164:167], v[84:87]\n\t"
"s_waitcnt lgkmcnt(4)\n\t"
"v_mfma_f32_16x16x32_bf16 v[88:91], v[216:219], v[160:163], v[88:91]\n\t"
"ds_read_b128 v[208:211], v232 offset:2048\n\t"
"v_mfma_f32_16x16x32_bf16 v[88:91], v[220:223], v[168:171], v[88:91]\n\t"
"v_mfma_f32_16x16x32_bf16 v[92:95], v[220:223], v[172:175], v[92:95]\n\t"
"ds_read_b128 v[212:215], v232 offset:8192\n\t"
"v_mfma_f32_16x16x32_bf16 v[92:95], v[216:219], v[164:167], v[92:95]\n\t"
"s_waitcnt lgkmcnt(4)\n\t"
"v_mfma_f32_16x16x32_bf16 v[96:99], v[192:195], v[176:179], v[96:99]\n\t"
"ds_read_b128 v[216:219], v232 offset:3072\n\t"
"v_readlane_b32 s8, v233, 0\n\t"
"v_mfma_f32_16x16x32_bf16 v[96:99], v[196:199], v[184:187], v[96:99]\n\t"
"ds_read_b128 v[220:223], v232 offset:9216\n\t"
"v_mfma_f32_16x16x32_bf16 v[104:107], v[196:199], v[188:191], v[104:107]\n\t"
"s_add_u32 s87, s63, 8192\n\t"
"buffer_load_dwordx4 v[160:163], v240, s[56:59], s87 offen offset:0\n\t"
"v_mfma_f32_16x16x32_bf16 v[104:107], v[192:195], v[180:183], v[104:107]\n\t"
"v_readlane_b32 s9, v233, 1\n\t"
"s_waitcnt lgkmcnt(4)\n\t"
"v_mfma_f32_16x16x32_bf16 v[100:103], v[200:203], v[176:179], v[100:103]\n\t"
"ds_read_b128 v[192:195], v232 offset:4096\n\t"
"v_readlane_b32 s10, v233, 2\n\t"
"v_mfma_f32_16x16x32_bf16 v[100:103], v[204:207], v[184:187], v[100:103]\n\t"
"ds_read_b128 v[196:199], v232 offset:10240\n\t"
"v_mfma_f32_16x16x32_bf16 v[108:111], v[204:207], v[188:191], v[108:111]\n\t"
"s_add_u32 s87, s63, 28672\n\t"
"buffer_load_dwordx4 v[164:167], v240, s[56:59], s87 offen offset:0\n\t"
"v_mfma_f32_16x16x32_bf16 v[108:111], v[200:203], v[180:183], v[108:111]\n\t"
"v_readlane_b32 s11, v233, 3\n\t"
"s_waitcnt lgkmcnt(4)\n\t"
"v_mfma_f32_16x16x32_bf16 v[112:115], v[208:211], v[176:179], v[112:115]\n\t"
"ds_read_b128 v[200:203], v232 offset:5120\n\t"
"v_readlane_b32 s12, v233, 4\n\t"
"v_mfma_f32_16x16x32_bf16 v[112:115], v[212:215], v[184:187], v[112:115]\n\t"
"ds_read_b128 v[204:207], v232 offset:11264\n\t"
"s_add_u32 m0, s61, 24640\n\t"
"v_mfma_f32_16x16x32_bf16 v[120:123], v[212:215], v[188:191], v[120:123]\n\t"
"s_add_u32 s76, s65, 3145728\n\t"
"buffer_load_dwordx4 v241, s[40:43], s76 offen lds\n\t"
"v_mfma_f32_16x16x32_bf16 v[120:123], v[208:211], v[180:183], v[120:123]\n\t"
"v_readlane_b32 s13, v233, 5\n\t"
"s_waitcnt lgkmcnt(4)\n\t"
"v_mfma_f32_16x16x32_bf16 v[116:119], v[216:219], v[176:179], v[116:119]\n\t"
"ds_read_b128 v[208:211], v232 offset:12288\n\t"
"v_readlane_b32 s14, v233, 6\n\t"
"v_mfma_f32_16x16x32_bf16 v[116:119], v[220:223], v[184:187], v[116:119]\n\t"
"ds_read_b128 v[212:215], v232 offset:18432\n\t"
"s_add_u32 m0, s73, 147840\n\t"
"v_mfma_f32_16x16x32_bf16 v[124:127], v[220:223], v[188:191], v[124:127]\n\t"
"s_add_u32 s76, s72, 256\n\t"
"buffer_load_dwordx4 v242, s[92:95], s76 offen nt lds\n\t"
"v_mfma_f32_16x16x32_bf16 v[124:127], v[216:219], v[180:183], v[124:127]\n\t"
"v_readlane_b32 s15, v233, 7\n\t"
"s_waitcnt lgkmcnt(4)\n\t"
"v_mfma_f32_16x16x32_bf16 v[128:131], v[192:195], v[176:179], v[128:131]\n\t"
"ds_read_b128 v[216:219], v232 offset:13312\n\t"
"v_readlane_b32 s16, v233, 8\n\t"
"v_mfma_f32_16x16x32_bf16 v[128:131], v[196:199], v[184:187], v[128:131]\n\t"
"ds_read_b128 v[220:223], v232 offset:19456\n\t"
"v_mfma_f32_16x16x32_bf16 v[136:139], v[196:199], v[188:191], v[136:139]\n\t"
"s_add_u32 s87, s63, 9216\n\t"
"buffer_load_dwordx4 v[168:171], v240, s[56:59], s87 offen offset:0\n\t"
"v_mfma_f32_16x16x32_bf16 v[136:139], v[192:195], v[180:183], v[136:139]\n\t"
"v_readlane_b32 s17, v233, 9\n\t"
"s_waitcnt lgkmcnt(4)\n\t"
"v_mfma_f32_16x16x32_bf16 v[132:135], v[200:203], v[176:179], v[132:135]\n\t"
"ds_read_b128 v[192:195], v232 offset:14336\n\t"
"s_add_u32 m0, s61, 25664\n\t"
"s_add_u32 s76, s65, 3145856\n\t"
"v_mfma_f32_16x16x32_bf16 v[132:135], v[204:207], v[184:187], v[132:135]\n\t"
"buffer_load_dwordx4 v241, s[40:43], s76 offen lds\n\t"
"v_readlane_b32 s18, v233, 10\n\t"
"v_mfma_f32_16x16x32_bf16 v[140:143], v[204:207], v[188:191], v[140:143]\n\t"
"ds_read_b128 v[196:199], v232 offset:20480\n\t"
"s_add_u32 s87, s63, 29696\n\t"
"v_mfma_f32_16x16x32_bf16 v[140:143], v[200:203], v[180:183], v[140:143]\n\t"
"buffer_load_dwordx4 v[172:175], v240, s[56:59], s87 offen offset:0\n\t"
"v_readlane_b32 s19, v233, 11\n\t"
"s_waitcnt vmcnt(8) lgkmcnt(4)\n\t"
"v_mfma_f32_16x16x32_bf16 v[96:99], v[208:211], v[144:147], v[96:99]\n\t"
"ds_read_b128 v[200:203], v232 offset:15360\n\t"
"v_mfma_f32_16x16x32_bf16 v[96:99], v[212:215], v[152:155], v[96:99]\n\t"
"v_readlane_b32 s20, v233, 12\n\t"
"v_mfma_f32_16x16x32_bf16 v[104:107], v[212:215], v[156:159], v[104:107]\n\t"
"ds_read_b128 v[204:207], v232 offset:21504\n\t"
"v_mfma_f32_16x16x32_bf16 v[104:107], v[208:211], v[148:151], v[104:107]\n\t"
"v_readlane_b32 s21, v233, 13\n\t"
"s_waitcnt lgkmcnt(4)\n\t"
"v_mfma_f32_16x16x32_bf16 v[100:103], v[216:219], v[144:147], v[100:103]\n\t"
"ds_read_b128 v[208:211], v232 offset:16384\n\t"
"v_readlane_b32 s22, v233, 14\n\t"
"v_mfma_f32_16x16x32_bf16 v[100:103], v[220:223], v[152:155], v[100:103]\n\t"
"ds_read_b128 v[212:215], v232 offset:22528\n\t"
"s_add_u32 m0, s61, 26688\n\t"
"v_mfma_f32_16x16x32_bf16 v[108:111], v[220:223], v[156:159], v[108:111]\n\t"
"s_add_u32 s76, s65, 3145984\n\t"
"buffer_load_dwordx4 v241, s[40:43], s76 offen lds\n\t"
"v_mfma_f32_16x16x32_bf16 v[108:111], v[216:219], v[148:151], v[108:111]\n\t"
"v_readlane_b32 s23, v233, 15\n\t"
"s_waitcnt lgkmcnt(4)\n\t"
"v_mfma_f32_16x16x32_bf16 v[112:115], v[192:195], v[144:147], v[112:115]\n\t"
"ds_read_b128 v[216:219], v232 offset:17408\n\t"
"v_mfma_f32_16x16x32_bf16 v[112:115], v[196:199], v[152:155], v[112:115]\n\t"
"v_readlane_b32 s24, v233, 16\n\t"
"v_mfma_f32_16x16x32_bf16 v[120:123], v[196:199], v[156:159], v[120:123]\n\t"
"ds_read_b128 v[220:223], v232 offset:23552\n\t"
"v_mfma_f32_16x16x32_bf16 v[120:123], v[192:195], v[148:151], v[120:123]\n\t"
"v_readlane_b32 s25, v233, 17\n\t"
"s_barrier\n\t"
"v_add_u32 v232, 98560, v248\n\t"
"s_waitcnt lgkmcnt(4)\n\t"
"v_mfma_f32_16x16x32_bf16 v[116:119], v[200:203], v[144:147], v[116:119]\n\t"
"ds_read_b64_tr_b16 v[192:193], v232 offset:0\n\t"
"ds_read_b64_tr_b16 v[194:195], v232 offset:256\n\t"
"v_mfma_f32_16x16x32_bf16 v[116:119], v[204:207], v[152:155], v[116:119]\n\t"
"v_readlane_b32 s26, v233, 18\n\t"
"v_mfma_f32_16x16x32_bf16 v[124:127], v[204:207], v[156:159], v[124:127]\n\t"
"ds_read_b64_tr_b16 v[196:197], v232 offset:12320\n\t"
"ds_read_b64_tr_b16 v[198:199], v232 offset:12576\n\t"
"v_mfma_f32_16x16x32_bf16 v[124:127], v[200:203], v[148:151], v[124:127]\n\t"
"v_readlane_b32 s27, v233, 19\n\t"
"s_waitcnt lgkmcnt(6)\n\t"
"v_mfma_f32_16x16x32_bf16 v[128:131], v[208:211], v[144:147], v[128:131]\n\t"
"ds_read_b64_tr_b16 v[200:201], v232 offset:8\n\t"
"ds_read_b64_tr_b16 v[202:203], v232 offset:264\n\t"
"v_mfma_f32_16x16x32_bf16 v[128:131], v[212:215], v[152:155], v[128:131]\n\t"
"v_readlane_b32 s28, v233, 20\n\t"
"v_mfma_f32_16x16x32_bf16 v[136:139], v[212:215], v[156:159], v[136:139]\n\t"
"ds_read_b64_tr_b16 v[204:205], v232 offset:12328\n\t"
"ds_read_b64_tr_b16 v[206:207], v232 offset:12584\n\t"
"v_mfma_f32_16x16x32_bf16 v[136:139], v[208:211], v[148:151], v[136:139]\n\t"
"v_readlane_b32 s29, v233, 21\n\t"
"s_waitcnt lgkmcnt(8)\n\t"
"v_mfma_f32_16x16x32_bf16 v[132:135], v[216:219], v[144:147], v[132:135]\n\t"
"ds_read_b64_tr_b16 v[208:209], v232 offset:512\n\t"
"ds_read_b64_tr_b16 v[210:211], v232 offset:768\n\t"
"v_mfma_f32_16x16x32_bf16 v[132:135], v[220:223], v[152:155], v[132:135]\n\t"
"v_readlane_b32 s30, v233, 22\n\t"
"v_mfma_f32_16x16x32_bf16 v[140:143], v[220:223], v[156:159], v[140:143]\n\t"
"ds_read_b64_tr_b16 v[212:213], v232 offset:12832\n\t"
"ds_read_b64_tr_b16 v[214:215], v232 offset:13088\n\t"
"v_mfma_f32_16x16x32_bf16 v[140:143], v[216:219], v[148:151], v[140:143]\n\t"
"v_readlane_b32 s31, v233, 23\n\t"
"s_waitcnt lgkmcnt(8)\n\t"
"v_mfma_f32_16x16x32_bf16 v[0:3], v[192:195], v[176:179], v[0:3]\n\t"
"ds_read_b64_tr_b16 v[216:217], v232 offset:520\n\t"
"ds_read_b64_tr_b16 v[218:219], v232 offset:776\n\t"
"v_mfma_f32_16x16x32_bf16 v[0:3], v[196:199], v[184:187], v[0:3]\n\t"
"v_readlane_b32 s32, v233, 24\n\t"
"v_mfma_f32_16x16x32_bf16 v[4:7], v[196:199], v[188:191], v[4:7]\n\t"
"ds_read_b64_tr_b16 v[220:221], v232 offset:12840\n\t"
"ds_read_b64_tr_b16 v[222:223], v232 offset:13096\n\t"
"v_mfma_f32_16x16x32_bf16 v[4:7], v[192:195], v[180:183], v[4:7]\n\t"
"v_readlane_b32 s33, v233, 25\n\t"
"s_waitcnt lgkmcnt(8)\n\t"
"v_mfma_f32_16x16x32_bf16 v[8:11], v[200:203], v[176:179], v[8:11]\n\t"
"ds_read_b64_tr_b16 v[192:193], v232 offset:1024\n\t"
"ds_read_b64_tr_b16 v[194:195], v232 offset:1280\n\t"
"v_mfma_f32_16x16x32_bf16 v[8:11], v[204:207], v[184:187], v[8:11]\n\t"
"v_readlane_b32 s67, v233, 26\n\t"
"v_mfma_f32_16x16x32_bf16 v[12:15], v[204:207], v[188:191], v[12:15]\n\t"
"ds_read_b64_tr_b16 v[196:197], v232 offset:13344\n\t"
"ds_read_b64_tr_b16 v[198:199], v232 offset:13600\n\t"
"v_mfma_f32_16x16x32_bf16 v[12:15], v[200:203], v[180:183], v[12:15]\n\t"
"v_readlane_b32 s68, v233, 27\n\t"
"s_waitcnt lgkmcnt(8)\n\t"
"v_mfma_f32_16x16x32_bf16 v[16:19], v[208:211], v[176:179], v[16:19]\n\t"
"ds_read_b64_tr_b16 v[200:201], v232 offset:1032\n\t"
"ds_read_b64_tr_b16 v[202:203], v232 offset:1288\n\t"
"v_readlane_b32 s69, v233, 28\n\t"
"v_mfma_f32_16x16x32_bf16 v[16:19], v[212:215], v[184:187], v[16:19]\n\t"
"ds_read_b64_tr_b16 v[204:205], v232 offset:13352\n\t"
"ds_read_b64_tr_b16 v[206:207], v232 offset:13608\n\t"
"v_mfma_f32_16x16x32_bf16 v[20:23], v[212:215], v[188:191], v[20:23]\n\t"
"s_add_u32 m0, s61, 49280\n\t"
"s_add_u32 s76, s65, 3932160\n\t"
"v_mfma_f32_16x16x32_bf16 v[20:23], v[208:211], v[180:183], v[20:23]\n\t"
"buffer_load_dwordx4 v241, s[40:43], s76 offen lds\n\t"
"v_readlane_b32 s70, v233, 29\n\t"
"s_waitcnt lgkmcnt(8)\n\t"
"v_mfma_f32_16x16x32_bf16 v[24:27], v[216:219], v[176:179], v[24:27]\n\t"
"ds_read_b64_tr_b16 v[208:209], v232 offset:1536\n\t"
"ds_read_b64_tr_b16 v[210:211], v232 offset:1792\n\t"
"v_mfma_f32_16x16x32_bf16 v[24:27], v[220:223], v[184:187], v[24:27]\n\t"
"v_readlane_b32 s35, v233, 30\n\t"
"v_mfma_f32_16x16x32_bf16 v[28:31], v[220:223], v[188:191], v[28:31]\n\t"
"ds_read_b64_tr_b16 v[212:213], v232 offset:13856\n\t"
"ds_read_b64_tr_b16 v[214:215], v232 offset:14112\n\t"
"v_mfma_f32_16x16x32_bf16 v[28:31], v[216:219], v[180:183], v[28:31]\n\t"
"v_readlane_b32 s66, v233, 37\n\t"
"s_waitcnt lgkmcnt(8)\n\t"
"v_mfma_f32_16x16x32_bf16 v[32:35], v[192:195], v[176:179], v[32:35]\n\t"
"ds_read_b64_tr_b16 v[216:217], v232 offset:1544\n\t"
"ds_read_b64_tr_b16 v[218:219], v232 offset:1800\n\t"
"v_mfma_f32_16x16x32_bf16 v[32:35], v[196:199], v[184:187], v[32:35]\n\t"
"v_readlane_b32 s71, v233, 38\n\t"
"v_mfma_f32_16x16x32_bf16 v[36:39], v[196:199], v[188:191], v[36:39]\n\t"
"ds_read_b64_tr_b16 v[220:221], v232 offset:13864\n\t"
"ds_read_b64_tr_b16 v[222:223], v232 offset:14120\n\t"
"v_mfma_f32_16x16x32_bf16 v[36:39], v[192:195], v[180:183], v[36:39]\n\t"
"v_readlane_b32 s75, v233, 39\n\t"
"s_waitcnt lgkmcnt(8)\n\t"
"v_mfma_f32_16x16x32_bf16 v[40:43], v[200:203], v[176:179], v[40:43]\n\t"
"ds_read_b64_tr_b16 v[192:193], v232 offset:2048\n\t"
"ds_read_b64_tr_b16 v[194:195], v232 offset:2304\n\t"
"s_add_u32 m0, s61, 50304\n\t"
"v_mfma_f32_16x16x32_bf16 v[40:43], v[204:207], v[184:187], v[40:43]\n\t"
"s_add_u32 s76, s65, 3932288\n\t"
"buffer_load_dwordx4 v241, s[40:43], s76 offen lds\n\t"
"v_readlane_b32 s39, v233, 40\n\t"
"v_mfma_f32_16x16x32_bf16 v[44:47], v[204:207], v[188:191], v[44:47]\n\t"
"ds_read_b64_tr_b16 v[196:197], v232 offset:14368\n\t"
"ds_read_b64_tr_b16 v[198:199], v232 offset:14624\n\t"
"s_and_b32 s49, s49, 0xffff\n\t"
"v_mfma_f32_16x16x32_bf16 v[44:47], v[200:203], v[180:183], v[44:47]\n\t"
"s_mov_b32 s50, 25165824\n\t"
"s_mov_b32 s51, 0x20000\n\t"
"s_waitcnt lgkmcnt(8)\n\t"
"v_mfma_f32_16x16x32_bf16 v[48:51], v[208:211], v[176:179], v[48:51]\n\t"
"ds_read_b64_tr_b16 v[200:201], v232 offset:2056\n\t"
"ds_read_b64_tr_b16 v[202:203], v232 offset:2312\n\t"
"v_mfma_f32_16x16x32_bf16 v[48:51], v[212:215], v[184:187], v[48:51]\n\t"
"s_and_b32 s53, s53, 0xffff\n\t"
"s_mov_b32 s54, 25165824\n\t"
"v_mfma_f32_16x16x32_bf16 v[52:55], v[212:215], v[188:191], v[52:55]\n\t"
"s_mov_b32 s55, 0x20000\n\t"
"ds_read_b64_tr_b16 v[204:205], v232 offset:14376\n\t"
"v_mfma_f32_16x16x32_bf16 v[52:55], v[208:211], v[180:183], v[52:55]\n\t"
"ds_read_b64_tr_b16 v[206:207], v232 offset:14632\n\t"
"s_add_u32 s8, s8, s60\n\t"
"s_waitcnt lgkmcnt(8)\n\t"
"v_mfma_f32_16x16x32_bf16 v[56:59], v[216:219], v[176:179], v[56:59]\n\t"
"ds_read_b64_tr_b16 v[208:209], v232 offset:2560\n\t"
"ds_read_b64_tr_b16 v[210:211], v232 offset:2816\n\t"
"s_add_u32 s9, s9, s60\n\t"
"v_mfma_f32_16x16x32_bf16 v[56:59], v[220:223], v[184:187], v[56:59]\n\t"
"ds_read_b64_tr_b16 v[212:213], v232 offset:14880\n\t"
"ds_read_b64_tr_b16 v[214:215], v232 offset:15136\n\t"
"v_mfma_f32_16x16x32_bf16 v[60:63], v[220:223], v[188:191], v[60:63]\n\t"
"s_add_u32 m0, s61, 51328\n\t"
"s_add_u32 s76, s65, 3932416\n\t"
"v_mfma_f32_16x16x32_bf16 v[60:63], v[216:219], v[180:183], v[60:63]\n\t"
"buffer_load_dwordx4 v241, s[40:43], s76 offen lds\n\t"
"s_add_u32 s10, s10, s60\n\t"
"s_waitcnt lgkmcnt(8)\n\t"
"v_mfma_f32_16x16x32_bf16 v[64:67], v[192:195], v[176:179], v[64:67]\n\t"
"ds_read_b64_tr_b16 v[216:217], v232 offset:2568\n\t"
"ds_read_b64_tr_b16 v[218:219], v232 offset:2824\n\t"
"v_mfma_f32_16x16x32_bf16 v[64:67], v[196:199], v[184:187], v[64:67]\n\t"
"s_add_u32 s12, s12, s60\n\t"
"v_mfma_f32_16x16x32_bf16 v[68:71], v[196:199], v[188:191], v[68:71]\n\t"
"ds_read_b64_tr_b16 v[220:221], v232 offset:14888\n\t"
"ds_read_b64_tr_b16 v[222:223], v232 offset:15144\n\t"
"v_mfma_f32_16x16x32_bf16 v[68:71], v[192:195], v[180:183], v[68:71]\n\t"
"s_add_u32 s31, s31, s60\n\t"
"s_waitcnt vmcnt(7)\n\t"
"s_barrier\n\t"
"v_add_u32 v232, 123200, v248\n\t"
"s_waitcnt lgkmcnt(8)\n\t"
"v_mfma_f32_16x16x32_bf16 v[72:75], v[200:203], v[176:179], v[72:75]\n\t"
"ds_read_b64_tr_b16 v[192:193], v232 offset:0\n\t"
"ds_read_b64_tr_b16 v[194:195], v232 offset:256\n\t"
"v_mfma_f32_16x16x32_bf16 v[72:75], v[204:207], v[184:187], v[72:75]\n\t"
"s_add_u32 s32, s32, s60\n\t"
"v_mfma_f32_16x16x32_bf16 v[76:79], v[204:207], v[188:191], v[76:79]\n\t"
"ds_read_b64_tr_b16 v[196:197], v232 offset:12320\n\t"
"ds_read_b64_tr_b16 v[198:199], v232 offset:12576\n\t"
"v_mfma_f32_16x16x32_bf16 v[76:79], v[200:203], v[180:183], v[76:79]\n\t"
"v_mov_b32 v253, 0xc2b669c2\n\t"
"s_waitcnt lgkmcnt(8)\n\t"
"v_mfma_f32_16x16x32_bf16 v[80:83], v[208:211], v[176:179], v[80:83]\n\t"
"ds_read_b64_tr_b16 v[200:201], v232 offset:8\n\t"
"ds_read_b64_tr_b16 v[202:203], v232 offset:264\n\t"
"v_mfma_f32_16x16x32_bf16 v[80:83], v[212:215], v[184:187], v[80:83]\n\t"
"s_mov_b32 s86, 0x3c319c33\n\t"
"v_mfma_f32_16x16x32_bf16 v[84:87], v[212:215], v[188:191], v[84:87]\n\t"
"ds_read_b64_tr_b16 v[204:205], v232 offset:12328\n\t"
"v_mfma_f32_16x16x32_bf16 v[84:87], v[208:211], v[180:183], v[84:87]\n\t"
"ds_read_b64_tr_b16 v[206:207], v232 offset:12584\n\t"
"s_waitcnt lgkmcnt(8)\n\t"
"v_mfma_f32_16x16x32_bf16 v[88:91], v[216:219], v[176:179], v[88:91]\n\t"
"ds_read_b64_tr_b16 v[208:209], v232 offset:512\n\t"
"v_mfma_f32_16x16x32_bf16 v[88:91], v[220:223], v[184:187], v[88:91]\n\t"
"ds_read_b64_tr_b16 v[210:211], v232 offset:768\n\t"
"v_mfma_f32_16x16x32_bf16 v[92:95], v[220:223], v[188:191], v[92:95]\n\t"
"ds_read_b64_tr_b16 v[212:213], v232 offset:12832\n\t"
"v_mfma_f32_16x16x32_bf16 v[92:95], v[216:219], v[180:183], v[92:95]\n\t"
"ds_read_b64_tr_b16 v[214:215], v232 offset:13088\n\t"
"s_waitcnt lgkmcnt(8)\n\t"
"v_mfma_f32_16x16x32_bf16 v[0:3], v[192:195], v[144:147], v[0:3]\n\t"
"ds_read_b64_tr_b16 v[216:217], v232 offset:520\n\t"
"ds_read_b64_tr_b16 v[218:219], v232 offset:776\n\t"
"v_mfma_f32_16x16x32_bf16 v[0:3], v[196:199], v[152:155], v[0:3]\n\t"
"ds_read_b64_tr_b16 v[234:235], v243 offset:0\n\t"
"ds_read_b64_tr_b16 v[236:237], v243 offset:4096\n\t"
"v_mfma_f32_16x16x32_bf16 v[4:7], v[196:199], v[156:159], v[4:7]\n\t"
"ds_read_b64_tr_b16 v[220:221], v232 offset:12840\n\t"
"ds_read_b64_tr_b16 v[222:223], v232 offset:13096\n\t"
"v_mfma_f32_16x16x32_bf16 v[4:7], v[192:195], v[148:151], v[4:7]\n\t"
"s_add_u32 s87, s63, 10240\n\t"
"buffer_load_dwordx4 v[176:179], v240, s[56:59], s87 offen offset:0\n\t"
"s_waitcnt lgkmcnt(10)\n\t"
"v_mfma_f32_16x16x32_bf16 v[8:11], v[200:203], v[144:147], v[8:11]\n\t"
"ds_read_b64_tr_b16 v[192:193], v232 offset:1024\n\t"
"ds_read_b64_tr_b16 v[194:195], v232 offset:1280\n\t"
"v_mfma_f32_16x16x32_bf16 v[8:11], v[204:207], v[152:155], v[8:11]\n\t"
"ds_read_b64_tr_b16 v[196:197], v232 offset:13344\n\t"
"v_mfma_f32_16x16x32_bf16 v[12:15], v[204:207], v[156:159], v[12:15]\n\t"
"ds_read_b64_tr_b16 v[198:199], v232 offset:13600\n\t"
"s_add_u32 s87, s63, 30720\n\t"
"v_mfma_f32_16x16x32_bf16 v[12:15], v[200:203], v[148:151], v[12:15]\n\t"
"buffer_load_dwordx4 v[180:183], v240, s[56:59], s87 offen offset:0\n\t"
"s_waitcnt lgkmcnt(10)\n\t"
"v_mfma_f32_16x16x32_bf16 v[16:19], v[208:211], v[144:147], v[16:19]\n\t"
"ds_read_b64_tr_b16 v[200:201], v232 offset:1032\n\t"
"ds_read_b64_tr_b16 v[202:203], v232 offset:1288\n\t"
"v_mfma_f32_16x16x32_bf16 v[16:19], v[212:215], v[152:155], v[16:19]\n\t"
"ds_read_b64_tr_b16 v[204:205], v232 offset:13352\n\t"
"ds_read_b64_tr_b16 v[206:207], v232 offset:13608\n\t"
"v_mfma_f32_16x16x32_bf16 v[20:23], v[212:215], v[156:159], v[20:23]\n\t"
"s_add_u32 m0, s62, 73920\n\t"
"s_add_u32 s76, s64, 73728\n\t"
"v_mfma_f32_16x16x32_bf16 v[20:23], v[208:211], v[148:151], v[20:23]\n\t"
"buffer_load_dwordx4 v240, s[56:59], s76 offen lds\n\t"
"s_waitcnt lgkmcnt(8)\n\t"
"v_mfma_f32_16x16x32_bf16 v[24:27], v[216:219], v[144:147], v[24:27]\n\t"
"ds_read_b64_tr_b16 v[208:209], v232 offset:1536\n\t"
"ds_read_b64_tr_b16 v[210:211], v232 offset:1792\n\t"
"v_mfma_f32_16x16x32_bf16 v[24:27], v[220:223], v[152:155], v[24:27]\n\t"
"ds_read_b64_tr_b16 v[212:213], v232 offset:13856\n\t"
"ds_read_b64_tr_b16 v[214:215], v232 offset:14112\n\t"
"v_mfma_f32_16x16x32_bf16 v[28:31], v[220:223], v[156:159], v[28:31]\n\t"
"s_add_u32 s76, s74, 8192\n\t"
"v_mfma_f32_16x16x32_bf16 v[28:31], v[216:219], v[148:151], v[28:31]\n\t"
"buffer_store_dwordx4 v[234:237], v240, s[56:59], s76 offen offset:0 sc1\n\t"
"s_waitcnt lgkmcnt(8)\n\t"
"v_mfma_f32_16x16x32_bf16 v[32:35], v[192:195], v[144:147], v[32:35]\n\t"
"ds_read_b64_tr_b16 v[216:217], v232 offset:1544\n\t"
"ds_read_b64_tr_b16 v[218:219], v232 offset:1800\n\t"
"v_mfma_f32_16x16x32_bf16 v[32:35], v[196:199], v[152:155], v[32:35]\n\t"
"ds_read_b64_tr_b16 v[220:221], v232 offset:13864\n\t"
"v_mfma_f32_16x16x32_bf16 v[36:39], v[196:199], v[156:159], v[36:39]\n\t"
"ds_read_b64_tr_b16 v[222:223], v232 offset:14120\n\t"
"s_add_u32 s87, s63, 11264\n\t"
"v_mfma_f32_16x16x32_bf16 v[36:39], v[192:195], v[148:151], v[36:39]\n\t"
"buffer_load_dwordx4 v[184:187], v240, s[56:59], s87 offen offset:0\n\t"
"s_waitcnt lgkmcnt(8)\n\t"
"v_mfma_f32_16x16x32_bf16 v[40:43], v[200:203], v[144:147], v[40:43]\n\t"
"ds_read_b64_tr_b16 v[192:193], v232 offset:2048\n\t"
"ds_read_b64_tr_b16 v[194:195], v232 offset:2304\n\t"
"s_add_u32 m0, s62, 74944\n\t"
"v_mfma_f32_16x16x32_bf16 v[40:43], v[204:207], v[152:155], v[40:43]\n\t"
"s_add_u32 s76, s64, 74752\n\t"
"buffer_load_dwordx4 v240, s[56:59], s76 offen lds\n\t"
"v_mfma_f32_16x16x32_bf16 v[44:47], v[204:207], v[156:159], v[44:47]\n\t"
"ds_read_b64_tr_b16 v[196:197], v232 offset:14368\n\t"
"ds_read_b64_tr_b16 v[198:199], v232 offset:14624\n\t"
"v_mfma_f32_16x16x32_bf16 v[44:47], v[200:203], v[148:151], v[44:47]\n\t"
"s_add_u32 s87, s63, 31744\n\t"
"buffer_load_dwordx4 v[188:191], v240, s[56:59], s87 offen offset:0\n\t"
"s_waitcnt lgkmcnt(8)\n\t"
"v_mfma_f32_16x16x32_bf16 v[48:51], v[208:211], v[144:147], v[48:51]\n\t"
"ds_read_b64_tr_b16 v[200:201], v232 offset:2056\n\t"
"v_mfma_f32_16x16x32_bf16 v[48:51], v[212:215], v[152:155], v[48:51]\n\t"
"ds_read_b64_tr_b16 v[202:203], v232 offset:2312\n\t"
"v_mfma_f32_16x16x32_bf16 v[52:55], v[212:215], v[156:159], v[52:55]\n\t"
"ds_read_b64_tr_b16 v[204:205], v232 offset:14376\n\t"
"v_mfma_f32_16x16x32_bf16 v[52:55], v[208:211], v[148:151], v[52:55]\n\t"
"ds_read_b64_tr_b16 v[206:207], v232 offset:14632\n\t"
"s_waitcnt lgkmcnt(8)\n\t"
"v_mfma_f32_16x16x32_bf16 v[56:59], v[216:219], v[144:147], v[56:59]\n\t"
"ds_read_b64_tr_b16 v[208:209], v232 offset:2560\n\t"
"ds_read_b64_tr_b16 v[210:211], v232 offset:2816\n\t"
"v_mfma_f32_16x16x32_bf16 v[56:59], v[220:223], v[152:155], v[56:59]\n\t"
"ds_read_b64_tr_b16 v[212:213], v232 offset:14880\n\t"
"ds_read_b64_tr_b16 v[214:215], v232 offset:15136\n\t"
"v_mfma_f32_16x16x32_bf16 v[60:63], v[220:223], v[156:159], v[60:63]\n\t"
"s_add_u32 m0, s62, 75968\n\t"
"s_add_u32 s76, s64, 75776\n\t"
"v_mfma_f32_16x16x32_bf16 v[60:63], v[216:219], v[148:151], v[60:63]\n\t"
"buffer_load_dwordx4 v240, s[56:59], s76 offen lds\n\t"
"s_waitcnt lgkmcnt(8)\n\t"
"v_mfma_f32_16x16x32_bf16 v[64:67], v[192:195], v[144:147], v[64:67]\n\t"
"ds_read_b64_tr_b16 v[216:217], v232 offset:2568\n\t"
"v_mfma_f32_16x16x32_bf16 v[64:67], v[196:199], v[152:155], v[64:67]\n\t"
"ds_read_b64_tr_b16 v[218:219], v232 offset:2824\n\t"
"v_mfma_f32_16x16x32_bf16 v[68:71], v[196:199], v[156:159], v[68:71]\n\t"
"ds_read_b64_tr_b16 v[220:221], v232 offset:14888\n\t"
"v_mfma_f32_16x16x32_bf16 v[68:71], v[192:195], v[148:151], v[68:71]\n\t"
"ds_read_b64_tr_b16 v[222:223], v232 offset:15144\n\t"
"s_barrier\n\t"
"v_add_u32 v232, 0, v247\n\t"
"s_waitcnt lgkmcnt(8)\n\t"
"v_mfma_f32_16x16x32_bf16 v[72:75], v[200:203], v[144:147], v[72:75]\n\t"
"ds_read_b128 v[192:195], v232 offset:0\n\t"
"v_mfma_f32_16x16x32_bf16 v[72:75], v[204:207], v[152:155], v[72:75]\n\t"
"v_mfma_f32_16x16x32_bf16 v[76:79], v[204:207], v[156:159], v[76:79]\n\t"
"ds_read_b128 v[196:199], v232 offset:6144\n\t"
"v_mfma_f32_16x16x32_bf16 v[76:79], v[200:203], v[148:151], v[76:79]\n\t"
"s_waitcnt lgkmcnt(6)\n\t"
"v_mfma_f32_16x16x32_bf16 v[80:83], v[208:211], v[144:147], v[80:83]\n\t"
"ds_read_b128 v[200:203], v232 offset:1024\n\t"
"v_mfma_f32_16x16x32_bf16 v[80:83], v[212:215], v[152:155], v[80:83]\n\t"
"v_mfma_f32_16x16x32_bf16 v[84:87], v[212:215], v[156:159], v[84:87]\n\t"
"ds_read_b128 v[204:207], v232 offset:7168\n\t"
"v_mfma_f32_16x16x32_bf16 v[84:87], v[208:211], v[148:151], v[84:87]\n\t"
"s_waitcnt lgkmcnt(4)\n\t"
"v_mfma_f32_16x16x32_bf16 v[88:91], v[216:219], v[144:147], v[88:91]\n\t"
"ds_read_b128 v[208:211], v232 offset:2048\n\t"
"v_mfma_f32_16x16x32_bf16 v[88:91], v[220:223], v[152:155], v[88:91]\n\t"
"v_mfma_f32_16x16x32_bf16 v[92:95], v[220:223], v[156:159], v[92:95]\n\t"
"ds_read_b128 v[212:215], v232 offset:8192\n\t"
"v_mfma_f32_16x16x32_bf16 v[92:95], v[216:219], v[148:151], v[92:95]\n\t"
"s_waitcnt vmcnt(12) lgkmcnt(4)\n\t"
"v_mfma_f32_16x16x32_bf16 v[96:99], v[192:195], v[160:163], v[96:99]\n\t"
"ds_read_b128 v[216:219], v232 offset:3072\n\t"
"v_mfma_f32_16x16x32_bf16 v[96:99], v[196:199], v[168:171], v[96:99]\n\t"
"ds_read_b128 v[220:223], v232 offset:9216\n\t"
"v_mfma_f32_16x16x32_bf16 v[104:107], v[196:199], v[172:175], v[104:107]\n\t"
"s_add_u32 s87, s63, 12288\n\t"
"v_mfma_f32_16x16x32_bf16 v[104:107], v[192:195], v[164:167], v[104:107]\n\t"
"buffer_load_dwordx4 v[144:147], v240, s[56:59], s87 offen offset:0\n\t"
"s_waitcnt lgkmcnt(4)\n\t"
"v_mfma_f32_16x16x32_bf16 v[100:103], v[200:203], v[160:163], v[100:103]\n\t"
"ds_read_b128 v[192:195], v232 offset:4096\n\t"
"v_mfma_f32_16x16x32_bf16 v[100:103], v[204:207], v[168:171], v[100:103]\n\t"
"ds_read_b128 v[196:199], v232 offset:10240\n\t"
"v_mfma_f32_16x16x32_bf16 v[108:111], v[204:207], v[172:175], v[108:111]\n\t"
"s_add_u32 s87, s63, 32768\n\t"
"v_mfma_f32_16x16x32_bf16 v[108:111], v[200:203], v[164:167], v[108:111]\n\t"
"buffer_load_dwordx4 v[148:151], v240, s[56:59], s87 offen offset:0\n\t"
"s_waitcnt lgkmcnt(4)\n\t"
"v_mfma_f32_16x16x32_bf16 v[112:115], v[208:211], v[160:163], v[112:115]\n\t"
"ds_read_b128 v[200:203], v232 offset:5120\n\t"
"ds_read_b128 v[204:207], v232 offset:11264\n\t"
"v_mfma_f32_16x16x32_bf16 v[112:115], v[212:215], v[168:171], v[112:115]\n\t"
"s_add_u32 m0, s61, 98560\n\t"
"v_mfma_f32_16x16x32_bf16 v[120:123], v[212:215], v[172:175], v[120:123]\n\t"
"s_add_u32 s76, s65, 4718592\n\t"
"v_mfma_f32_16x16x32_bf16 v[120:123], v[208:211], v[164:167], v[120:123]\n\t"
"buffer_load_dwordx4 v241, s[40:43], s76 offen lds\n\t"
"s_waitcnt lgkmcnt(4)\n\t"
"v_mfma_f32_16x16x32_bf16 v[116:119], v[216:219], v[160:163], v[116:119]\n\t"
"ds_read_b128 v[208:211], v232 offset:12288\n\t"
"ds_read_b128 v[212:215], v232 offset:18432\n\t"
"v_mfma_f32_16x16x32_bf16 v[116:119], v[220:223], v[168:171], v[116:119]\n\t"
"s_add_u32 m0, s73, 147840\n\t"
"v_mfma_f32_16x16x32_bf16 v[124:127], v[220:223], v[172:175], v[124:127]\n\t"
"s_add_u32 s76, s72, 512\n\t"
"v_mfma_f32_16x16x32_bf16 v[124:127], v[216:219], v[164:167], v[124:127]\n\t"
"buffer_load_dwordx4 v242, s[92:95], s76 offen nt lds\n\t"
"s_waitcnt lgkmcnt(4)\n\t"
"v_mfma_f32_16x16x32_bf16 v[128:131], v[192:195], v[160:163], v[128:131]\n\t"
"ds_read_b128 v[216:219], v232 offset:13312\n\t"
"v_mfma_f32_16x16x32_bf16 v[128:131], v[196:199], v[168:171], v[128:131]\n\t"
"ds_read_b128 v[220:223], v232 offset:19456\n\t"
"v_mfma_f32_16x16x32_bf16 v[136:139], v[196:199], v[172:175], v[136:139]\n\t"
"s_add_u32 s87, s63, 13312\n\t"
"v_mfma_f32_16x16x32_bf16 v[136:139], v[192:195], v[164:167], v[136:139]\n\t"
"buffer_load_dwordx4 v[152:155], v240, s[56:59], s87 offen offset:0\n\t"
"s_waitcnt lgkmcnt(4)\n\t"
"v_mfma_f32_16x16x32_bf16 v[132:135], v[200:203], v[160:163], v[132:135]\n\t"
"ds_read_b128 v[192:195], v232 offset:14336\n\t"
"s_add_u32 m0, s61, 99584\n\t"
"v_mfma_f32_16x16x32_bf16 v[132:135], v[204:207], v[168:171], v[132:135]\n\t"
"s_add_u32 s76, s65, 4718720\n\t"
"buffer_load_dwordx4 v241, s[40:43], s76 offen lds\n\t"
"v_mfma_f32_16x16x32_bf16 v[140:143], v[204:207], v[172:175], v[140:143]\n\t"
"ds_read_b128 v[196:199], v232 offset:20480\n\t"
"s_add_u32 s87, s63, 33792\n\t"
"v_mfma_f32_16x16x32_bf16 v[140:143], v[200:203], v[164:167], v[140:143]\n\t"
"buffer_load_dwordx4 v[156:159], v240, s[56:59], s87 offen offset:0\n\t"
"s_waitcnt vmcnt(8) lgkmcnt(4)\n\t"
"v_mfma_f32_16x16x32_bf16 v[96:99], v[208:211], v[176:179], v[96:99]\n\t"
"ds_read_b128 v[200:203], v232 offset:15360\n\t"
"v_mfma_f32_16x16x32_bf16 v[96:99], v[212:215], v[184:187], v[96:99]\n\t"
"v_mfma_f32_16x16x32_bf16 v[104:107], v[212:215], v[188:191], v[104:107]\n\t"
"ds_read_b128 v[204:207], v232 offset:21504\n\t"
"v_mfma_f32_16x16x32_bf16 v[104:107], v[208:211], v[180:183], v[104:107]\n\t"
"s_waitcnt lgkmcnt(4)\n\t"
"v_mfma_f32_16x16x32_bf16 v[100:103], v[216:219], v[176:179], v[100:103]\n\t"
"ds_read_b128 v[208:211], v232 offset:16384\n\t"
"ds_read_b128 v[212:215], v232 offset:22528\n\t"
"v_mfma_f32_16x16x32_bf16 v[100:103], v[220:223], v[184:187], v[100:103]\n\t"
"s_add_u32 m0, s61, 100608\n\t"
"v_mfma_f32_16x16x32_bf16 v[108:111], v[220:223], v[188:191], v[108:111]\n\t"
"s_add_u32 s76, s65, 4718848\n\t"
"v_mfma_f32_16x16x32_bf16 v[108:111], v[216:219], v[180:183], v[108:111]\n\t"
"buffer_load_dwordx4 v241, s[40:43], s76 offen lds\n\t"
"s_waitcnt lgkmcnt(4)\n\t"
"v_mfma_f32_16x16x32_bf16 v[112:115], v[192:195], v[176:179], v[112:115]\n\t"
"ds_read_b128 v[216:219], v232 offset:17408\n\t"
"v_mfma_f32_16x16x32_bf16 v[112:115], v[196:199], v[184:187], v[112:115]\n\t"
"v_mfma_f32_16x16x32_bf16 v[120:123], v[196:199], v[188:191], v[120:123]\n\t"
"ds_read_b128 v[220:223], v232 offset:23552\n\t"
"v_mfma_f32_16x16x32_bf16 v[120:123], v[192:195], v[180:183], v[120:123]\n\t"
"s_barrier\n\t"
"v_add_u32 v232, 24640, v248\n\t"
"s_waitcnt lgkmcnt(4)\n\t"
"v_mfma_f32_16x16x32_bf16 v[116:119], v[200:203], v[176:179], v[116:119]\n\t"
"ds_read_b64_tr_b16 v[192:193], v232 offset:0\n\t"
"v_mfma_f32_16x16x32_bf16 v[116:119], v[204:207], v[184:187], v[116:119]\n\t"
"ds_read_b64_tr_b16 v[194:195], v232 offset:256\n\t"
"v_mfma_f32_16x16x32_bf16 v[124:127], v[204:207], v[188:191], v[124:127]\n\t"
"ds_read_b64_tr_b16 v[196:197], v232 offset:12320\n\t"
"v_mfma_f32_16x16x32_bf16 v[124:127], v[200:203], v[180:183], v[124:127]\n\t"
"ds_read_b64_tr_b16 v[198:199], v232 offset:12576\n\t"
"s_waitcnt lgkmcnt(6)\n\t"
"v_mfma_f32_16x16x32_bf16 v[128:131], v[208:211], v[176:179], v[128:131]\n\t"
"ds_read_b64_tr_b16 v[200:201], v232 offset:8\n\t"
"v_mfma_f32_16x16x32_bf16 v[128:131], v[212:215], v[184:187], v[128:131]\n\t"
"ds_read_b64_tr_b16 v[202:203], v232 offset:264\n\t"
"v_mfma_f32_16x16x32_bf16 v[136:139], v[212:215], v[188:191], v[136:139]\n\t"
"ds_read_b64_tr_b16 v[204:205], v232 offset:12328\n\t"
"v_mfma_f32_16x16x32_bf16 v[136:139], v[208:211], v[180:183], v[136:139]\n\t"
"ds_read_b64_tr_b16 v[206:207], v232 offset:12584\n\t"
"s_waitcnt lgkmcnt(8)\n\t"
"v_mfma_f32_16x16x32_bf16 v[132:135], v[216:219], v[176:179], v[132:135]\n\t"
"ds_read_b64_tr_b16 v[208:209], v232 offset:512\n\t"
"v_mfma_f32_16x16x32_bf16 v[132:135], v[220:223], v[184:187], v[132:135]\n\t"
"ds_read_b64_tr_b16 v[210:211], v232 offset:768\n\t"
"v_mfma_f32_16x16x32_bf16 v[140:143], v[220:223], v[188:191], v[140:143]\n\t"
"ds_read_b64_tr_b16 v[212:213], v232 offset:12832\n\t"
"v_mfma_f32_16x16x32_bf16 v[140:143], v[216:219], v[180:183], v[140:143]\n\t"
"ds_read_b64_tr_b16 v[214:215], v232 offset:13088\n\t"
"s_waitcnt lgkmcnt(8)\n\t"
"v_mfma_f32_16x16x32_bf16 v[0:3], v[192:195], v[160:163], v[0:3]\n\t"
"ds_read_b64_tr_b16 v[216:217], v232 offset:520\n\t"
"v_mfma_f32_16x16x32_bf16 v[0:3], v[196:199], v[168:171], v[0:3]\n\t"
"ds_read_b64_tr_b16 v[218:219], v232 offset:776\n\t"
"v_mfma_f32_16x16x32_bf16 v[4:7], v[196:199], v[172:175], v[4:7]\n\t"
"ds_read_b64_tr_b16 v[220:221], v232 offset:12840\n\t"
"v_mfma_f32_16x16x32_bf16 v[4:7], v[192:195], v[164:167], v[4:7]\n\t"
"ds_read_b64_tr_b16 v[222:223], v232 offset:13096\n\t"
"s_waitcnt lgkmcnt(8)\n\t"
"v_mfma_f32_16x16x32_bf16 v[8:11], v[200:203], v[160:163], v[8:11]\n\t"
"ds_read_b64_tr_b16 v[192:193], v232 offset:1024\n\t"
"v_mfma_f32_16x16x32_bf16 v[8:11], v[204:207], v[168:171], v[8:11]\n\t"
"ds_read_b64_tr_b16 v[194:195], v232 offset:1280\n\t"
"v_mfma_f32_16x16x32_bf16 v[12:15], v[204:207], v[172:175], v[12:15]\n\t"
"ds_read_b64_tr_b16 v[196:197], v232 offset:13344\n\t"
"v_mfma_f32_16x16x32_bf16 v[12:15], v[200:203], v[164:167], v[12:15]\n\t"
"ds_read_b64_tr_b16 v[198:199], v232 offset:13600\n\t"
"s_waitcnt lgkmcnt(8)\n\t"
"v_mfma_f32_16x16x32_bf16 v[16:19], v[208:211], v[160:163], v[16:19]\n\t"
"ds_read_b64_tr_b16 v[200:201], v232 offset:1032\n\t"
"ds_read_b64_tr_b16 v[202:203], v232 offset:1288\n\t"
"v_mfma_f32_16x16x32_bf16 v[16:19], v[212:215], v[168:171], v[16:19]\n\t"
"ds_read_b64_tr_b16 v[204:205], v232 offset:13352\n\t"
"ds_read_b64_tr_b16 v[206:207], v232 offset:13608\n\t"
"v_mfma_f32_16x16x32_bf16 v[20:23], v[212:215], v[172:175], v[20:23]\n\t"
"s_add_u32 m0, s61, 123200\n\t"
"s_add_u32 s76, s65, 5505024\n\t"
"v_mfma_f32_16x16x32_bf16 v[20:23], v[208:211], v[164:167], v[20:23]\n\t"
"buffer_load_dwordx4 v241, s[40:43], s76 offen lds\n\t"
"s_waitcnt lgkmcnt(8)\n\t"
"v_mfma_f32_16x16x32_bf16 v[24:27], v[216:219], v[160:163], v[24:27]\n\t"
"ds_read_b64_tr_b16 v[208:209], v232 offset:1536\n\t"
"v_mfma_f32_16x16x32_bf16 v[24:27], v[220:223], v[168:171], v[24:27]\n\t"
"ds_read_b64_tr_b16 v[210:211], v232 offset:1792\n\t"
"v_mfma_f32_16x16x32_bf16 v[28:31], v[220:223], v[172:175], v[28:31]\n\t"
"ds_read_b64_tr_b16 v[212:213], v232 offset:13856\n\t"
"v_mfma_f32_16x16x32_bf16 v[28:31], v[216:219], v[164:167], v[28:31]\n\t"
"ds_read_b64_tr_b16 v[214:215], v232 offset:14112\n\t"
"s_waitcnt lgkmcnt(8)\n\t"
"v_mfma_f32_16x16x32_bf16 v[32:35], v[192:195], v[160:163], v[32:35]\n\t"
"ds_read_b64_tr_b16 v[216:217], v232 offset:1544\n\t"
"v_mfma_f32_16x16x32_bf16 v[32:35], v[196:199], v[168:171], v[32:35]\n\t"
"ds_read_b64_tr_b16 v[218:219], v232 offset:1800\n\t"
"v_mfma_f32_16x16x32_bf16 v[36:39], v[196:199], v[172:175], v[36:39]\n\t"
"ds_read_b64_tr_b16 v[220:221], v232 offset:13864\n\t"
"v_mfma_f32_16x16x32_bf16 v[36:39], v[192:195], v[164:167], v[36:39]\n\t"
"ds_read_b64_tr_b16 v[222:223], v232 offset:14120\n\t"
"s_waitcnt lgkmcnt(8)\n\t"
"v_mfma_f32_16x16x32_bf16 v[40:43], v[200:203], v[160:163], v[40:43]\n\t"
"ds_read_b64_tr_b16 v[192:193], v232 offset:2048\n\t"
"ds_read_b64_tr_b16 v[194:195], v232 offset:2304\n\t"
"v_mfma_f32_16x16x32_bf16 v[40:43], v[204:207], v[168:171], v[40:43]\n\t"
"s_add_u32 m0, s61, 124224\n\t"
"s_add_u32 s76, s65, 5505152\n\t"
"v_mfma_f32_16x16x32_bf16 v[44:47], v[204:207], v[172:175], v[44:47]\n\t"
"buffer_load_dwordx4 v241, s[40:43], s76 offen lds\n\t"
"ds_read_b64_tr_b16 v[196:197], v232 offset:14368\n\t"
"v_mfma_f32_16x16x32_bf16 v[44:47], v[200:203], v[164:167], v[44:47]\n\t"
"ds_read_b64_tr_b16 v[198:199], v232 offset:14624\n\t"
"s_waitcnt lgkmcnt(8)\n\t"
"v_mfma_f32_16x16x32_bf16 v[48:51], v[208:211], v[160:163], v[48:51]\n\t"
"ds_read_b64_tr_b16 v[200:201], v232 offset:2056\n\t"
"v_mfma_f32_16x16x32_bf16 v[48:51], v[212:215], v[168:171], v[48:51]\n\t"
"ds_read_b64_tr_b16 v[202:203], v232 offset:2312\n\t"
"v_mfma_f32_16x16x32_bf16 v[52:55], v[212:215], v[172:175], v[52:55]\n\t"
"ds_read_b64_tr_b16 v[204:205], v232 offset:14376\n\t"
"v_mfma_f32_16x16x32_bf16 v[52:55], v[208:211], v[164:167], v[52:55]\n\t"
"ds_read_b64_tr_b16 v[206:207], v232 offset:14632\n\t"
"s_waitcnt lgkmcnt(8)\n\t"
"v_mfma_f32_16x16x32_bf16 v[56:59], v[216:219], v[160:163], v[56:59]\n\t"
"ds_read_b64_tr_b16 v[208:209], v232 offset:2560\n\t"
"ds_read_b64_tr_b16 v[210:211], v232 offset:2816\n\t"
"v_mfma_f32_16x16x32_bf16 v[56:59], v[220:223], v[168:171], v[56:59]\n\t"
"ds_read_b64_tr_b16 v[212:213], v232 offset:14880\n\t"
"ds_read_b64_tr_b16 v[214:215], v232 offset:15136\n\t"
"v_mfma_f32_16x16x32_bf16 v[60:63], v[220:223], v[172:175], v[60:63]\n\t"
"s_add_u32 m0, s61, 125248\n\t"
"s_add_u32 s76, s65, 5505280\n\t"
"v_mfma_f32_16x16x32_bf16 v[60:63], v[216:219], v[164:167], v[60:63]\n\t"
"buffer_load_dwordx4 v241, s[40:43], s76 offen lds\n\t"
"s_waitcnt lgkmcnt(8)\n\t"
"v_mfma_f32_16x16x32_bf16 v[64:67], v[192:195], v[160:163], v[64:67]\n\t"
"ds_read_b64_tr_b16 v[216:217], v232 offset:2568\n\t"
"v_mfma_f32_16x16x32_bf16 v[64:67], v[196:199], v[168:171], v[64:67]\n\t"
"ds_read_b64_tr_b16 v[218:219], v232 offset:2824\n\t"
"v_mfma_f32_16x16x32_bf16 v[68:71], v[196:199], v[172:175], v[68:71]\n\t"
"ds_read_b64_tr_b16 v[220:221], v232 offset:14888\n\t"
"v_mfma_f32_16x16x32_bf16 v[68:71], v[192:195], v[164:167], v[68:71]\n\t"
"ds_read_b64_tr_b16 v[222:223], v232 offset:15144\n\t"
"s_waitcnt vmcnt(7)\n\t"
"s_barrier\n\t"
"v_add_u32 v232, 49280, v248\n\t"
"s_waitcnt lgkmcnt(8)\n\t"
"v_mfma_f32_16x16x32_bf16 v[72:75], v[200:203], v[160:163], v[72:75]\n\t"
"ds_read_b64_tr_b16 v[192:193], v232 offset:0\n\t"
"v_mfma_f32_16x16x32_bf16 v[72:75], v[204:207], v[168:171], v[72:75]\n\t"
"ds_read_b64_tr_b16 v[194:195], v232 offset:256\n\t"
"v_mfma_f32_16x16x32_bf16 v[76:79], v[204:207], v[172:175], v[76:79]\n\t"
"ds_read_b64_tr_b16 v[196:197], v232 offset:12320\n\t"
"v_mfma_f32_16x16x32_bf16 v[76:79], v[200:203], v[164:167], v[76:79]\n\t"
"ds_read_b64_tr_b16 v[198:199], v232 offset:12576\n\t"
"s_waitcnt lgkmcnt(8)\n\t"
"v_mfma_f32_16x16x32_bf16 v[80:83], v[208:211], v[160:163], v[80:83]\n\t"
"ds_read_b64_tr_b16 v[200:201], v232 offset:8\n\t"
"v_mfma_f32_16x16x32_bf16 v[80:83], v[212:215], v[168:171], v[80:83]\n\t"
"ds_read_b64_tr_b16 v[202:203], v232 offset:264\n\t"
"v_mfma_f32_16x16x32_bf16 v[84:87], v[212:215], v[172:175], v[84:87]\n\t"
"ds_read_b64_tr_b16 v[204:205], v232 offset:12328\n\t"
"v_mfma_f32_16x16x32_bf16 v[84:87], v[208:211], v[164:167], v[84:87]\n\t"
"ds_read_b64_tr_b16 v[206:207], v232 offset:12584\n\t"
"s_waitcnt lgkmcnt(8)\n\t"
"v_mfma_f32_16x16x32_bf16 v[88:91], v[216:219], v[160:163], v[88:91]\n\t"
"ds_read_b64_tr_b16 v[208:209], v232 offset:512\n\t"
"v_mfma_f32_16x16x32_bf16 v[88:91], v[220:223], v[168:171], v[88:91]\n\t"
"ds_read_b64_tr_b16 v[210:211], v232 offset:768\n\t"
"v_mfma_f32_16x16x32_bf16 v[92:95], v[220:223], v[172:175], v[92:95]\n\t"
"ds_read_b64_tr_b16 v[212:213], v232 offset:12832\n\t"
"v_mfma_f32_16x16x32_bf16 v[92:95], v[216:219], v[164:167], v[92:95]\n\t"
"ds_read_b64_tr_b16 v[214:215], v232 offset:13088\n\t"
"s_waitcnt lgkmcnt(8)\n\t"
"v_mfma_f32_16x16x32_bf16 v[0:3], v[192:195], v[176:179], v[0:3]\n\t"
"ds_read_b64_tr_b16 v[216:217], v232 offset:520\n\t"
"ds_read_b64_tr_b16 v[218:219], v232 offset:776\n\t"
"v_mfma_f32_16x16x32_bf16 v[0:3], v[196:199], v[184:187], v[0:3]\n\t"
"ds_read_b64_tr_b16 v[234:235], v243 offset:0\n\t"
"ds_read_b64_tr_b16 v[236:237], v243 offset:4096\n\t"
"v_mfma_f32_16x16x32_bf16 v[4:7], v[196:199], v[188:191], v[4:7]\n\t"
"ds_read_b64_tr_b16 v[220:221], v232 offset:12840\n\t"
"ds_read_b64_tr_b16 v[222:223], v232 offset:13096\n\t"
"v_mfma_f32_16x16x32_bf16 v[4:7], v[192:195], v[180:183], v[4:7]\n\t"
"s_add_u32 s87, s63, 14336\n\t"
"buffer_load_dwordx4 v[160:163], v240, s[56:59], s87 offen offset:0\n\t"
"s_waitcnt lgkmcnt(10)\n\t"
"v_mfma_f32_16x16x32_bf16 v[8:11], v[200:203], v[176:179], v[8:11]\n\t"
"ds_read_b64_tr_b16 v[192:193], v232 offset:1024\n\t"
"ds_read_b64_tr_b16 v[194:195], v232 offset:1280\n\t"
"v_mfma_f32_16x16x32_bf16 v[8:11], v[204:207], v[184:187], v[8:11]\n\t"
"ds_read_b64_tr_b16 v[196:197], v232 offset:13344\n\t"
"v_mfma_f32_16x16x32_bf16 v[12:15], v[204:207], v[188:191], v[12:15]\n\t"
"ds_read_b64_tr_b16 v[198:199], v232 offset:13600\n\t"
"s_add_u32 s87, s63, 34816\n\t"
"v_mfma_f32_16x16x32_bf16 v[12:15], v[200:203], v[180:183], v[12:15]\n\t"
"buffer_load_dwordx4 v[164:167], v240, s[56:59], s87 offen offset:0\n\t"
"s_waitcnt lgkmcnt(10)\n\t"
"v_mfma_f32_16x16x32_bf16 v[16:19], v[208:211], v[176:179], v[16:19]\n\t"
"ds_read_b64_tr_b16 v[200:201], v232 offset:1032\n\t"
"ds_read_b64_tr_b16 v[202:203], v232 offset:1288\n\t"
"v_mfma_f32_16x16x32_bf16 v[16:19], v[212:215], v[184:187], v[16:19]\n\t"
"ds_read_b64_tr_b16 v[204:205], v232 offset:13352\n\t"
"ds_read_b64_tr_b16 v[206:207], v232 offset:13608\n\t"
"v_mfma_f32_16x16x32_bf16 v[20:23], v[212:215], v[188:191], v[20:23]\n\t"
"s_add_u32 m0, s62, 0\n\t"
"s_add_u32 s76, s64, 98304\n\t"
"v_mfma_f32_16x16x32_bf16 v[20:23], v[208:211], v[180:183], v[20:23]\n\t"
"buffer_load_dwordx4 v240, s[56:59], s76 offen lds\n\t"
"s_waitcnt lgkmcnt(8)\n\t"
"v_mfma_f32_16x16x32_bf16 v[24:27], v[216:219], v[176:179], v[24:27]\n\t"
"ds_read_b64_tr_b16 v[208:209], v232 offset:1536\n\t"
"ds_read_b64_tr_b16 v[210:211], v232 offset:1792\n\t"
"v_mfma_f32_16x16x32_bf16 v[24:27], v[220:223], v[184:187], v[24:27]\n\t"
"ds_read_b64_tr_b16 v[212:213], v232 offset:13856\n\t"
"ds_read_b64_tr_b16 v[214:215], v232 offset:14112\n\t"
"v_mfma_f32_16x16x32_bf16 v[28:31], v[220:223], v[188:191], v[28:31]\n\t"
"s_add_u32 s76, s74, 16384\n\t"
"v_mfma_f32_16x16x32_bf16 v[28:31], v[216:219], v[180:183], v[28:31]\n\t"
"buffer_store_dwordx4 v[234:237], v240, s[56:59], s76 offen offset:0 sc1\n\t"
"s_waitcnt lgkmcnt(8)\n\t"
"v_mfma_f32_16x16x32_bf16 v[32:35], v[192:195], v[176:179], v[32:35]\n\t"
"ds_read_b64_tr_b16 v[216:217], v232 offset:1544\n\t"
"ds_read_b64_tr_b16 v[218:219], v232 offset:1800\n\t"
"v_mfma_f32_16x16x32_bf16 v[32:35], v[196:199], v[184:187], v[32:35]\n\t"
"ds_read_b64_tr_b16 v[220:221], v232 offset:13864\n\t"
"v_mfma_f32_16x16x32_bf16 v[36:39], v[196:199], v[188:191], v[36:39]\n\t"
"ds_read_b64_tr_b16 v[222:223], v232 offset:14120\n\t"
"s_add_u32 s87, s63, 15360\n\t"
"v_mfma_f32_16x16x32_bf16 v[36:39], v[192:195], v[180:183], v[36:39]\n\t"
"buffer_load_dwordx4 v[168:171], v240, s[56:59], s87 offen offset:0\n\t"
"s_waitcnt lgkmcnt(8)\n\t"
"v_mfma_f32_16x16x32_bf16 v[40:43], v[200:203], v[176:179], v[40:43]\n\t"
"ds_read_b64_tr_b16 v[192:193], v232 offset:2048\n\t"
"ds_read_b64_tr_b16 v[194:195], v232 offset:2304\n\t"
"s_add_u32 m0, s62, 1024\n\t"
"v_mfma_f32_16x16x32_bf16 v[40:43], v[204:207], v[184:187], v[40:43]\n\t"
"s_add_u32 s76, s64, 99328\n\t"
"buffer_load_dwordx4 v240, s[56:59], s76 offen lds\n\t"
"v_mfma_f32_16x16x32_bf16 v[44:47], v[204:207], v[188:191], v[44:47]\n\t"
"ds_read_b64_tr_b16 v[196:197], v232 offset:14368\n\t"
"ds_read_b64_tr_b16 v[198:199], v232 offset:14624\n\t"
"v_mfma_f32_16x16x32_bf16 v[44:47], v[200:203], v[180:183], v[44:47]\n\t"
"s_add_u32 s87, s63, 35840\n\t"
"buffer_load_dwordx4 v[172:175], v240, s[56:59], s87 offen offset:0\n\t"
"s_waitcnt lgkmcnt(8)\n\t"
"v_mfma_f32_16x16x32_bf16 v[48:51], v[208:211], v[176:179], v[48:51]\n\t"
"ds_read_b64_tr_b16 v[200:201], v232 offset:2056\n\t"
"v_mfma_f32_16x16x32_bf16 v[48:51], v[212:215], v[184:187], v[48:51]\n\t"
"ds_read_b64_tr_b16 v[202:203], v232 offset:2312\n\t"
"v_mfma_f32_16x16x32_bf16 v[52:55], v[212:215], v[188:191], v[52:55]\n\t"
"ds_read_b64_tr_b16 v[204:205], v232 offset:14376\n\t"
"v_mfma_f32_16x16x32_bf16 v[52:55], v[208:211], v[180:183], v[52:55]\n\t"
"ds_read_b64_tr_b16 v[206:207], v232 offset:14632\n\t"
"s_waitcnt lgkmcnt(8)\n\t"
"v_mfma_f32_16x16x32_bf16 v[56:59], v[216:219], v[176:179], v[56:59]\n\t"
"ds_read_b64_tr_b16 v[208:209], v232 offset:2560\n\t"
"ds_read_b64_tr_b16 v[210:211], v232 offset:2816\n\t"
"v_mfma_f32_16x16x32_bf16 v[56:59], v[220:223], v[184:187], v[56:59]\n\t"
"ds_read_b64_tr_b16 v[212:213], v232 offset:14880\n\t"
"ds_read_b64_tr_b16 v[214:215], v232 offset:15136\n\t"
"v_mfma_f32_16x16x32_bf16 v[60:63], v[220:223], v[188:191], v[60:63]\n\t"
"s_add_u32 m0, s62, 2048\n\t"
"s_add_u32 s76, s64, 100352\n\t"
"v_mfma_f32_16x16x32_bf16 v[60:63], v[216:219], v[180:183], v[60:63]\n\t"
"buffer_load_dwordx4 v240, s[56:59], s76 offen lds\n\t"
"s_waitcnt lgkmcnt(8)\n\t"
"v_mfma_f32_16x16x32_bf16 v[64:67], v[192:195], v[176:179], v[64:67]\n\t"
"ds_read_b64_tr_b16 v[216:217], v232 offset:2568\n\t"
"v_mfma_f32_16x16x32_bf16 v[64:67], v[196:199], v[184:187], v[64:67]\n\t"
"ds_read_b64_tr_b16 v[218:219], v232 offset:2824\n\t"
"v_mfma_f32_16x16x32_bf16 v[68:71], v[196:199], v[188:191], v[68:71]\n\t"
"ds_read_b64_tr_b16 v[220:221], v232 offset:14888\n\t"
"v_mfma_f32_16x16x32_bf16 v[68:71], v[192:195], v[180:183], v[68:71]\n\t"
"ds_read_b64_tr_b16 v[222:223], v232 offset:15144\n\t"
"s_barrier\n\t"
"v_add_u32 v232, 73920, v247\n\t"
"s_waitcnt lgkmcnt(8)\n\t"
"v_mfma_f32_16x16x32_bf16 v[72:75], v[200:203], v[176:179], v[72:75]\n\t"
"ds_read_b128 v[192:195], v232 offset:0\n\t"
"v_mfma_f32_16x16x32_bf16 v[72:75], v[204:207], v[184:187], v[72:75]\n\t"
"v_mfma_f32_16x16x32_bf16 v[76:79], v[204:207], v[188:191], v[76:79]\n\t"
"ds_read_b128 v[196:199], v232 offset:6144\n\t"
"v_mfma_f32_16x16x32_bf16 v[76:79], v[200:203], v[180:183], v[76:79]\n\t"
"s_waitcnt lgkmcnt(6)\n\t"
"v_mfma_f32_16x16x32_bf16 v[80:83], v[208:211], v[176:179], v[80:83]\n\t"
"ds_read_b128 v[200:203], v232 offset:1024\n\t"
"v_mfma_f32_16x16x32_bf16 v[80:83], v[212:215], v[184:187], v[80:83]\n\t"
"v_mfma_f32_16x16x32_bf16 v[84:87], v[212:215], v[188:191], v[84:87]\n\t"
"ds_read_b128 v[204:207], v232 offset:7168\n\t"
"v_mfma_f32_16x16x32_bf16 v[84:87], v[208:211], v[180:183], v[84:87]\n\t"
"s_waitcnt lgkmcnt(4)\n\t"
"v_mfma_f32_16x16x32_bf16 v[88:91], v[216:219], v[176:179], v[88:91]\n\t"
"ds_read_b128 v[208:211], v232 offset:2048\n\t"
"v_mfma_f32_16x16x32_bf16 v[88:91], v[220:223], v[184:187], v[88:91]\n\t"
"v_mfma_f32_16x16x32_bf16 v[92:95], v[220:223], v[188:191], v[92:95]\n\t"
"ds_read_b128 v[212:215], v232 offset:8192\n\t"
"v_mfma_f32_16x16x32_bf16 v[92:95], v[216:219], v[180:183], v[92:95]\n\t"
"s_waitcnt vmcnt(12) lgkmcnt(4)\n\t"
"v_mfma_f32_16x16x32_bf16 v[96:99], v[192:195], v[144:147], v[96:99]\n\t"
"ds_read_b128 v[216:219], v232 offset:3072\n\t"
"v_mfma_f32_16x16x32_bf16 v[96:99], v[196:199], v[152:155], v[96:99]\n\t"
"ds_read_b128 v[220:223], v232 offset:9216\n\t"
"v_mfma_f32_16x16x32_bf16 v[104:107], v[196:199], v[156:159], v[104:107]\n\t"
"s_add_u32 s87, s63, 16384\n\t"
"v_mfma_f32_16x16x32_bf16 v[104:107], v[192:195], v[148:151], v[104:107]\n\t"
"buffer_load_dwordx4 v[176:179], v240, s[56:59], s87 offen offset:0\n\t"
"s_waitcnt lgkmcnt(4)\n\t"
"v_mfma_f32_16x16x32_bf16 v[100:103], v[200:203], v[144:147], v[100:103]\n\t"
"ds_read_b128 v[192:195], v232 offset:4096\n\t"
"v_mfma_f32_16x16x32_bf16 v[100:103], v[204:207], v[152:155], v[100:103]\n\t"
"ds_read_b128 v[196:199], v232 offset:10240\n\t"
"v_mfma_f32_16x16x32_bf16 v[108:111], v[204:207], v[156:159], v[108:111]\n\t"
"s_add_u32 s87, s63, 36864\n\t"
"v_mfma_f32_16x16x32_bf16 v[108:111], v[200:203], v[148:151], v[108:111]\n\t"
"buffer_load_dwordx4 v[180:183], v240, s[56:59], s87 offen offset:0\n\t"
"s_waitcnt lgkmcnt(4)\n\t"
"v_mfma_f32_16x16x32_bf16 v[112:115], v[208:211], v[144:147], v[112:115]\n\t"
"ds_read_b128 v[200:203], v232 offset:5120\n\t"
"ds_read_b128 v[204:207], v232 offset:11264\n\t"
"v_mfma_f32_16x16x32_bf16 v[112:115], v[212:215], v[152:155], v[112:115]\n\t"
"s_add_u32 m0, s61, 24640\n\t"
"v_mfma_f32_16x16x32_bf16 v[120:123], v[212:215], v[156:159], v[120:123]\n\t"
"s_add_u32 s76, s65, 6291456\n\t"
"v_mfma_f32_16x16x32_bf16 v[120:123], v[208:211], v[148:151], v[120:123]\n\t"
"buffer_load_dwordx4 v241, s[40:43], s76 offen lds\n\t"
"s_waitcnt lgkmcnt(4)\n\t"
"v_mfma_f32_16x16x32_bf16 v[116:119], v[216:219], v[144:147], v[116:119]\n\t"
"ds_read_b128 v[208:211], v232 offset:12288\n\t"
"ds_read_b128 v[212:215], v232 offset:18432\n\t"
"v_mfma_f32_16x16x32_bf16 v[116:119], v[220:223], v[152:155], v[116:119]\n\t"
"s_add_u32 m0, s73, 147840\n\t"
"v_mfma_f32_16x16x32_bf16 v[124:127], v[220:223], v[156:159], v[124:127]\n\t"
"s_add_u32 s76, s72, 768\n\t"
"v_mfma_f32_16x16x32_bf16 v[124:127], v[216:219], v[148:151], v[124:127]\n\t"
"buffer_load_dwordx4 v242, s[92:95], s76 offen nt lds\n\t"
"s_waitcnt lgkmcnt(4)\n\t"
"v_mfma_f32_16x16x32_bf16 v[128:131], v[192:195], v[144:147], v[128:131]\n\t"
"ds_read_b128 v[216:219], v232 offset:13312\n\t"
"v_mfma_f32_16x16x32_bf16 v[128:131], v[196:199], v[152:155], v[128:131]\n\t"
"ds_read_b128 v[220:223], v232 offset:19456\n\t"
"v_mfma_f32_16x16x32_bf16 v[136:139], v[196:199], v[156:159], v[136:139]\n\t"
"s_add_u32 s87, s63, 17408\n\t"
"v_mfma_f32_16x16x32_bf16 v[136:139], v[192:195], v[148:151], v[136:139]\n\t"
"buffer_load_dwordx4 v[184:187], v240, s[56:59], s87 offen offset:0\n\t"
"s_waitcnt lgkmcnt(4)\n\t"
"v_mfma_f32_16x16x32_bf16 v[132:135], v[200:203], v[144:147], v[132:135]\n\t"
"ds_read_b128 v[192:195], v232 offset:14336\n\t"
"s_add_u32 m0, s61, 25664\n\t"
"v_mfma_f32_16x16x32_bf16 v[132:135], v[204:207], v[152:155], v[132:135]\n\t"
"s_add_u32 s76, s65, 6291584\n\t"
"buffer_load_dwordx4 v241, s[40:43], s76 offen lds\n\t"
"v_mfma_f32_16x16x32_bf16 v[140:143], v[204:207], v[156:159], v[140:143]\n\t"
"ds_read_b128 v[196:199], v232 offset:20480\n\t"
"s_add_u32 s87, s63, 37888\n\t"
"v_mfma_f32_16x16x32_bf16 v[140:143], v[200:203], v[148:151], v[140:143]\n\t"
"buffer_load_dwordx4 v[188:191], v240, s[56:59], s87 offen offset:0\n\t"
"s_waitcnt vmcnt(8) lgkmcnt(4)\n\t"
"v_mfma_f32_16x16x32_bf16 v[96:99], v[208:211], v[160:163], v[96:99]\n\t"
"ds_read_b128 v[200:203], v232 offset:15360\n\t"
"v_mfma_f32_16x16x32_bf16 v[96:99], v[212:215], v[168:171], v[96:99]\n\t"
"v_mfma_f32_16x16x32_bf16 v[104:107], v[212:215], v[172:175], v[104:107]\n\t"
"ds_read_b128 v[204:207], v232 offset:21504\n\t"
"v_mfma_f32_16x16x32_bf16 v[104:107], v[208:211], v[164:167], v[104:107]\n\t"
"s_waitcnt lgkmcnt(4)\n\t"
"v_mfma_f32_16x16x32_bf16 v[100:103], v[216:219], v[160:163], v[100:103]\n\t"
"ds_read_b128 v[208:211], v232 offset:16384\n\t"
"ds_read_b128 v[212:215], v232 offset:22528\n\t"
"v_mfma_f32_16x16x32_bf16 v[100:103], v[220:223], v[168:171], v[100:103]\n\t"
"s_add_u32 m0, s61, 26688\n\t"
"v_mfma_f32_16x16x32_bf16 v[108:111], v[220:223], v[172:175], v[108:111]\n\t"
"s_add_u32 s76, s65, 6291712\n\t"
"v_mfma_f32_16x16x32_bf16 v[108:111], v[216:219], v[164:167], v[108:111]\n\t"
"buffer_load_dwordx4 v241, s[40:43], s76 offen lds\n\t"
"s_waitcnt lgkmcnt(4)\n\t"
"v_mfma_f32_16x16x32_bf16 v[112:115], v[192:195], v[160:163], v[112:115]\n\t"
"ds_read_b128 v[216:219], v232 offset:17408\n\t"
"v_mfma_f32_16x16x32_bf16 v[112:115], v[196:199], v[168:171], v[112:115]\n\t"
"v_mfma_f32_16x16x32_bf16 v[120:123], v[196:199], v[172:175], v[120:123]\n\t"
"ds_read_b128 v[220:223], v232 offset:23552\n\t"
"v_mfma_f32_16x16x32_bf16 v[120:123], v[192:195], v[164:167], v[120:123]\n\t"
"s_barrier\n\t"
"v_add_u32 v232, 98560, v248\n\t"
"s_waitcnt lgkmcnt(4)\n\t"
"v_mfma_f32_16x16x32_bf16 v[116:119], v[200:203], v[160:163], v[116:119]\n\t"
"ds_read_b64_tr_b16 v[192:193], v232 offset:0\n\t"
"v_mfma_f32_16x16x32_bf16 v[116:119], v[204:207], v[168:171], v[116:119]\n\t"
"ds_read_b64_tr_b16 v[194:195], v232 offset:256\n\t"
"v_mfma_f32_16x16x32_bf16 v[124:127], v[204:207], v[172:175], v[124:127]\n\t"
"ds_read_b64_tr_b16 v[196:197], v232 offset:12320\n\t"
"v_mfma_f32_16x16x32_bf16 v[124:127], v[200:203], v[164:167], v[124:127]\n\t"
"ds_read_b64_tr_b16 v[198:199], v232 offset:12576\n\t"
"s_waitcnt lgkmcnt(6)\n\t"
"v_mfma_f32_16x16x32_bf16 v[128:131], v[208:211], v[160:163], v[128:131]\n\t"
"ds_read_b64_tr_b16 v[200:201], v232 offset:8\n\t"
"v_mfma_f32_16x16x32_bf16 v[128:131], v[212:215], v[168:171], v[128:131]\n\t"
"ds_read_b64_tr_b16 v[202:203], v232 offset:264\n\t"
"v_mfma_f32_16x16x32_bf16 v[136:139], v[212:215], v[172:175], v[136:139]\n\t"
"ds_read_b64_tr_b16 v[204:205], v232 offset:12328\n\t"
"v_mfma_f32_16x16x32_bf16 v[136:139], v[208:211], v[164:167], v[136:139]\n\t"
"ds_read_b64_tr_b16 v[206:207], v232 offset:12584\n\t"
"s_waitcnt lgkmcnt(8)\n\t"
"v_mfma_f32_16x16x32_bf16 v[132:135], v[216:219], v[160:163], v[132:135]\n\t"
"ds_read_b64_tr_b16 v[208:209], v232 offset:512\n\t"
"v_mfma_f32_16x16x32_bf16 v[132:135], v[220:223], v[168:171], v[132:135]\n\t"
"ds_read_b64_tr_b16 v[210:211], v232 offset:768\n\t"
"v_mfma_f32_16x16x32_bf16 v[140:143], v[220:223], v[172:175], v[140:143]\n\t"
"ds_read_b64_tr_b16 v[212:213], v232 offset:12832\n\t"
"v_mfma_f32_16x16x32_bf16 v[140:143], v[216:219], v[164:167], v[140:143]\n\t"
"ds_read_b64_tr_b16 v[214:215], v232 offset:13088\n\t"
"s_waitcnt lgkmcnt(8)\n\t"
"v_mfma_f32_16x16x32_bf16 v[0:3], v[192:195], v[144:147], v[0:3]\n\t"
"ds_read_b64_tr_b16 v[216:217], v232 offset:520\n\t"
"v_mfma_f32_16x16x32_bf16 v[0:3], v[196:199], v[152:155], v[0:3]\n\t"
"ds_read_b64_tr_b16 v[218:219], v232 offset:776\n\t"
"v_mfma_f32_16x16x32_bf16 v[4:7], v[196:199], v[156:159], v[4:7]\n\t"
"ds_read_b64_tr_b16 v[220:221], v232 offset:12840\n\t"
"v_mfma_f32_16x16x32_bf16 v[4:7], v[192:195], v[148:151], v[4:7]\n\t"
"ds_read_b64_tr_b16 v[222:223], v232 offset:13096\n\t"
"s_waitcnt lgkmcnt(8)\n\t"
"v_mfma_f32_16x16x32_bf16 v[8:11], v[200:203], v[144:147], v[8:11]\n\t"
"ds_read_b64_tr_b16 v[192:193], v232 offset:1024\n\t"
"v_mfma_f32_16x16x32_bf16 v[8:11], v[204:207], v[152:155], v[8:11]\n\t"
"ds_read_b64_tr_b16 v[194:195], v232 offset:1280\n\t"
"v_mfma_f32_16x16x32_bf16 v[12:15], v[204:207], v[156:159], v[12:15]\n\t"
"ds_read_b64_tr_b16 v[196:197], v232 offset:13344\n\t"
"v_mfma_f32_16x16x32_bf16 v[12:15], v[200:203], v[148:151], v[12:15]\n\t"
"ds_read_b64_tr_b16 v[198:199], v232 offset:13600\n\t"
"s_waitcnt lgkmcnt(8)\n\t"
"v_mfma_f32_16x16x32_bf16 v[16:19], v[208:211], v[144:147], v[16:19]\n\t"
"ds_read_b64_tr_b16 v[200:201], v232 offset:1032\n\t"
"ds_read_b64_tr_b16 v[202:203], v232 offset:1288\n\t"
"v_mfma_f32_16x16x32_bf16 v[16:19], v[212:215], v[152:155], v[16:19]\n\t"
"ds_read_b64_tr_b16 v[204:205], v232 offset:13352\n\t"
"ds_read_b64_tr_b16 v[206:207], v232 offset:13608\n\t"
"v_mfma_f32_16x16x32_bf16 v[20:23], v[212:215], v[156:159], v[20:23]\n\t"
"s_add_u32 m0, s61, 49280\n\t"
"s_add_u32 s76, s65, 7077888\n\t"
"v_mfma_f32_16x16x32_bf16 v[20:23], v[208:211], v[148:151], v[20:23]\n\t"
"buffer_load_dwordx4 v241, s[40:43], s76 offen lds\n\t"
"s_waitcnt lgkmcnt(8)\n\t"
"v_mfma_f32_16x16x32_bf16 v[24:27], v[216:219], v[144:147], v[24:27]\n\t"
"ds_read_b64_tr_b16 v[208:209], v232 offset:1536\n\t"
"v_mfma_f32_16x16x32_bf16 v[24:27], v[220:223], v[152:155], v[24:27]\n\t"
"ds_read_b64_tr_b16 v[210:211], v232 offset:1792\n\t"
"v_mfma_f32_16x16x32_bf16 v[28:31], v[220:223], v[156:159], v[28:31]\n\t"
"ds_read_b64_tr_b16 v[212:213], v232 offset:13856\n\t"
"v_mfma_f32_16x16x32_bf16 v[28:31], v[216:219], v[148:151], v[28:31]\n\t"
"ds_read_b64_tr_b16 v[214:215], v232 offset:14112\n\t"
"s_waitcnt lgkmcnt(8)\n\t"
"v_mfma_f32_16x16x32_bf16 v[32:35], v[192:195], v[144:147], v[32:35]\n\t"
"ds_read_b64_tr_b16 v[216:217], v232 offset:1544\n\t"
"v_mfma_f32_16x16x32_bf16 v[32:35], v[196:199], v[152:155], v[32:35]\n\t"
"ds_read_b64_tr_b16 v[218:219], v232 offset:1800\n\t"
"v_mfma_f32_16x16x32_bf16 v[36:39], v[196:199], v[156:159], v[36:39]\n\t"
"ds_read_b64_tr_b16 v[220:221], v232 offset:13864\n\t"
"v_mfma_f32_16x16x32_bf16 v[36:39], v[192:195], v[148:151], v[36:39]\n\t"
"ds_read_b64_tr_b16 v[222:223], v232 offset:14120\n\t"
"s_waitcnt lgkmcnt(8)\n\t"
"v_mfma_f32_16x16x32_bf16 v[40:43], v[200:203], v[144:147], v[40:43]\n\t"
"ds_read_b64_tr_b16 v[192:193], v232 offset:2048\n\t"
"ds_read_b64_tr_b16 v[194:195], v232 offset:2304\n\t"
"v_mfma_f32_16x16x32_bf16 v[40:43], v[204:207], v[152:155], v[40:43]\n\t"
"s_add_u32 m0, s61, 50304\n\t"
"s_add_u32 s76, s65, 7078016\n\t"
"v_mfma_f32_16x16x32_bf16 v[44:47], v[204:207], v[156:159], v[44:47]\n\t"
"buffer_load_dwordx4 v241, s[40:43], s76 offen lds\n\t"
"ds_read_b64_tr_b16 v[196:197], v232 offset:14368\n\t"
"v_mfma_f32_16x16x32_bf16 v[44:47], v[200:203], v[148:151], v[44:47]\n\t"
"ds_read_b64_tr_b16 v[198:199], v232 offset:14624\n\t"
"s_waitcnt lgkmcnt(8)\n\t"
"v_mfma_f32_16x16x32_bf16 v[48:51], v[208:211], v[144:147], v[48:51]\n\t"
"ds_read_b64_tr_b16 v[200:201], v232 offset:2056\n\t"
"v_mfma_f32_16x16x32_bf16 v[48:51], v[212:215], v[152:155], v[48:51]\n\t"
"ds_read_b64_tr_b16 v[202:203], v232 offset:2312\n\t"
"v_mfma_f32_16x16x32_bf16 v[52:55], v[212:215], v[156:159], v[52:55]\n\t"
"ds_read_b64_tr_b16 v[204:205], v232 offset:14376\n\t"
"v_mfma_f32_16x16x32_bf16 v[52:55], v[208:211], v[148:151], v[52:55]\n\t"
"ds_read_b64_tr_b16 v[206:207], v232 offset:14632\n\t"
"s_waitcnt lgkmcnt(8)\n\t"
"v_mfma_f32_16x16x32_bf16 v[56:59], v[216:219], v[144:147], v[56:59]\n\t"
"ds_read_b64_tr_b16 v[208:209], v232 offset:2560\n\t"
"ds_read_b64_tr_b16 v[210:211], v232 offset:2816\n\t"
"v_mfma_f32_16x16x32_bf16 v[56:59], v[220:223], v[152:155], v[56:59]\n\t"
"ds_read_b64_tr_b16 v[212:213], v232 offset:14880\n\t"
"ds_read_b64_tr_b16 v[214:215], v232 offset:15136\n\t"
"v_mfma_f32_16x16x32_bf16 v[60:63], v[220:223], v[156:159], v[60:63]\n\t"
"s_add_u32 m0, s61, 51328\n\t"
"s_add_u32 s76, s65, 7078144\n\t"
"v_mfma_f32_16x16x32_bf16 v[60:63], v[216:219], v[148:151], v[60:63]\n\t"
"buffer_load_dwordx4 v241, s[40:43], s76 offen lds\n\t"
"s_waitcnt lgkmcnt(8)\n\t"
"v_mfma_f32_16x16x32_bf16 v[64:67], v[192:195], v[144:147], v[64:67]\n\t"
"ds_read_b64_tr_b16 v[216:217], v232 offset:2568\n\t"
"v_mfma_f32_16x16x32_bf16 v[64:67], v[196:199], v[152:155], v[64:67]\n\t"
"ds_read_b64_tr_b16 v[218:219], v232 offset:2824\n\t"
"v_mfma_f32_16x16x32_bf16 v[68:71], v[196:199], v[156:159], v[68:71]\n\t"
"ds_read_b64_tr_b16 v[220:221], v232 offset:14888\n\t"
"v_mfma_f32_16x16x32_bf16 v[68:71], v[192:195], v[148:151], v[68:71]\n\t"
"ds_read_b64_tr_b16 v[222:223], v232 offset:15144\n\t"
"s_waitcnt vmcnt(7)\n\t"
"s_barrier\n\t"
"v_add_u32 v232, 123200, v248\n\t"
"s_waitcnt lgkmcnt(8)\n\t"
"v_mfma_f32_16x16x32_bf16 v[72:75], v[200:203], v[144:147], v[72:75]\n\t"
"ds_read_b64_tr_b16 v[192:193], v232 offset:0\n\t"
"v_mfma_f32_16x16x32_bf16 v[72:75], v[204:207], v[152:155], v[72:75]\n\t"
"ds_read_b64_tr_b16 v[194:195], v232 offset:256\n\t"
"v_mfma_f32_16x16x32_bf16 v[76:79], v[204:207], v[156:159], v[76:79]\n\t"
"ds_read_b64_tr_b16 v[196:197], v232 offset:12320\n\t"
"v_mfma_f32_16x16x32_bf16 v[76:79], v[200:203], v[148:151], v[76:79]\n\t"
"ds_read_b64_tr_b16 v[198:199], v232 offset:12576\n\t"
"s_waitcnt lgkmcnt(8)\n\t"
"v_mfma_f32_16x16x32_bf16 v[80:83], v[208:211], v[144:147], v[80:83]\n\t"
"ds_read_b64_tr_b16 v[200:201], v232 offset:8\n\t"
"v_mfma_f32_16x16x32_bf16 v[80:83], v[212:215], v[152:155], v[80:83]\n\t"
"ds_read_b64_tr_b16 v[202:203], v232 offset:264\n\t"
"v_mfma_f32_16x16x32_bf16 v[84:87], v[212:215], v[156:159], v[84:87]\n\t"
"ds_read_b64_tr_b16 v[204:205], v232 offset:12328\n\t"
"v_mfma_f32_16x16x32_bf16 v[84:87], v[208:211], v[148:151], v[84:87]\n\t"
"ds_read_b64_tr_b16 v[206:207], v232 offset:12584\n\t"
"s_waitcnt lgkmcnt(8)\n\t"
"v_mfma_f32_16x16x32_bf16 v[88:91], v[216:219], v[144:147], v[88:91]\n\t"
"ds_read_b64_tr_b16 v[208:209], v232 offset:512\n\t"
"v_mfma_f32_16x16x32_bf16 v[88:91], v[220:223], v[152:155], v[88:91]\n\t"
"ds_read_b64_tr_b16 v[210:211], v232 offset:768\n\t"
"v_mfma_f32_16x16x32_bf16 v[92:95], v[220:223], v[156:159], v[92:95]\n\t"
"ds_read_b64_tr_b16 v[212:213], v232 offset:12832\n\t"
"v_mfma_f32_16x16x32_bf16 v[92:95], v[216:219], v[148:151], v[92:95]\n\t"
"ds_read_b64_tr_b16 v[214:215], v232 offset:13088\n\t"
"s_waitcnt lgkmcnt(8)\n\t"
"v_mfma_f32_16x16x32_bf16 v[0:3], v[192:195], v[160:163], v[0:3]\n\t"
"ds_read_b64_tr_b16 v[216:217], v232 offset:520\n\t"
"ds_read_b64_tr_b16 v[218:219], v232 offset:776\n\t"
"v_mfma_f32_16x16x32_bf16 v[0:3], v[196:199], v[168:171], v[0:3]\n\t"
"ds_read_b64_tr_b16 v[234:235], v243 offset:0\n\t"
"ds_read_b64_tr_b16 v[236:237], v243 offset:4096\n\t"
"v_mfma_f32_16x16x32_bf16 v[4:7], v[196:199], v[172:175], v[4:7]\n\t"
"ds_read_b64_tr_b16 v[220:221], v232 offset:12840\n\t"
"ds_read_b64_tr_b16 v[222:223], v232 offset:13096\n\t"
"v_mfma_f32_16x16x32_bf16 v[4:7], v[192:195], v[164:167], v[4:7]\n\t"
"s_add_u32 s87, s63, 18432\n\t"
"buffer_load_dwordx4 v[144:147], v240, s[56:59], s87 offen offset:0\n\t"
"s_waitcnt lgkmcnt(10)\n\t"
"v_mfma_f32_16x16x32_bf16 v[8:11], v[200:203], v[160:163], v[8:11]\n\t"
"ds_read_b64_tr_b16 v[192:193], v232 offset:1024\n\t"
"ds_read_b64_tr_b16 v[194:195], v232 offset:1280\n\t"
"v_mfma_f32_16x16x32_bf16 v[8:11], v[204:207], v[168:171], v[8:11]\n\t"
"ds_read_b64_tr_b16 v[196:197], v232 offset:13344\n\t"
"v_mfma_f32_16x16x32_bf16 v[12:15], v[204:207], v[172:175], v[12:15]\n\t"
"ds_read_b64_tr_b16 v[198:199], v232 offset:13600\n\t"
"s_add_u32 s87, s63, 38912\n\t"
"v_mfma_f32_16x16x32_bf16 v[12:15], v[200:203], v[164:167], v[12:15]\n\t"
"buffer_load_dwordx4 v[148:151], v240, s[56:59], s87 offen offset:0\n\t"
"s_waitcnt lgkmcnt(10)\n\t"
"v_mfma_f32_16x16x32_bf16 v[16:19], v[208:211], v[160:163], v[16:19]\n\t"
"ds_read_b64_tr_b16 v[200:201], v232 offset:1032\n\t"
"ds_read_b64_tr_b16 v[202:203], v232 offset:1288\n\t"
"v_mfma_f32_16x16x32_bf16 v[16:19], v[212:215], v[168:171], v[16:19]\n\t"
"ds_read_b64_tr_b16 v[204:205], v232 offset:13352\n\t"
"ds_read_b64_tr_b16 v[206:207], v232 offset:13608\n\t"
"v_mfma_f32_16x16x32_bf16 v[20:23], v[212:215], v[172:175], v[20:23]\n\t"
"s_add_u32 m0, s62, 73920\n\t"
"s_add_u32 s76, s66, 0\n\t"
"v_mfma_f32_16x16x32_bf16 v[20:23], v[208:211], v[164:167], v[20:23]\n\t"
"buffer_load_dwordx4 v245, s[48:51], s76 offen lds\n\t"
"s_waitcnt lgkmcnt(8)\n\t"
"v_mfma_f32_16x16x32_bf16 v[24:27], v[216:219], v[160:163], v[24:27]\n\t"
"ds_read_b64_tr_b16 v[208:209], v232 offset:1536\n\t"
"ds_read_b64_tr_b16 v[210:211], v232 offset:1792\n\t"
"v_mfma_f32_16x16x32_bf16 v[24:27], v[220:223], v[168:171], v[24:27]\n\t"
"ds_read_b64_tr_b16 v[212:213], v232 offset:13856\n\t"
"ds_read_b64_tr_b16 v[214:215], v232 offset:14112\n\t"
"v_mfma_f32_16x16x32_bf16 v[28:31], v[220:223], v[172:175], v[28:31]\n\t"
"s_add_u32 s76, s74, 24576\n\t"
"v_mfma_f32_16x16x32_bf16 v[28:31], v[216:219], v[164:167], v[28:31]\n\t"
"buffer_store_dwordx4 v[234:237], v240, s[56:59], s76 offen offset:0 sc1\n\t"
"s_waitcnt lgkmcnt(8)\n\t"
"v_mfma_f32_16x16x32_bf16 v[32:35], v[192:195], v[160:163], v[32:35]\n\t"
"ds_read_b64_tr_b16 v[216:217], v232 offset:1544\n\t"
"ds_read_b64_tr_b16 v[218:219], v232 offset:1800\n\t"
"v_mfma_f32_16x16x32_bf16 v[32:35], v[196:199], v[168:171], v[32:35]\n\t"
"ds_read_b64_tr_b16 v[220:221], v232 offset:13864\n\t"
"v_mfma_f32_16x16x32_bf16 v[36:39], v[196:199], v[172:175], v[36:39]\n\t"
"ds_read_b64_tr_b16 v[222:223], v232 offset:14120\n\t"
"s_add_u32 s87, s63, 19456\n\t"
"v_mfma_f32_16x16x32_bf16 v[36:39], v[192:195], v[164:167], v[36:39]\n\t"
"buffer_load_dwordx4 v[152:155], v240, s[56:59], s87 offen offset:0\n\t"
"s_waitcnt lgkmcnt(8)\n\t"
"v_mfma_f32_16x16x32_bf16 v[40:43], v[200:203], v[160:163], v[40:43]\n\t"
"ds_read_b64_tr_b16 v[192:193], v232 offset:2048\n\t"
"ds_read_b64_tr_b16 v[194:195], v232 offset:2304\n\t"
"s_add_u32 m0, s62, 74944\n\t"
"v_mfma_f32_16x16x32_bf16 v[40:43], v[204:207], v[168:171], v[40:43]\n\t"
"s_add_u32 s76, s66, 128\n\t"
"buffer_load_dwordx4 v245, s[48:51], s76 offen lds\n\t"
"v_mfma_f32_16x16x32_bf16 v[44:47], v[204:207], v[172:175], v[44:47]\n\t"
"ds_read_b64_tr_b16 v[196:197], v232 offset:14368\n\t"
"ds_read_b64_tr_b16 v[198:199], v232 offset:14624\n\t"
"v_mfma_f32_16x16x32_bf16 v[44:47], v[200:203], v[164:167], v[44:47]\n\t"
"s_add_u32 s87, s63, 39936\n\t"
"buffer_load_dwordx4 v[156:159], v240, s[56:59], s87 offen offset:0\n\t"
"s_waitcnt lgkmcnt(8)\n\t"
"v_mfma_f32_16x16x32_bf16 v[48:51], v[208:211], v[160:163], v[48:51]\n\t"
"ds_read_b64_tr_b16 v[200:201], v232 offset:2056\n\t"
"v_mfma_f32_16x16x32_bf16 v[48:51], v[212:215], v[168:171], v[48:51]\n\t"
"ds_read_b64_tr_b16 v[202:203], v232 offset:2312\n\t"
"v_mfma_f32_16x16x32_bf16 v[52:55], v[212:215], v[172:175], v[52:55]\n\t"
"ds_read_b64_tr_b16 v[204:205], v232 offset:14376\n\t"
"v_mfma_f32_16x16x32_bf16 v[52:55], v[208:211], v[164:167], v[52:55]\n\t"
"ds_read_b64_tr_b16 v[206:207], v232 offset:14632\n\t"
"s_waitcnt lgkmcnt(8)\n\t"
"v_mfma_f32_16x16x32_bf16 v[56:59], v[216:219], v[160:163], v[56:59]\n\t"
"ds_read_b64_tr_b16 v[208:209], v232 offset:2560\n\t"
"ds_read_b64_tr_b16 v[210:211], v232 offset:2816\n\t"
"v_mfma_f32_16x16x32_bf16 v[56:59], v[220:223], v[168:171], v[56:59]\n\t"
"ds_read_b64_tr_b16 v[212:213], v232 offset:14880\n\t"
"ds_read_b64_tr_b16 v[214:215], v232 offset:15136\n\t"
"v_mfma_f32_16x16x32_bf16 v[60:63], v[220:223], v[172:175], v[60:63]\n\t"
"s_add_u32 m0, s62, 75968\n\t"
"s_add_u32 s76, s66, 256\n\t"
"v_mfma_f32_16x16x32_bf16 v[60:63], v[216:219], v[164:167], v[60:63]\n\t"
"buffer_load_dwordx4 v245, s[48:51], s76 offen lds\n\t"
"s_waitcnt lgkmcnt(8)\n\t"
"v_mfma_f32_16x16x32_bf16 v[64:67], v[192:195], v[160:163], v[64:67]\n\t"
"ds_read_b64_tr_b16 v[216:217], v232 offset:2568\n\t"
"v_mfma_f32_16x16x32_bf16 v[64:67], v[196:199], v[168:171], v[64:67]\n\t"
"ds_read_b64_tr_b16 v[218:219], v232 offset:2824\n\t"
"v_mfma_f32_16x16x32_bf16 v[68:71], v[196:199], v[172:175], v[68:71]\n\t"
"ds_read_b64_tr_b16 v[220:221], v232 offset:14888\n\t"
"v_mfma_f32_16x16x32_bf16 v[68:71], v[192:195], v[164:167], v[68:71]\n\t"
"ds_read_b64_tr_b16 v[222:223], v232 offset:15144\n\t"
"s_barrier\n\t"
"v_add_u32 v232, 0, v247\n\t"
"s_waitcnt lgkmcnt(8)\n\t"
"v_mfma_f32_16x16x32_bf16 v[72:75], v[200:203], v[160:163], v[72:75]\n\t"
"ds_read_b128 v[192:195], v232 offset:0\n\t"
"v_mfma_f32_16x16x32_bf16 v[72:75], v[204:207], v[168:171], v[72:75]\n\t"
"v_mfma_f32_16x16x32_bf16 v[76:79], v[204:207], v[172:175], v[76:79]\n\t"
"ds_read_b128 v[196:199], v232 offset:6144\n\t"
"v_mfma_f32_16x16x32_bf16 v[76:79], v[200:203], v[164:167], v[76:79]\n\t"
"s_waitcnt lgkmcnt(6)\n\t"
"v_mfma_f32_16x16x32_bf16 v[80:83], v[208:211], v[160:163], v[80:83]\n\t"
"ds_read_b128 v[200:203], v232 offset:1024\n\t"
"v_mfma_f32_16x16x32_bf16 v[80:83], v[212:215], v[168:171], v[80:83]\n\t"
"v_mfma_f32_16x16x32_bf16 v[84:87], v[212:215], v[172:175], v[84:87]\n\t"
"ds_read_b128 v[204:207], v232 offset:7168\n\t"
"v_mfma_f32_16x16x32_bf16 v[84:87], v[208:211], v[164:167], v[84:87]\n\t"
"s_waitcnt lgkmcnt(4)\n\t"
"v_mfma_f32_16x16x32_bf16 v[88:91], v[216:219], v[160:163], v[88:91]\n\t"
"ds_read_b128 v[208:211], v232 offset:2048\n\t"
"v_mfma_f32_16x16x32_bf16 v[88:91], v[220:223], v[168:171], v[88:91]\n\t"
"v_mfma_f32_16x16x32_bf16 v[92:95], v[220:223], v[172:175], v[92:95]\n\t"
"ds_read_b128 v[212:215], v232 offset:8192\n\t"
"v_mfma_f32_16x16x32_bf16 v[92:95], v[216:219], v[164:167], v[92:95]\n\t"
"s_waitcnt vmcnt(12) lgkmcnt(4)\n\t"
"v_mfma_f32_16x16x32_bf16 v[96:99], v[192:195], v[176:179], v[96:99]\n\t"
"ds_read_b128 v[216:219], v232 offset:3072\n\t"
"v_mfma_f32_16x16x32_bf16 v[96:99], v[196:199], v[184:187], v[96:99]\n\t"
"v_mfma_f32_16x16x32_bf16 v[104:107], v[196:199], v[188:191], v[104:107]\n\t"
"ds_read_b128 v[220:223], v232 offset:9216\n\t"
"v_mfma_f32_16x16x32_bf16 v[104:107], v[192:195], v[180:183], v[104:107]\n\t"
"s_waitcnt lgkmcnt(4)\n\t"
"v_mfma_f32_16x16x32_bf16 v[100:103], v[200:203], v[176:179], v[100:103]\n\t"
"ds_read_b128 v[192:195], v232 offset:4096\n\t"
"v_mfma_f32_16x16x32_bf16 v[100:103], v[204:207], v[184:187], v[100:103]\n\t"
"v_lshrrev_b32 v247, 1, v240\n\t"
"v_mfma_f32_16x16x32_bf16 v[108:111], v[204:207], v[188:191], v[108:111]\n\t"
"ds_read_b128 v[196:199], v232 offset:10240\n\t"
"v_mfma_f32_16x16x32_bf16 v[108:111], v[200:203], v[180:183], v[108:111]\n\t"
"s_waitcnt lgkmcnt(4)\n\t"
"v_mfma_f32_16x16x32_bf16 v[112:115], v[208:211], v[176:179], v[112:115]\n\t"
"ds_read_b128 v[200:203], v232 offset:5120\n\t"
"ds_read_b128 v[204:207], v232 offset:11264\n\t"
"v_mfma_f32_16x16x32_bf16 v[112:115], v[212:215], v[184:187], v[112:115]\n\t"
"s_add_u32 m0, s62, 98560\n\t"
"v_mfma_f32_16x16x32_bf16 v[120:123], v[212:215], v[188:191], v[120:123]\n\t"
"s_add_u32 s76, s66, 0\n\t"
"v_mfma_f32_16x16x32_bf16 v[120:123], v[208:211], v[180:183], v[120:123]\n\t"
"buffer_load_dwordx4 v246, s[52:55], s76 offen lds\n\t"
"s_waitcnt lgkmcnt(4)\n\t"
"v_mfma_f32_16x16x32_bf16 v[116:119], v[216:219], v[176:179], v[116:119]\n\t"
"ds_read_b128 v[208:211], v232 offset:12288\n\t"
"ds_read_b128 v[212:215], v232 offset:18432\n\t"
"v_mfma_f32_16x16x32_bf16 v[116:119], v[220:223], v[184:187], v[116:119]\n\t"
"s_add_u32 m0, s73, 147840\n\t"
"v_mfma_f32_16x16x32_bf16 v[124:127], v[220:223], v[188:191], v[124:127]\n\t"
"s_add_u32 s76, s72, 1024\n\t"
"v_mfma_f32_16x16x32_bf16 v[124:127], v[216:219], v[180:183], v[124:127]\n\t"
"buffer_load_dwordx4 v242, s[92:95], s76 offen nt lds\n\t"
"s_waitcnt lgkmcnt(4)\n\t"
"v_mfma_f32_16x16x32_bf16 v[128:131], v[192:195], v[176:179], v[128:131]\n\t"
"ds_read_b128 v[216:219], v232 offset:13312\n\t"
"v_mfma_f32_16x16x32_bf16 v[128:131], v[196:199], v[184:187], v[128:131]\n\t"
"v_mfma_f32_16x16x32_bf16 v[136:139], v[196:199], v[188:191], v[136:139]\n\t"
"ds_read_b128 v[220:223], v232 offset:19456\n\t"
"v_mfma_f32_16x16x32_bf16 v[136:139], v[192:195], v[180:183], v[136:139]\n\t"
"s_waitcnt lgkmcnt(4)\n\t"
"v_mfma_f32_16x16x32_bf16 v[132:135], v[200:203], v[176:179], v[132:135]\n\t"
"ds_read_b128 v[192:195], v232 offset:14336\n\t"
"s_add_u32 m0, s62, 99584\n\t"
"v_mfma_f32_16x16x32_bf16 v[132:135], v[204:207], v[184:187], v[132:135]\n\t"
"s_add_u32 s76, s66, 128\n\t"
"v_mfma_f32_16x16x32_bf16 v[140:143], v[204:207], v[188:191], v[140:143]\n\t"
"buffer_load_dwordx4 v246, s[52:55], s76 offen lds\n\t"
"v_mfma_f32_16x16x32_bf16 v[140:143], v[200:203], v[180:183], v[140:143]\n\t"
"ds_read_b128 v[196:199], v232 offset:20480\n\t"
"s_waitcnt vmcnt(4) lgkmcnt(4)\n\t"
"v_mfma_f32_16x16x32_bf16 v[96:99], v[208:211], v[144:147], v[96:99]\n\t"
"ds_read_b128 v[200:203], v232 offset:15360\n\t"
"v_mfma_f32_16x16x32_bf16 v[96:99], v[212:215], v[152:155], v[96:99]\n\t"
"v_mfma_f32_16x16x32_bf16 v[104:107], v[212:215], v[156:159], v[104:107]\n\t"
"ds_read_b128 v[204:207], v232 offset:21504\n\t"
"v_mfma_f32_16x16x32_bf16 v[104:107], v[208:211], v[148:151], v[104:107]\n\t"
"s_waitcnt lgkmcnt(4)\n\t"
"v_mfma_f32_16x16x32_bf16 v[100:103], v[216:219], v[144:147], v[100:103]\n\t"
"ds_read_b128 v[208:211], v232 offset:16384\n\t"
"ds_read_b128 v[212:215], v232 offset:22528\n\t"
"v_mfma_f32_16x16x32_bf16 v[100:103], v[220:223], v[152:155], v[100:103]\n\t"
"s_add_u32 m0, s62, 100608\n\t"
"v_mfma_f32_16x16x32_bf16 v[108:111], v[220:223], v[156:159], v[108:111]\n\t"
"s_add_u32 s76, s66, 256\n\t"
"v_mfma_f32_16x16x32_bf16 v[108:111], v[216:219], v[148:151], v[108:111]\n\t"
"buffer_load_dwordx4 v246, s[52:55], s76 offen lds\n\t"
"s_waitcnt lgkmcnt(4)\n\t"
"v_mfma_f32_16x16x32_bf16 v[112:115], v[192:195], v[144:147], v[112:115]\n\t"
"ds_read_b128 v[216:219], v232 offset:17408\n\t"
"v_mfma_f32_16x16x32_bf16 v[112:115], v[196:199], v[152:155], v[112:115]\n\t"
"v_mfma_f32_16x16x32_bf16 v[120:123], v[196:199], v[156:159], v[120:123]\n\t"
"ds_read_b128 v[220:223], v232 offset:23552\n\t"
"v_mfma_f32_16x16x32_bf16 v[120:123], v[192:195], v[148:151], v[120:123]\n\t"
"s_barrier\n\t"
"v_add_u32 v232, 24640, v248\n\t"
"s_waitcnt lgkmcnt(4)\n\t"
"v_mfma_f32_16x16x32_bf16 v[116:119], v[200:203], v[144:147], v[116:119]\n\t"
"ds_read_b64_tr_b16 v[192:193], v232 offset:0\n\t"
"v_mfma_f32_16x16x32_bf16 v[116:119], v[204:207], v[152:155], v[116:119]\n\t"
"ds_read_b64_tr_b16 v[194:195], v232 offset:256\n\t"
"v_mfma_f32_16x16x32_bf16 v[124:127], v[204:207], v[156:159], v[124:127]\n\t"
"ds_read_b64_tr_b16 v[196:197], v232 offset:12320\n\t"
"v_mfma_f32_16x16x32_bf16 v[124:127], v[200:203], v[148:151], v[124:127]\n\t"
"ds_read_b64_tr_b16 v[198:199], v232 offset:12576\n\t"
"s_waitcnt lgkmcnt(6)\n\t"
"v_mfma_f32_16x16x32_bf16 v[128:131], v[208:211], v[144:147], v[128:131]\n\t"
"ds_read_b64_tr_b16 v[200:201], v232 offset:8\n\t"
"v_mfma_f32_16x16x32_bf16 v[128:131], v[212:215], v[152:155], v[128:131]\n\t"
"ds_read_b64_tr_b16 v[202:203], v232 offset:264\n\t"
"v_mfma_f32_16x16x32_bf16 v[136:139], v[212:215], v[156:159], v[136:139]\n\t"
"ds_read_b64_tr_b16 v[204:205], v232 offset:12328\n\t"
"v_mfma_f32_16x16x32_bf16 v[136:139], v[208:211], v[148:151], v[136:139]\n\t"
"ds_read_b64_tr_b16 v[206:207], v232 offset:12584\n\t"
"s_waitcnt lgkmcnt(8)\n\t"
"v_mfma_f32_16x16x32_bf16 v[132:135], v[216:219], v[144:147], v[132:135]\n\t"
"ds_read_b64_tr_b16 v[208:209], v232 offset:512\n\t"
"v_mfma_f32_16x16x32_bf16 v[132:135], v[220:223], v[152:155], v[132:135]\n\t"
"ds_read_b64_tr_b16 v[210:211], v232 offset:768\n\t"
"v_mfma_f32_16x16x32_bf16 v[140:143], v[220:223], v[156:159], v[140:143]\n\t"
"ds_read_b64_tr_b16 v[212:213], v232 offset:12832\n\t"
"v_mfma_f32_16x16x32_bf16 v[140:143], v[216:219], v[148:151], v[140:143]\n\t"
"ds_read_b64_tr_b16 v[214:215], v232 offset:13088\n\t"
"s_waitcnt lgkmcnt(8)\n\t"
"v_mfma_f32_16x16x32_bf16 v[0:3], v[192:195], v[176:179], v[0:3]\n\t"
"ds_read_b64_tr_b16 v[216:217], v232 offset:520\n\t"
"v_mfma_f32_16x16x32_bf16 v[0:3], v[196:199], v[184:187], v[0:3]\n\t"
"ds_read_b64_tr_b16 v[218:219], v232 offset:776\n\t"
"v_mfma_f32_16x16x32_bf16 v[4:7], v[196:199], v[188:191], v[4:7]\n\t"
"ds_read_b64_tr_b16 v[220:221], v232 offset:12840\n\t"
"v_mfma_f32_16x16x32_bf16 v[4:7], v[192:195], v[180:183], v[4:7]\n\t"
"ds_read_b64_tr_b16 v[222:223], v232 offset:13096\n\t"
"s_waitcnt lgkmcnt(8)\n\t"
"v_mfma_f32_16x16x32_bf16 v[8:11], v[200:203], v[176:179], v[8:11]\n\t"
"ds_read_b64_tr_b16 v[192:193], v232 offset:1024\n\t"
"ds_read_b64_tr_b16 v[194:195], v232 offset:1280\n\t"
"v_mul_f32 v224, v96, v96\n\t"
"v_mul_f32 v225, v97, v97\n\t"
"v_mul_f32 v226, v98, v98\n\t"
"v_mul_f32 v227, v99, v99\n\t"
"v_mfma_f32_16x16x32_bf16 v[8:11], v[204:207], v[184:187], v[8:11]\n\t"
"v_fmaak_f32 v224, v224, v253, 0xc1b0e49c\n\t"
"v_fmaak_f32 v225, v225, v253, 0xc1b0e49c\n\t"
"v_fmaak_f32 v226, v226, v253, 0xc1b0e49c\n\t"
"v_fmaak_f32 v227, v227, v253, 0xc1b0e49c\n\t"
"v_mul_f32 v224, v96, v224\n\t"
"v_mul_f32 v225, v97, v225\n\t"
"v_mfma_f32_16x16x32_bf16 v[12:15], v[204:207], v[188:191], v[12:15]\n\t"
"v_mul_f32 v226, v98, v226\n\t"
"v_mul_f32 v227, v99, v227\n\t"
"v_exp_f32 v224, v224\n\t"
"v_exp_f32 v225, v225\n\t"
"ds_read_b64_tr_b16 v[196:197], v232 offset:13344\n\t"
"ds_read_b64_tr_b16 v[198:199], v232 offset:13600\n\t"
"v_mfma_f32_16x16x32_bf16 v[12:15], v[200:203], v[180:183], v[12:15]\n\t"
"v_exp_f32 v226, v226\n\t"
"v_exp_f32 v227, v227\n\t"
"v_mul_f32 v228, v96, v100\n\t"
"v_mul_f32 v229, v97, v101\n\t"
"v_mul_f32 v230, v98, v102\n\t"
"s_waitcnt lgkmcnt(8)\n\t"
"v_mfma_f32_16x16x32_bf16 v[16:19], v[208:211], v[176:179], v[16:19]\n\t"
"ds_read_b64_tr_b16 v[200:201], v232 offset:1032\n\t"
"ds_read_b64_tr_b16 v[202:203], v232 offset:1288\n\t"
"v_mul_f32 v231, v99, v103\n\t"
"v_fma_f32 v224, v224, s86, s86\n\t"
"v_mfma_f32_16x16x32_bf16 v[16:19], v[212:215], v[184:187], v[16:19]\n\t"
"v_fma_f32 v225, v225, s86, s86\n\t"
"v_fma_f32 v226, v226, s86, s86\n\t"
"ds_read_b64_tr_b16 v[204:205], v232 offset:13352\n\t"
"ds_read_b64_tr_b16 v[206:207], v232 offset:13608\n\t"
"v_mfma_f32_16x16x32_bf16 v[20:23], v[212:215], v[188:191], v[20:23]\n\t"
"s_add_u32 m0, s62, 123200\n\t"
"s_add_u32 s76, s66, 24576\n\t"
"buffer_load_dwordx4 v245, s[48:51], s76 offen lds\n\t"
"v_fma_f32 v227, v227, s86, s86\n\t"
"v_mfma_f32_16x16x32_bf16 v[20:23], v[208:211], v[180:183], v[20:23]\n\t"
"v_rcp_f32 v224, v224\n\t"
"v_rcp_f32 v225, v225\n\t"
"v_rcp_f32 v226, v226\n\t"
"v_rcp_f32 v227, v227\n\t"
"s_waitcnt lgkmcnt(8)\n\t"
"v_mfma_f32_16x16x32_bf16 v[24:27], v[216:219], v[176:179], v[24:27]\n\t"
"ds_read_b64_tr_b16 v[208:209], v232 offset:1536\n\t"
"ds_read_b64_tr_b16 v[210:211], v232 offset:1792\n\t"
"v_mul_f32 v228, v228, v224\n\t"
"v_mul_f32 v229, v229, v225\n\t"
"v_mfma_f32_16x16x32_bf16 v[24:27], v[220:223], v[184:187], v[24:27]\n\t"
"v_mul_f32 v230, v230, v226\n\t"
"v_mul_f32 v231, v231, v227\n\t"
"ds_read_b64_tr_b16 v[212:213], v232 offset:13856\n\t"
"v_mfma_f32_16x16x32_bf16 v[28:31], v[220:223], v[188:191], v[28:31]\n\t"
"ds_read_b64_tr_b16 v[214:215], v232 offset:14112\n\t"
"v_cvt_pk_bf16_f32 v238, v228, v229\n\t"
"v_cvt_pk_bf16_f32 v239, v230, v231\n\t"
"s_add_u32 s87, s68, 0\n\t"
"v_mfma_f32_16x16x32_bf16 v[28:31], v[216:219], v[180:183], v[28:31]\n\t"
"buffer_store_dwordx2 v[238:239], v247, s[56:59], s87 offen offset:0 sc1\n\t"
"v_mul_f32 v224, v104, v104\n\t"
"v_mul_f32 v225, v105, v105\n\t"
"s_waitcnt lgkmcnt(8)\n\t"
"v_mfma_f32_16x16x32_bf16 v[32:35], v[192:195], v[176:179], v[32:35]\n\t"
"ds_read_b64_tr_b16 v[216:217], v232 offset:1544\n\t"
"ds_read_b64_tr_b16 v[218:219], v232 offset:1800\n\t"
"v_mul_f32 v226, v106, v106\n\t"
"v_mul_f32 v227, v107, v107\n\t"
"v_mfma_f32_16x16x32_bf16 v[32:35], v[196:199], v[184:187], v[32:35]\n\t"
"v_fmaak_f32 v224, v224, v253, 0xc1b0e49c\n\t"
"v_fmaak_f32 v225, v225, v253, 0xc1b0e49c\n\t"
"v_fmaak_f32 v226, v226, v253, 0xc1b0e49c\n\t"
"v_mfma_f32_16x16x32_bf16 v[36:39], v[196:199], v[188:191], v[36:39]\n\t"
"ds_read_b64_tr_b16 v[220:221], v232 offset:13864\n\t"
"ds_read_b64_tr_b16 v[222:223], v232 offset:14120\n\t"
"v_fmaak_f32 v227, v227, v253, 0xc1b0e49c\n\t"
"v_mfma_f32_16x16x32_bf16 v[36:39], v[192:195], v[180:183], v[36:39]\n\t"
"v_mul_f32 v224, v104, v224\n\t"
"v_mul_f32 v225, v105, v225\n\t"
"v_mul_f32 v226, v106, v226\n\t"
"s_waitcnt lgkmcnt(8)\n\t"
"v_mfma_f32_16x16x32_bf16 v[40:43], v[200:203], v[176:179], v[40:43]\n\t"
"ds_read_b64_tr_b16 v[192:193], v232 offset:2048\n\t"
"ds_read_b64_tr_b16 v[194:195], v232 offset:2304\n\t"
"s_add_u32 m0, s62, 124224\n\t"
"s_add_u32 s76, s66, 24704\n\t"
"v_mfma_f32_16x16x32_bf16 v[40:43], v[204:207], v[184:187], v[40:43]\n\t"
"buffer_load_dwordx4 v245, s[48:51], s76 offen lds\n\t"
"v_mul_f32 v227, v107, v227\n\t"
"v_exp_f32 v224, v224\n\t"
"v_exp_f32 v225, v225\n\t"
"v_mfma_f32_16x16x32_bf16 v[44:47], v[204:207], v[188:191], v[44:47]\n\t"
"v_exp_f32 v226, v226\n\t"
"v_exp_f32 v227, v227\n\t"
"ds_read_b64_tr_b16 v[196:197], v232 offset:14368\n\t"
"ds_read_b64_tr_b16 v[198:199], v232 offset:14624\n\t"
"v_mfma_f32_16x16x32_bf16 v[44:47], v[200:203], v[180:183], v[44:47]\n\t"
"v_mul_f32 v228, v104, v108\n\t"
"v_mul_f32 v229, v105, v109\n\t"
"v_mul_f32 v230, v106, v110\n\t"
"v_mul_f32 v231, v107, v111\n\t"
"s_waitcnt lgkmcnt(8)\n\t"
"v_mfma_f32_16x16x32_bf16 v[48:51], v[208:211], v[176:179], v[48:51]\n\t"
"ds_read_b64_tr_b16 v[200:201], v232 offset:2056\n\t"
"ds_read_b64_tr_b16 v[202:203], v232 offset:2312\n\t"
"v_fma_f32 v224, v224, s86, s86\n\t"
"v_fma_f32 v225, v225, s86, s86\n\t"
"v_mfma_f32_16x16x32_bf16 v[48:51], v[212:215], v[184:187], v[48:51]\n\t"
"v_fma_f32 v226, v226, s86, s86\n\t"
"v_fma_f32 v227, v227, s86, s86\n\t"
"v_rcp_f32 v224, v224\n\t"
"v_mfma_f32_16x16x32_bf16 v[52:55], v[212:215], v[188:191], v[52:55]\n\t"
"ds_read_b64_tr_b16 v[204:205], v232 offset:14376\n\t"
"ds_read_b64_tr_b16 v[206:207], v232 offset:14632\n\t"
"v_rcp_f32 v225, v225\n\t"
"v_mfma_f32_16x16x32_bf16 v[52:55], v[208:211], v[180:183], v[52:55]\n\t"
"v_rcp_f32 v226, v226\n\t"
"v_rcp_f32 v227, v227\n\t"
"v_mul_f32 v228, v228, v224\n\t"
"s_waitcnt lgkmcnt(8)\n\t"
"v_mfma_f32_16x16x32_bf16 v[56:59], v[216:219], v[176:179], v[56:59]\n\t"
"ds_read_b64_tr_b16 v[208:209], v232 offset:2560\n\t"
"ds_read_b64_tr_b16 v[210:211], v232 offset:2816\n\t"
"v_mul_f32 v229, v229, v225\n\t"
"v_mul_f32 v230, v230, v226\n\t"
"v_mul_f32 v231, v231, v227\n\t"
"v_mfma_f32_16x16x32_bf16 v[56:59], v[220:223], v[184:187], v[56:59]\n\t"
"v_cvt_pk_bf16_f32 v238, v228, v229\n\t"
"v_cvt_pk_bf16_f32 v239, v230, v231\n\t"
"ds_read_b64_tr_b16 v[212:213], v232 offset:14880\n\t"
"ds_read_b64_tr_b16 v[214:215], v232 offset:15136\n\t"
"v_mfma_f32_16x16x32_bf16 v[60:63], v[220:223], v[188:191], v[60:63]\n\t"
"s_add_u32 m0, s62, 125248\n\t"
"s_add_u32 s76, s66, 24832\n\t"
"buffer_load_dwordx4 v245, s[48:51], s76 offen lds\n\t"
"s_add_u32 s87, s68, 245760\n\t"
"buffer_store_dwordx2 v[238:239], v247, s[56:59], s87 offen offset:0 sc1\n\t"
"v_mfma_f32_16x16x32_bf16 v[60:63], v[216:219], v[180:183], v[60:63]\n\t"
"v_mul_f32 v224, v112, v112\n\t"
"v_mul_f32 v225, v113, v113\n\t"
"v_mul_f32 v226, v114, v114\n\t"
"v_mul_f32 v227, v115, v115\n\t"
"s_waitcnt lgkmcnt(8)\n\t"
"v_mfma_f32_16x16x32_bf16 v[64:67], v[192:195], v[176:179], v[64:67]\n\t"
"ds_read_b64_tr_b16 v[216:217], v232 offset:2568\n\t"
"ds_read_b64_tr_b16 v[218:219], v232 offset:2824\n\t"
"v_fmaak_f32 v224, v224, v253, 0xc1b0e49c\n\t"
"v_fmaak_f32 v225, v225, v253, 0xc1b0e49c\n\t"
"v_mfma_f32_16x16x32_bf16 v[64:67], v[196:199], v[184:187], v[64:67]\n\t"
"v_fmaak_f32 v226, v226, v253, 0xc1b0e49c\n\t"
"v_fmaak_f32 v227, v227, v253, 0xc1b0e49c\n\t"
"ds_read_b64_tr_b16 v[220:221], v232 offset:14888\n\t"
"v_mfma_f32_16x16x32_bf16 v[68:71], v[196:199], v[188:191], v[68:71]\n\t"
"ds_read_b64_tr_b16 v[222:223], v232 offset:15144\n\t"
"v_mul_f32 v224, v112, v224\n\t"
"v_mul_f32 v225, v113, v225\n\t"
"v_mfma_f32_16x16x32_bf16 v[68:71], v[192:195], v[180:183], v[68:71]\n\t"
"v_mul_f32 v226, v114, v226\n\t"
"v_mul_f32 v227, v115, v227\n\t"
"v_exp_f32 v224, v224\n\t"
"s_waitcnt vmcnt(7)\n\t"
"s_barrier\n\t"
"v_add_u32 v232, 49280, v248\n\t"
"s_waitcnt lgkmcnt(8)\n\t"
"v_mfma_f32_16x16x32_bf16 v[72:75], v[200:203], v[176:179], v[72:75]\n\t"
"ds_read_b64_tr_b16 v[192:193], v232 offset:0\n\t"
"ds_read_b64_tr_b16 v[194:195], v232 offset:256\n\t"
"v_exp_f32 v225, v225\n\t"
"v_exp_f32 v226, v226\n\t"
"v_mfma_f32_16x16x32_bf16 v[72:75], v[204:207], v[184:187], v[72:75]\n\t"
"v_exp_f32 v227, v227\n\t"
"v_mul_f32 v228, v112, v116\n\t"
"ds_read_b64_tr_b16 v[196:197], v232 offset:12320\n\t"
"v_mfma_f32_16x16x32_bf16 v[76:79], v[204:207], v[188:191], v[76:79]\n\t"
"ds_read_b64_tr_b16 v[198:199], v232 offset:12576\n\t"
"v_mul_f32 v229, v113, v117\n\t"
"v_mul_f32 v230, v114, v118\n\t"
"v_mfma_f32_16x16x32_bf16 v[76:79], v[200:203], v[180:183], v[76:79]\n\t"
"v_mul_f32 v231, v115, v119\n\t"
"v_fma_f32 v224, v224, s86, s86\n\t"
"v_fma_f32 v225, v225, s86, s86\n\t"
"s_waitcnt lgkmcnt(8)\n\t"
"v_mfma_f32_16x16x32_bf16 v[80:83], v[208:211], v[176:179], v[80:83]\n\t"
"ds_read_b64_tr_b16 v[200:201], v232 offset:8\n\t"
"ds_read_b64_tr_b16 v[202:203], v232 offset:264\n\t"
"v_fma_f32 v226, v226, s86, s86\n\t"
"v_fma_f32 v227, v227, s86, s86\n\t"
"v_mfma_f32_16x16x32_bf16 v[80:83], v[212:215], v[184:187], v[80:83]\n\t"
"v_rcp_f32 v224, v224\n\t"
"v_rcp_f32 v225, v225\n\t"
"ds_read_b64_tr_b16 v[204:205], v232 offset:12328\n\t"
"v_mfma_f32_16x16x32_bf16 v[84:87], v[212:215], v[188:191], v[84:87]\n\t"
"ds_read_b64_tr_b16 v[206:207], v232 offset:12584\n\t"
"v_rcp_f32 v226, v226\n\t"
"v_rcp_f32 v227, v227\n\t"
"v_mfma_f32_16x16x32_bf16 v[84:87], v[208:211], v[180:183], v[84:87]\n\t"
"v_mul_f32 v228, v228, v224\n\t"
"v_mul_f32 v229, v229, v225\n\t"
"v_mul_f32 v230, v230, v226\n\t"
"s_waitcnt lgkmcnt(8)\n\t"
"v_mfma_f32_16x16x32_bf16 v[88:91], v[216:219], v[176:179], v[88:91]\n\t"
"ds_read_b64_tr_b16 v[208:209], v232 offset:512\n\t"
"ds_read_b64_tr_b16 v[210:211], v232 offset:768\n\t"
"v_mul_f32 v231, v231, v227\n\t"
"v_cvt_pk_bf16_f32 v238, v228, v229\n\t"
"v_mfma_f32_16x16x32_bf16 v[88:91], v[220:223], v[184:187], v[88:91]\n\t"
"v_cvt_pk_bf16_f32 v239, v230, v231\n\t"
"s_add_u32 s87, s69, 0\n\t"
"buffer_store_dwordx2 v[238:239], v247, s[56:59], s87 offen offset:0 sc1\n\t"
"v_mfma_f32_16x16x32_bf16 v[92:95], v[220:223], v[188:191], v[92:95]\n\t"
"ds_read_b64_tr_b16 v[212:213], v232 offset:12832\n\t"
"ds_read_b64_tr_b16 v[214:215], v232 offset:13088\n\t"
"v_mul_f32 v224, v120, v120\n\t"
"v_mul_f32 v225, v121, v121\n\t"
"v_mfma_f32_16x16x32_bf16 v[92:95], v[216:219], v[180:183], v[92:95]\n\t"
"v_mul_f32 v226, v122, v122\n\t"
"v_mul_f32 v227, v123, v123\n\t"
"v_fmaak_f32 v224, v224, v253, 0xc1b0e49c\n\t"
"s_waitcnt lgkmcnt(8)\n\t"
"v_mfma_f32_16x16x32_bf16 v[0:3], v[192:195], v[144:147], v[0:3]\n\t"
"ds_read_b64_tr_b16 v[216:217], v232 offset:520\n\t"
"ds_read_b64_tr_b16 v[218:219], v232 offset:776\n\t"
"ds_read_b64_tr_b16 v[234:235], v243 offset:0\n\t"
"ds_read_b64_tr_b16 v[236:237], v243 offset:4096\n\t"
"v_mfma_f32_16x16x32_bf16 v[0:3], v[196:199], v[152:155], v[0:3]\n\t"
"v_fmaak_f32 v225, v225, v253, 0xc1b0e49c\n\t"
"v_fmaak_f32 v226, v226, v253, 0xc1b0e49c\n\t"
"v_fmaak_f32 v227, v227, v253, 0xc1b0e49c\n\t"
"v_mul_f32 v224, v120, v224\n\t"
"v_mfma_f32_16x16x32_bf16 v[4:7], v[196:199], v[156:159], v[4:7]\n\t"
"v_mul_f32 v225, v121, v225\n\t"
"ds_read_b64_tr_b16 v[220:221], v232 offset:12840\n\t"
"ds_read_b64_tr_b16 v[222:223], v232 offset:13096\n\t"
"v_mul_f32 v226, v122, v226\n\t"
"v_mfma_f32_16x16x32_bf16 v[4:7], v[192:195], v[148:151], v[4:7]\n\t"
"v_mul_f32 v227, v123, v227\n\t"
"v_exp_f32 v224, v224\n\t"
"v_exp_f32 v225, v225\n\t"
"s_waitcnt lgkmcnt(10)\n\t"
"v_mfma_f32_16x16x32_bf16 v[8:11], v[200:203], v[144:147], v[8:11]\n\t"
"ds_read_b64_tr_b16 v[192:193], v232 offset:1024\n\t"
"ds_read_b64_tr_b16 v[194:195], v232 offset:1280\n\t"
"v_exp_f32 v226, v226\n\t"
"v_exp_f32 v227, v227\n\t"
"v_mfma_f32_16x16x32_bf16 v[8:11], v[204:207], v[152:155], v[8:11]\n\t"
"v_mul_f32 v228, v120, v124\n\t"
"v_mul_f32 v229, v121, v125\n\t"
"v_mul_f32 v230, v122, v126\n\t"
"v_mfma_f32_16x16x32_bf16 v[12:15], v[204:207], v[156:159], v[12:15]\n\t"
"ds_read_b64_tr_b16 v[196:197], v232 offset:13344\n\t"
"ds_read_b64_tr_b16 v[198:199], v232 offset:13600\n\t"
"v_mul_f32 v231, v123, v127\n\t"
"v_mfma_f32_16x16x32_bf16 v[12:15], v[200:203], v[148:151], v[12:15]\n\t"
"v_fma_f32 v224, v224, s86, s86\n\t"
"v_fma_f32 v225, v225, s86, s86\n\t"
"v_fma_f32 v226, v226, s86, s86\n\t"
"s_waitcnt lgkmcnt(10)\n\t"
"v_mfma_f32_16x16x32_bf16 v[16:19], v[208:211], v[144:147], v[16:19]\n\t"
"ds_read_b64_tr_b16 v[200:201], v232 offset:1032\n\t"
"ds_read_b64_tr_b16 v[202:203], v232 offset:1288\n\t"
"v_fma_f32 v227, v227, s86, s86\n\t"
"v_rcp_f32 v224, v224\n\t"
"v_mfma_f32_16x16x32_bf16 v[16:19], v[212:215], v[152:155], v[16:19]\n\t"
"v_rcp_f32 v225, v225\n\t"
"v_rcp_f32 v226, v226\n\t"
"v_rcp_f32 v227, v227\n\t"
"ds_read_b64_tr_b16 v[204:205], v232 offset:13352\n\t"
"v_mfma_f32_16x16x32_bf16 v[20:23], v[212:215], v[156:159], v[20:23]\n\t"
"ds_read_b64_tr_b16 v[206:207], v232 offset:13608\n\t"
"s_add_u32 m0, s62, 0\n\t"
"s_add_u32 s76, s66, 24576\n\t"
"buffer_load_dwordx4 v246, s[52:55], s76 offen lds\n\t"
"v_mfma_f32_16x16x32_bf16 v[20:23], v[208:211], v[148:151], v[20:23]\n\t"
"v_mul_f32 v228, v228, v224\n\t"
"v_mul_f32 v229, v229, v225\n\t"
"v_mul_f32 v230, v230, v226\n\t"
"v_mul_f32 v231, v231, v227\n\t"
"s_waitcnt lgkmcnt(8)\n\t"
"v_mfma_f32_16x16x32_bf16 v[24:27], v[216:219], v[144:147], v[24:27]\n\t"
"ds_read_b64_tr_b16 v[208:209], v232 offset:1536\n\t"
"ds_read_b64_tr_b16 v[210:211], v232 offset:1792\n\t"
"v_cvt_pk_bf16_f32 v238, v228, v229\n\t"
"v_cvt_pk_bf16_f32 v239, v230, v231\n\t"
"s_add_u32 s87, s69, 245760\n\t"
"v_mfma_f32_16x16x32_bf16 v[24:27], v[220:223], v[152:155], v[24:27]\n\t"
"buffer_store_dwordx2 v[238:239], v247, s[56:59], s87 offen offset:0 sc1\n\t"
"v_mul_f32 v224, v128, v128\n\t"
"v_mul_f32 v225, v129, v129\n\t"
"ds_read_b64_tr_b16 v[212:213], v232 offset:13856\n\t"
"v_mfma_f32_16x16x32_bf16 v[28:31], v[220:223], v[156:159], v[28:31]\n\t"
"ds_read_b64_tr_b16 v[214:215], v232 offset:14112\n\t"
"s_add_u32 s76, s74, 32768\n\t"
"buffer_store_dwordx4 v[234:237], v240, s[56:59], s76 offen offset:0 sc1\n\t"
"v_mul_f32 v226, v130, v130\n\t"
"v_mfma_f32_16x16x32_bf16 v[28:31], v[216:219], v[148:151], v[28:31]\n\t"
"v_mul_f32 v227, v131, v131\n\t"
"v_fmaak_f32 v224, v224, v253, 0xc1b0e49c\n\t"
"v_fmaak_f32 v225, v225, v253, 0xc1b0e49c\n\t"
"v_fmaak_f32 v226, v226, v253, 0xc1b0e49c\n\t"
"s_waitcnt lgkmcnt(8)\n\t"
"v_mfma_f32_16x16x32_bf16 v[32:35], v[192:195], v[144:147], v[32:35]\n\t"
"ds_read_b64_tr_b16 v[216:217], v232 offset:1544\n\t"
"ds_read_b64_tr_b16 v[218:219], v232 offset:1800\n\t"
"v_fmaak_f32 v227, v227, v253, 0xc1b0e49c\n\t"
"v_mul_f32 v224, v128, v224\n\t"
"v_mfma_f32_16x16x32_bf16 v[32:35], v[196:199], v[152:155], v[32:35]\n\t"
"v_mul_f32 v225, v129, v225\n\t"
"v_mul_f32 v226, v130, v226\n\t"
"ds_read_b64_tr_b16 v[220:221], v232 offset:13864\n\t"
"v_mfma_f32_16x16x32_bf16 v[36:39], v[196:199], v[156:159], v[36:39]\n\t"
"ds_read_b64_tr_b16 v[222:223], v232 offset:14120\n\t"
"v_mul_f32 v227, v131, v227\n\t"
"v_exp_f32 v224, v224\n\t"
"v_mfma_f32_16x16x32_bf16 v[36:39], v[192:195], v[148:151], v[36:39]\n\t"
"v_exp_f32 v225, v225\n\t"
"v_exp_f32 v226, v226\n\t"
"v_exp_f32 v227, v227\n\t"
"s_waitcnt lgkmcnt(8)\n\t"
"v_mfma_f32_16x16x32_bf16 v[40:43], v[200:203], v[144:147], v[40:43]\n\t"
"ds_read_b64_tr_b16 v[192:193], v232 offset:2048\n\t"
"ds_read_b64_tr_b16 v[194:195], v232 offset:2304\n\t"
"s_add_u32 m0, s62, 1024\n\t"
"s_add_u32 s76, s66, 24704\n\t"
"v_mfma_f32_16x16x32_bf16 v[40:43], v[204:207], v[152:155], v[40:43]\n\t"
"buffer_load_dwordx4 v246, s[52:55], s76 offen lds\n\t"
"v_mul_f32 v228, v128, v132\n\t"
"v_mul_f32 v229, v129, v133\n\t"
"v_mul_f32 v230, v130, v134\n\t"
"v_mfma_f32_16x16x32_bf16 v[44:47], v[204:207], v[156:159], v[44:47]\n\t"
"v_mul_f32 v231, v131, v135\n\t"
"ds_read_b64_tr_b16 v[196:197], v232 offset:14368\n\t"
"ds_read_b64_tr_b16 v[198:199], v232 offset:14624\n\t"
"v_fma_f32 v224, v224, s86, s86\n\t"
"v_mfma_f32_16x16x32_bf16 v[44:47], v[200:203], v[148:151], v[44:47]\n\t"
"v_fma_f32 v225, v225, s86, s86\n\t"
"v_fma_f32 v226, v226, s86, s86\n\t"
"v_fma_f32 v227, v227, s86, s86\n\t"
"v_rcp_f32 v224, v224\n\t"
"s_waitcnt lgkmcnt(8)\n\t"
"v_mfma_f32_16x16x32_bf16 v[48:51], v[208:211], v[144:147], v[48:51]\n\t"
"ds_read_b64_tr_b16 v[200:201], v232 offset:2056\n\t"
"ds_read_b64_tr_b16 v[202:203], v232 offset:2312\n\t"
"v_rcp_f32 v225, v225\n\t"
"v_rcp_f32 v226, v226\n\t"
"v_mfma_f32_16x16x32_bf16 v[48:51], v[212:215], v[152:155], v[48:51]\n\t"
"v_rcp_f32 v227, v227\n\t"
"v_mul_f32 v228, v228, v224\n\t"
"ds_read_b64_tr_b16 v[204:205], v232 offset:14376\n\t"
"v_mfma_f32_16x16x32_bf16 v[52:55], v[212:215], v[156:159], v[52:55]\n\t"
"ds_read_b64_tr_b16 v[206:207], v232 offset:14632\n\t"
"v_mul_f32 v229, v229, v225\n\t"
"v_mul_f32 v230, v230, v226\n\t"
"v_mfma_f32_16x16x32_bf16 v[52:55], v[208:211], v[148:151], v[52:55]\n\t"
"v_mul_f32 v231, v231, v227\n\t"
"v_cvt_pk_bf16_f32 v238, v228, v229\n\t"
"v_cvt_pk_bf16_f32 v239, v230, v231\n\t"
"s_waitcnt lgkmcnt(8)\n\t"
"v_mfma_f32_16x16x32_bf16 v[56:59], v[216:219], v[144:147], v[56:59]\n\t"
"ds_read_b64_tr_b16 v[208:209], v232 offset:2560\n\t"
"ds_read_b64_tr_b16 v[210:211], v232 offset:2816\n\t"
"s_add_u32 s87, s70, 0\n\t"
"buffer_store_dwordx2 v[238:239], v247, s[56:59], s87 offen offset:0 sc1\n\t"
"v_mul_f32 v224, v136, v136\n\t"
"v_mul_f32 v225, v137, v137\n\t"
"v_mfma_f32_16x16x32_bf16 v[56:59], v[220:223], v[152:155], v[56:59]\n\t"
"v_mul_f32 v226, v138, v138\n\t"
"v_mul_f32 v227, v139, v139\n\t"
"ds_read_b64_tr_b16 v[212:213], v232 offset:14880\n\t"
"ds_read_b64_tr_b16 v[214:215], v232 offset:15136\n\t"
"s_add_u32 m0, s62, 2048\n\t"
"v_mfma_f32_16x16x32_bf16 v[60:63], v[220:223], v[156:159], v[60:63]\n\t"
"s_add_u32 s76, s66, 24832\n\t"
"buffer_load_dwordx4 v246, s[52:55], s76 offen lds\n\t"
"v_fmaak_f32 v224, v224, v253, 0xc1b0e49c\n\t"
"v_fmaak_f32 v225, v225, v253, 0xc1b0e49c\n\t"
"v_fmaak_f32 v226, v226, v253, 0xc1b0e49c\n\t"
"v_mfma_f32_16x16x32_bf16 v[60:63], v[216:219], v[148:151], v[60:63]\n\t"
"v_fmaak_f32 v227, v227, v253, 0xc1b0e49c\n\t"
"v_cvt_pk_bf16_f32 v96, v0, v1\n\t"
"v_cvt_pk_bf16_f32 v97, v2, v3\n\t"
"v_cvt_pk_bf16_f32 v100, v4, v5\n\t"
"v_cvt_pk_bf16_f32 v101, v6, v7\n\t"
"s_waitcnt lgkmcnt(8)\n\t"
"v_mfma_f32_16x16x32_bf16 v[64:67], v[192:195], v[144:147], v[64:67]\n\t"
"ds_read_b64_tr_b16 v[216:217], v232 offset:2568\n\t"
"ds_read_b64_tr_b16 v[218:219], v232 offset:2824\n\t"
"v_mul_f32 v224, v136, v224\n\t"
"v_mul_f32 v225, v137, v225\n\t"
"v_mul_f32 v226, v138, v226\n\t"
"v_mul_f32 v227, v139, v227\n\t"
"v_mfma_f32_16x16x32_bf16 v[64:67], v[196:199], v[152:155], v[64:67]\n\t"
"v_exp_f32 v224, v224\n\t"
"v_cvt_pk_bf16_f32 v98, v8, v9\n\t"
"v_cvt_pk_bf16_f32 v99, v10, v11\n\t"
"v_cvt_pk_bf16_f32 v102, v12, v13\n\t"
"v_cvt_pk_bf16_f32 v103, v14, v15\n\t"
"v_mfma_f32_16x16x32_bf16 v[68:71], v[196:199], v[156:159], v[68:71]\n\t"
"ds_read_b64_tr_b16 v[220:221], v232 offset:14888\n\t"
"ds_read_b64_tr_b16 v[222:223], v232 offset:15144\n\t"
"v_exp_f32 v225, v225\n\t"
"v_exp_f32 v226, v226\n\t"
"v_exp_f32 v227, v227\n\t"
"v_mfma_f32_16x16x32_bf16 v[68:71], v[192:195], v[148:151], v[68:71]\n\t"
"v_mul_f32 v228, v136, v140\n\t"
"v_cvt_pk_bf16_f32 v104, v16, v17\n\t"
"v_cvt_pk_bf16_f32 v105, v18, v19\n\t"
"v_cvt_pk_bf16_f32 v108, v20, v21\n\t"
"v_cvt_pk_bf16_f32 v109, v22, v23\n\t"
"s_waitcnt vmcnt(12)\n\t"
"s_barrier\n\t"
"v_add_u32 v232, 73920, v249\n\t"
"v_add_u32 v233, 73920, v250\n\t"
"s_waitcnt lgkmcnt(8)\n\t"
"v_mfma_f32_16x16x32_bf16 v[72:75], v[200:203], v[144:147], v[72:75]\n\t"
"ds_read_b128 v[192:195], v232 offset:0\n\t"
"v_mul_f32 v229, v137, v141\n\t"
"v_mul_f32 v230, v138, v142\n\t"
"v_mul_f32 v231, v139, v143\n\t"
"v_fma_f32 v224, v224, s86, s86\n\t"
"v_mfma_f32_16x16x32_bf16 v[72:75], v[204:207], v[152:155], v[72:75]\n\t"
"v_fma_f32 v225, v225, s86, s86\n\t"
"v_cvt_pk_bf16_f32 v106, v24, v25\n\t"
"v_cvt_pk_bf16_f32 v107, v26, v27\n\t"
"v_cvt_pk_bf16_f32 v110, v28, v29\n\t"
"v_cvt_pk_bf16_f32 v111, v30, v31\n\t"
"v_mfma_f32_16x16x32_bf16 v[76:79], v[204:207], v[156:159], v[76:79]\n\t"
"ds_read_b128 v[196:199], v233 offset:0\n\t"
"v_fma_f32 v226, v226, s86, s86\n\t"
"v_fma_f32 v227, v227, s86, s86\n\t"
"v_rcp_f32 v224, v224\n\t"
"v_rcp_f32 v225, v225\n\t"
"v_mfma_f32_16x16x32_bf16 v[76:79], v[200:203], v[148:151], v[76:79]\n\t"
"v_cvt_pk_bf16_f32 v112, v32, v33\n\t"
"v_cvt_pk_bf16_f32 v113, v34, v35\n\t"
"v_cvt_pk_bf16_f32 v116, v36, v37\n\t"
"v_cvt_pk_bf16_f32 v117, v38, v39\n\t"
"s_waitcnt lgkmcnt(6)\n\t"
"v_mfma_f32_16x16x32_bf16 v[80:83], v[208:211], v[144:147], v[80:83]\n\t"
"ds_read_b128 v[200:203], v232 offset:1024\n\t"
"v_rcp_f32 v226, v226\n\t"
"v_rcp_f32 v227, v227\n\t"
"v_mul_f32 v228, v228, v224\n\t"
"v_mul_f32 v229, v229, v225\n\t"
"v_mfma_f32_16x16x32_bf16 v[80:83], v[212:215], v[152:155], v[80:83]\n\t"
"v_mul_f32 v230, v230, v226\n\t"
"v_cvt_pk_bf16_f32 v114, v40, v41\n\t"
"v_cvt_pk_bf16_f32 v115, v42, v43\n\t"
"v_cvt_pk_bf16_f32 v118, v44, v45\n\t"
"v_cvt_pk_bf16_f32 v119, v46, v47\n\t"
"v_mfma_f32_16x16x32_bf16 v[84:87], v[212:215], v[156:159], v[84:87]\n\t"
"ds_read_b128 v[204:207], v233 offset:1024\n\t"
"v_mul_f32 v231, v231, v227\n\t"
"v_cvt_pk_bf16_f32 v238, v228, v229\n\t"
"v_cvt_pk_bf16_f32 v239, v230, v231\n\t"
"s_add_u32 s87, s70, 245760\n\t"
"v_mfma_f32_16x16x32_bf16 v[84:87], v[208:211], v[148:151], v[84:87]\n\t"
"buffer_store_dwordx2 v[238:239], v247, s[56:59], s87 offen offset:0 sc1\n\t"
"v_cvt_pk_bf16_f32 v120, v48, v49\n\t"
"v_cvt_pk_bf16_f32 v121, v50, v51\n\t"
"v_cvt_pk_bf16_f32 v124, v52, v53\n\t"
"v_cvt_pk_bf16_f32 v125, v54, v55\n\t"
"s_waitcnt lgkmcnt(4)\n\t"
"v_mfma_f32_16x16x32_bf16 v[88:91], v[216:219], v[144:147], v[88:91]\n\t"
"ds_read_b128 v[208:211], v232 offset:2048\n\t"
"v_cvt_pk_bf16_f32 v122, v56, v57\n\t"
"v_cvt_pk_bf16_f32 v123, v58, v59\n\t"
"v_mfma_f32_16x16x32_bf16 v[88:91], v[220:223], v[152:155], v[88:91]\n\t"
"v_cvt_pk_bf16_f32 v126, v60, v61\n\t"
"v_cvt_pk_bf16_f32 v127, v62, v63\n\t"
"v_mfma_f32_16x16x32_bf16 v[92:95], v[220:223], v[156:159], v[92:95]\n\t"
"ds_read_b128 v[212:215], v233 offset:2048\n\t"
"v_cvt_pk_bf16_f32 v128, v64, v65\n\t"
"v_cvt_pk_bf16_f32 v129, v66, v67\n\t"
"v_mfma_f32_16x16x32_bf16 v[92:95], v[216:219], v[148:151], v[92:95]\n\t"
"v_cvt_pk_bf16_f32 v132, v68, v69\n\t"
"v_cvt_pk_bf16_f32 v133, v70, v71\n\t"
"s_nop 1\n\t"
"v_cvt_pk_bf16_f32 v130, v72, v73\n\t"
"v_cvt_pk_bf16_f32 v131, v74, v75\n\t"
"v_cvt_pk_bf16_f32 v134, v76, v77\n\t"
"v_cvt_pk_bf16_f32 v135, v78, v79\n\t"
"v_cvt_pk_bf16_f32 v136, v80, v81\n\t"
"v_cvt_pk_bf16_f32 v137, v82, v83\n\t"
"v_cvt_pk_bf16_f32 v140, v84, v85\n\t"
"v_cvt_pk_bf16_f32 v141, v86, v87\n\t"
"s_nop 1\n\t"
"v_cvt_pk_bf16_f32 v138, v88, v89\n\t"
"v_cvt_pk_bf16_f32 v139, v90, v91\n\t"
"v_cvt_pk_bf16_f32 v142, v92, v93\n\t"
"v_cvt_pk_bf16_f32 v143, v94, v95\n\t"
"LP1J_%=:\n\t"
"s_mov_b32 s79, 1\n\t"
"s_mov_b32 s80, 0x42700000\n\t"
"s_mov_b32 s77, 98560\n\t"
"s_cmp_lg_u32 s71, 0\n\t"
"s_cselect_b32 s78, 1, 2\n\t"
"s_cselect_b64 s[44:45], s[48:49], s[52:53]\n\t"
"s_cselect_b64 s[46:47], s[50:51], s[54:55]\n\t"
"s_mul_i32 s78, s78, 24640\n\t"
"s_add_u32 s88, s60, s75\n\t"
"s_add_u32 s78, s78, s88\n\t"
"s_add_u32 s88, s88, 147840\n\t"
"s_add_u32 s82, s39, 49152\n\t"
"LLOOPF_%=:\n\t"
"s_setprio 1\n\t"
"v_mov_b32 v237, 0xff800000\n\t"
"v_mov_b32 v238, 0xff800000\n\t"
"s_waitcnt lgkmcnt(5)\n\t"
"v_mfma_f32_16x16x32_bf16 v[144:147], v[192:195], v[96:99], 0\n\t"
"ds_read_b128 v[216:219], v232 offset:6144\n\t"
"s_waitcnt lgkmcnt(5)\n\t"
"v_mfma_f32_16x16x32_bf16 v[144:147], v[196:199], v[104:107], v[144:147]\n\t"
"s_waitcnt lgkmcnt(4)\n\t"
"v_mfma_f32_16x16x32_bf16 v[144:147], v[200:203], v[112:115], v[144:147]\n\t"
"v_mfma_f32_16x16x32_bf16 v[148:151], v[200:203], v[116:119], 0\n\t"
"ds_read_b128 v[220:223], v233 offset:6144\n\t"
"v_mfma_f32_16x16x32_bf16 v[148:151], v[196:199], v[108:111], v[148:151]\n\t"
"v_mfma_f32_16x16x32_bf16 v[148:151], v[192:195], v[100:103], v[148:151]\n\t"
"v_mov_b32 v241, 1\n\t"
"v_lshrrev_b32 v243, 2, v240\n\t"
"s_waitcnt lgkmcnt(4)\n\t"
"v_mfma_f32_16x16x32_bf16 v[148:151], v[204:207], v[124:127], v[148:151]\n\t"
"ds_read_b128 v[192:195], v232 offset:7168\n\t"
"s_waitcnt lgkmcnt(4)\n\t"
"v_mfma_f32_16x16x32_bf16 v[148:151], v[208:211], v[132:135], v[148:151]\n\t"
"s_waitcnt lgkmcnt(3)\n\t"
"v_mfma_f32_16x16x32_bf16 v[148:151], v[212:215], v[140:143], v[148:151]\n\t"
"ds_read_b128 v[196:199], v233 offset:7168\n\t"
"v_mfma_f32_16x16x32_bf16 v[144:147], v[212:215], v[136:139], v[144:147]\n\t"
"v_mfma_f32_16x16x32_bf16 v[144:147], v[208:211], v[128:131], v[144:147]\n\t"
"ds_read_b128 v[200:203], v232 offset:8192\n\t"
"v_mfma_f32_16x16x32_bf16 v[144:147], v[204:207], v[120:123], v[144:147]\n\t"
"v_min_u32 v248, 236, v243\n\t"
"v_and_b32 v243, 28, v243\n\t"
"s_waitcnt lgkmcnt(4)\n\t"
"v_mfma_f32_16x16x32_bf16 v[152:155], v[216:219], v[96:99], 0\n\t"
"ds_read_b128 v[204:207], v233 offset:8192\n\t"
"s_waitcnt lgkmcnt(4)\n\t"
"v_mfma_f32_16x16x32_bf16 v[152:155], v[220:223], v[104:107], v[152:155]\n\t"
"ds_read_b128 v[208:211], v232 offset:12288\n\t"
"s_waitcnt lgkmcnt(4)\n\t"
"v_mfma_f32_16x16x32_bf16 v[152:155], v[192:195], v[112:115], v[152:155]\n\t"
"ds_read_b128 v[212:215], v233 offset:12288\n\t"
"v_mfma_f32_16x16x32_bf16 v[156:159], v[192:195], v[116:119], 0\n\t"
"v_max3_f32 v238, v238, v148, v149\n\t"
"v_mfma_f32_16x16x32_bf16 v[156:159], v[220:223], v[108:111], v[156:159]\n\t"
"v_max3_f32 v238, v238, v150, v151\n\t"
"v_mfma_f32_16x16x32_bf16 v[156:159], v[216:219], v[100:103], v[156:159]\n\t"
"v_add_u32 v253, s10, v240\n\t"
"s_waitcnt lgkmcnt(4)\n\t"
"v_mfma_f32_16x16x32_bf16 v[156:159], v[196:199], v[124:127], v[156:159]\n\t"
"ds_read_b128 v[216:219], v232 offset:13312\n\t"
"s_waitcnt lgkmcnt(4)\n\t"
"v_mfma_f32_16x16x32_bf16 v[156:159], v[200:203], v[132:135], v[156:159]\n\t"
"ds_read_b128 v[220:223], v233 offset:13312\n\t"
"s_waitcnt lgkmcnt(4)\n\t"
"v_mfma_f32_16x16x32_bf16 v[156:159], v[204:207], v[140:143], v[156:159]\n\t"
"ds_read_b128 v[192:195], v232 offset:14336\n\t"
"v_mfma_f32_16x16x32_bf16 v[152:155], v[204:207], v[136:139], v[152:155]\n\t"
"v_max3_f32 v237, v237, v144, v145\n\t"
"v_mfma_f32_16x16x32_bf16 v[152:155], v[200:203], v[128:131], v[152:155]\n\t"
"v_max3_f32 v237, v237, v146, v147\n\t"
"v_mfma_f32_16x16x32_bf16 v[152:155], v[196:199], v[120:123], v[152:155]\n\t"
"s_waitcnt lgkmcnt(4)\n\t"
"v_mfma_f32_16x16x32_bf16 v[160:163], v[208:211], v[96:99], 0\n\t"
"ds_read_b128 v[196:199], v233 offset:14336\n\t"
"s_waitcnt lgkmcnt(4)\n\t"
"v_mfma_f32_16x16x32_bf16 v[160:163], v[212:215], v[104:107], v[160:163]\n\t"
"ds_read_b128 v[200:203], v232 offset:18432\n\t"
"s_waitcnt lgkmcnt(4)\n\t"
"v_mfma_f32_16x16x32_bf16 v[160:163], v[216:219], v[112:115], v[160:163]\n\t"
"ds_read_b128 v[204:207], v233 offset:18432\n\t"
"v_mfma_f32_16x16x32_bf16 v[164:167], v[216:219], v[116:119], 0\n\t"
"v_max3_f32 v238, v238, v156, v157\n\t"
"v_mfma_f32_16x16x32_bf16 v[164:167], v[212:215], v[108:111], v[164:167]\n\t"
"v_max3_f32 v238, v238, v158, v159\n\t"
"v_mfma_f32_16x16x32_bf16 v[164:167], v[208:211], v[100:103], v[164:167]\n\t"
"s_waitcnt lgkmcnt(4)\n\t"
"v_mfma_f32_16x16x32_bf16 v[164:167], v[220:223], v[124:127], v[164:167]\n\t"
"ds_read_b128 v[208:211], v232 offset:19456\n\t"
"s_waitcnt lgkmcnt(4)\n\t"
"v_mfma_f32_16x16x32_bf16 v[164:167], v[192:195], v[132:135], v[164:167]\n\t"
"ds_read_b128 v[212:215], v233 offset:19456\n\t"
"s_waitcnt lgkmcnt(4)\n\t"
"v_mfma_f32_16x16x32_bf16 v[164:167], v[196:199], v[140:143], v[164:167]\n\t"
"ds_read_b128 v[216:219], v232 offset:20480\n\t"
"v_mfma_f32_16x16x32_bf16 v[160:163], v[196:199], v[136:139], v[160:163]\n\t"
"v_max3_f32 v237, v237, v152, v153\n\t"
"v_mfma_f32_16x16x32_bf16 v[160:163], v[192:195], v[128:131], v[160:163]\n\t"
"v_max3_f32 v237, v237, v154, v155\n\t"
"v_mfma_f32_16x16x32_bf16 v[160:163], v[220:223], v[120:123], v[160:163]\n\t"
"s_waitcnt lgkmcnt(4)\n\t"
"v_mfma_f32_16x16x32_bf16 v[168:171], v[200:203], v[96:99], 0\n\t"
"ds_read_b128 v[220:223], v233 offset:20480\n\t"
"v_add_u32 v232, s77, v251\n\t"
"s_waitcnt lgkmcnt(4)\n\t"
"v_mfma_f32_16x16x32_bf16 v[168:171], v[204:207], v[104:107], v[168:171]\n\t"
"v_add_u32 v233, s77, v252\n\t"
"s_add_u32 s77, s77, 24640\n\t"
"s_waitcnt lgkmcnt(3)\n\t"
"v_mfma_f32_16x16x32_bf16 v[168:171], v[208:211], v[112:115], v[168:171]\n\t"
"s_cmp_ge_u32 s77, 147840\n\t"
"s_cselect_b32 s77, 0, s77\n\t"
"v_mfma_f32_16x16x32_bf16 v[172:175], v[208:211], v[116:119], 0\n\t"
"ds_read_b64_tr_b16 v[192:193], v232 offset:0\n\t"
"ds_read_b64_tr_b16 v[194:195], v232 offset:6144\n\t"
"v_mfma_f32_16x16x32_bf16 v[172:175], v[204:207], v[108:111], v[172:175]\n\t"
"ds_read_b64_tr_b16 v[196:197], v232 offset:12288\n\t"
"ds_read_b64_tr_b16 v[198:199], v232 offset:18432\n\t"
"v_mfma_f32_16x16x32_bf16 v[172:175], v[200:203], v[100:103], v[172:175]\n\t"
"v_max3_f32 v238, v238, v164, v165\n\t"
"v_max3_f32 v238, v238, v166, v167\n\t"
"s_waitcnt lgkmcnt(6)\n\t"
"v_mfma_f32_16x16x32_bf16 v[172:175], v[212:215], v[124:127], v[172:175]\n\t"
"ds_read_b64_tr_b16 v[200:201], v233 offset:0\n\t"
"ds_read_b64_tr_b16 v[202:203], v233 offset:6144\n\t"
"s_waitcnt lgkmcnt(7)\n\t"
"v_mfma_f32_16x16x32_bf16 v[172:175], v[216:219], v[132:135], v[172:175]\n\t"
"ds_read_b64_tr_b16 v[204:205], v233 offset:12288\n\t"
"s_waitcnt lgkmcnt(7)\n\t"
"v_mfma_f32_16x16x32_bf16 v[172:175], v[220:223], v[140:143], v[172:175]\n\t"
"ds_read_b64_tr_b16 v[206:207], v233 offset:18432\n\t"
"v_mfma_f32_16x16x32_bf16 v[168:171], v[220:223], v[136:139], v[168:171]\n\t"
"ds_read_b64_tr_b16 v[208:209], v232 offset:512\n\t"
"ds_read_b64_tr_b16 v[210:211], v232 offset:6656\n\t"
"v_mfma_f32_16x16x32_bf16 v[168:171], v[216:219], v[128:131], v[168:171]\n\t"
"v_max3_f32 v237, v237, v160, v161\n\t"
"v_mfma_f32_16x16x32_bf16 v[168:171], v[212:215], v[120:123], v[168:171]\n\t"
"v_max3_f32 v237, v237, v162, v163\n\t"
"ds_read_b64_tr_b16 v[212:213], v232 offset:12800\n\t"
"ds_read_b64_tr_b16 v[214:215], v232 offset:18944\n\t"
"s_setprio 0\n\t"
"s_cmp_lg_u32 s71, 0\n\t"
"s_cbranch_scc1 LB1F_%=\n\t"
"s_waitcnt vmcnt(0)\n\t"
"s_barrier\n\t"
"LB1F_%=:\n\t"
"s_add_u32 m0, s78, 0\n\t"
"s_add_u32 s76, s82, 0\n\t"
"buffer_load_dwordx4 v244, s[44:47], s76 offen lds\n\t"
"v_max3_f32 v238, v238, v172, v173\n\t"
"v_max3_f32 v238, v238, v174, v175\n\t"
"v_max3_f32 v237, v237, v168, v169\n\t"
"v_max3_f32 v237, v237, v170, v171\n\t"
"v_mov_b32 v239, v238\n\t"
"v_mov_b32 v236, v237\n\t"
"s_nop 1\n\t"
"v_permlane16_swap_b32 v238, v239\n\t"
"v_permlane16_swap_b32 v237, v236\n\t"
"s_nop 0\n\t"
"v_max_f32 v238, v238, v239\n\t"
"v_max_f32 v237, v237, v236\n\t"
"v_mov_b32 v239, v238\n\t"
"v_mov_b32 v236, v237\n\t"
"s_nop 1\n\t"
"v_permlane32_swap_b32 v238, v239\n\t"
"v_permlane32_swap_b32 v237, v236\n\t"
"s_nop 0\n\t"
"v_max_f32 v238, v238, v239\n\t"
"v_max_f32 v237, v237, v236\n\t"
"v_sub_f32 v224, 0, v237\n\t"
"v_sub_f32 v225, 0, v237\n\t"
"v_sub_f32 v226, 0, v237\n\t"
"v_sub_f32 v227, 0, v237\n\t"
"v_sub_f32 v228, 0, v238\n\t"
"v_sub_f32 v229, 0, v238\n\t"
"v_sub_f32 v230, 0, v238\n\t"
"v_sub_f32 v231, 0, v238\n\t"
"v_sub_f32 v144, v144, v237\n\t"
"v_sub_f32 v145, v145, v237\n\t"
"v_sub_f32 v146, v146, v237\n\t"
"v_sub_f32 v147, v147, v237\n\t"
"v_sub_f32 v152, v152, v237\n\t"
"v_sub_f32 v153, v153, v237\n\t"
"v_sub_f32 v154, v154, v237\n\t"
"v_sub_f32 v155, v155, v237\n\t"
"v_exp_f32 v144, v144\n\t"
"v_exp_f32 v145, v145\n\t"
"v_mov_b32 v234, v144\n\t"
"v_exp_f32 v146, v146\n\t"
"v_add_f32 v234, v234, v145\n\t"
"v_exp_f32 v147, v147\n\t"
"v_add_f32 v234, v234, v146\n\t"
"v_exp_f32 v152, v152\n\t"
"v_add_f32 v234, v234, v147\n\t"
"v_exp_f32 v153, v153\n\t"
"v_add_f32 v234, v234, v152\n\t"
"v_exp_f32 v154, v154\n\t"
"v_add_f32 v234, v234, v153\n\t"
"s_add_u32 m0, s78, 1024\n\t"
"s_add_u32 s76, s82, 128\n\t"
"buffer_load_dwordx4 v244, s[44:47], s76 offen lds\n\t"
"v_exp_f32 v155, v155\n\t"
"v_add_f32 v234, v234, v154\n\t"
"v_cvt_pk_bf16_f32 v176, v144, v145\n\t"
"v_add_f32 v234, v234, v155\n\t"
"v_cvt_pk_bf16_f32 v177, v146, v147\n\t"
"v_cvt_pk_bf16_f32 v178, v152, v153\n\t"
"v_cvt_pk_bf16_f32 v179, v154, v155\n\t"
"v_sub_f32 v148, v148, v238\n\t"
"v_sub_f32 v149, v149, v238\n\t"
"v_sub_f32 v150, v150, v238\n\t"
"v_sub_f32 v151, v151, v238\n\t"
"v_sub_f32 v156, v156, v238\n\t"
"v_sub_f32 v157, v157, v238\n\t"
"v_sub_f32 v158, v158, v238\n\t"
"v_sub_f32 v159, v159, v238\n\t"
"v_exp_f32 v148, v148\n\t"
"v_exp_f32 v149, v149\n\t"
"v_mov_b32 v235, v148\n\t"
"v_exp_f32 v150, v150\n\t"
"v_add_f32 v235, v235, v149\n\t"
"s_add_u32 m0, s78, 2048\n\t"
"s_add_u32 s76, s82, 256\n\t"
"buffer_load_dwordx4 v244, s[44:47], s76 offen lds\n\t"
"v_exp_f32 v151, v151\n\t"
"v_add_f32 v235, v235, v150\n\t"
"v_exp_f32 v156, v156\n\t"
"v_add_f32 v235, v235, v151\n\t"
"v_exp_f32 v157, v157\n\t"
"v_add_f32 v235, v235, v156\n\t"
"v_exp_f32 v158, v158\n\t"
"v_add_f32 v235, v235, v157\n\t"
"s_add_u32 m0, s78, 3072\n\t"
"s_add_u32 s76, s82, 3072\n\t"
"buffer_load_dwordx4 v255, s[44:47], s76 offen lds\n\t"
"v_exp_f32 v159, v159\n\t"
"v_add_f32 v235, v235, v158\n\t"
"v_cvt_pk_bf16_f32 v180, v148, v149\n\t"
"v_add_f32 v235, v235, v159\n\t"
"v_cvt_pk_bf16_f32 v181, v150, v151\n\t"
"v_cvt_pk_bf16_f32 v182, v156, v157\n\t"
"v_cvt_pk_bf16_f32 v183, v158, v159\n\t"
"v_sub_f32 v160, v160, v237\n\t"
"v_sub_f32 v161, v161, v237\n\t"
"v_sub_f32 v162, v162, v237\n\t"
"v_sub_f32 v163, v163, v237\n\t"
"v_sub_f32 v168, v168, v237\n\t"
"v_sub_f32 v169, v169, v237\n\t"
"v_sub_f32 v170, v170, v237\n\t"
"v_sub_f32 v171, v171, v237\n\t"
"v_exp_f32 v160, v160\n\t"
"v_exp_f32 v161, v161\n\t"
"v_add_f32 v234, v234, v160\n\t"
"v_exp_f32 v162, v162\n\t"
"v_add_f32 v234, v234, v161\n\t"
"s_add_u32 m0, s78, 4096\n\t"
"s_add_u32 s76, s82, 3200\n\t"
"buffer_load_dwordx4 v255, s[44:47], s76 offen lds\n\t"
"v_exp_f32 v163, v163\n\t"
"v_add_f32 v234, v234, v162\n\t"
"v_exp_f32 v168, v168\n\t"
"v_add_f32 v234, v234, v163\n\t"
"v_exp_f32 v169, v169\n\t"
"v_add_f32 v234, v234, v168\n\t"
"v_exp_f32 v170, v170\n\t"
"v_add_f32 v234, v234, v169\n\t"
"s_add_u32 m0, s78, 5120\n\t"
"s_add_u32 s76, s82, 3328\n\t"
"buffer_load_dwordx4 v255, s[44:47], s76 offen lds\n\t"
"v_exp_f32 v171, v171\n\t"
"v_add_f32 v234, v234, v170\n\t"
"v_cvt_pk_bf16_f32 v184, v160, v161\n\t"
"v_add_f32 v234, v234, v171\n\t"
"v_cvt_pk_bf16_f32 v185, v162, v163\n\t"
"v_cvt_pk_bf16_f32 v186, v168, v169\n\t"
"v_cvt_pk_bf16_f32 v187, v170, v171\n\t"
"v_sub_f32 v164, v164, v238\n\t"
"v_sub_f32 v165, v165, v238\n\t"
"v_sub_f32 v166, v166, v238\n\t"
"v_sub_f32 v167, v167, v238\n\t"
"v_sub_f32 v172, v172, v238\n\t"
"v_sub_f32 v173, v173, v238\n\t"
"v_sub_f32 v174, v174, v238\n\t"
"v_sub_f32 v175, v175, v238\n\t"
"v_exp_f32 v164, v164\n\t"
"v_exp_f32 v165, v165\n\t"
"v_add_f32 v235, v235, v164\n\t"
"v_exp_f32 v166, v166\n\t"
"v_add_f32 v235, v235, v165\n\t"
"v_exp_f32 v167, v167\n\t"
"v_add_f32 v235, v235, v166\n\t"
"v_exp_f32 v172, v172\n\t"
"v_add_f32 v235, v235, v167\n\t"
"v_exp_f32 v173, v173\n\t"
"v_add_f32 v235, v235, v172\n\t"
"v_exp_f32 v174, v174\n\t"
"v_add_f32 v235, v235, v173\n\t"
"v_exp_f32 v175, v175\n\t"
"v_add_f32 v235, v235, v174\n\t"
"v_cvt_pk_bf16_f32 v188, v164, v165\n\t"
"v_add_f32 v235, v235, v175\n\t"
"v_cvt_pk_bf16_f32 v189, v166, v167\n\t"
"v_cvt_pk_bf16_f32 v190, v172, v173\n\t"
"v_cvt_pk_bf16_f32 v191, v174, v175\n\t"
"s_add_u32 s78, s78, 49280\n\t"
"s_cmp_ge_u32 s78, s88\n\t"
"s_cselect_b32 s76, 147840, 0\n\t"
"s_sub_u32 s78, s78, s76\n\t"
"s_cmp_eq_u32 s79, 5\n\t"
"s_cselect_b32 s78, 0x30000, s78\n\t"
"s_cselect_b32 s82, 0x10000000, s82\n\t"
"s_add_u32 s82, s82, 24576\n\t"
"s_cmp_eq_u32 s71, 0\n\t"
"s_cbranch_scc1 LB2F_%=\n\t"
"s_waitcnt vmcnt(6)\n\t"
"s_barrier\n\t"
"LB2F_%=:\n\t"
"s_waitcnt lgkmcnt(10)\n\t"
"v_mfma_f32_16x16x32_bf16 v[0:3], v[192:195], v[176:179], 0\n\t"
"ds_read_b64_tr_b16 v[216:217], v233 offset:512\n\t"
"s_waitcnt lgkmcnt(9)\n\t"
"v_mfma_f32_16x16x32_bf16 v[0:3], v[196:199], v[184:187], v[0:3]\n\t"
"ds_read_b64_tr_b16 v[218:219], v233 offset:6656\n\t"
"v_mfma_f32_16x16x32_bf16 v[4:7], v[196:199], v[188:191], 0\n\t"
"ds_read_b64_tr_b16 v[220:221], v233 offset:12800\n\t"
"v_mfma_f32_16x16x32_bf16 v[4:7], v[192:195], v[180:183], v[4:7]\n\t"
"ds_read_b64_tr_b16 v[222:223], v233 offset:18944\n\t"
"s_mov_b64 exec, 1\n\t"
"buffer_store_dword v241, off, s[56:59], s35 sc1\n\t"
"buffer_store_dword v241, off, s[56:59], s21 sc1\n\t"
"s_mov_b64 exec, -1\n\t"
"s_waitcnt lgkmcnt(10)\n\t"
"v_mfma_f32_16x16x32_bf16 v[8:11], v[200:203], v[176:179], 0\n\t"
"ds_read_b64_tr_b16 v[192:193], v232 offset:1024\n\t"
"s_waitcnt lgkmcnt(9)\n\t"
"v_mfma_f32_16x16x32_bf16 v[8:11], v[204:207], v[184:187], v[8:11]\n\t"
"ds_read_b64_tr_b16 v[194:195], v232 offset:7168\n\t"
"v_mfma_f32_16x16x32_bf16 v[12:15], v[204:207], v[188:191], 0\n\t"
"ds_read_b64_tr_b16 v[196:197], v232 offset:13312\n\t"
"v_mfma_f32_16x16x32_bf16 v[12:15], v[200:203], v[180:183], v[12:15]\n\t"
"ds_read_b64_tr_b16 v[198:199], v232 offset:19456\n\t"
"s_add_u32 s21, s21, 2048\n\t"
"s_add_u32 s83, s83, 0\n\t"
"s_waitcnt lgkmcnt(10)\n\t"
"v_mfma_f32_16x16x32_bf16 v[16:19], v[208:211], v[176:179], 0\n\t"
"ds_read_b64_tr_b16 v[200:201], v233 offset:1024\n\t"
"s_waitcnt lgkmcnt(9)\n\t"
"v_mfma_f32_16x16x32_bf16 v[16:19], v[212:215], v[184:187], v[16:19]\n\t"
"ds_read_b64_tr_b16 v[202:203], v233 offset:7168\n\t"
"v_mfma_f32_16x16x32_bf16 v[20:23], v[212:215], v[188:191], 0\n\t"
"ds_read_b64_tr_b16 v[204:205], v233 offset:13312\n\t"
"v_mfma_f32_16x16x32_bf16 v[20:23], v[208:211], v[180:183], v[20:23]\n\t"
"ds_read_b64_tr_b16 v[206:207], v233 offset:19456\n\t"
"s_sub_u32 s34, s14, s33\n\t"
"s_sub_u32 s35, s15, s33\n\t"
"s_waitcnt lgkmcnt(10)\n\t"
"v_mfma_f32_16x16x32_bf16 v[24:27], v[216:219], v[176:179], 0\n\t"
"ds_read_b64_tr_b16 v[208:209], v232 offset:1536\n\t"
"s_waitcnt lgkmcnt(9)\n\t"
"v_mfma_f32_16x16x32_bf16 v[24:27], v[220:223], v[184:187], v[24:27]\n\t"
"ds_read_b64_tr_b16 v[210:211], v232 offset:7680\n\t"
"v_mfma_f32_16x16x32_bf16 v[28:31], v[220:223], v[188:191], 0\n\t"
"ds_read_b64_tr_b16 v[212:213], v232 offset:13824\n\t"
"v_mfma_f32_16x16x32_bf16 v[28:31], v[216:219], v[180:183], v[28:31]\n\t"
"ds_read_b64_tr_b16 v[214:215], v232 offset:19968\n\t"
"s_mov_b32 s48, s4\n\t"
"s_and_b32 s49, s5, 0xffff\n\t"
"s_waitcnt lgkmcnt(10)\n\t"
"v_mfma_f32_16x16x32_bf16 v[32:35], v[192:195], v[176:179], 0\n\t"
"ds_read_b64_tr_b16 v[216:217], v233 offset:1536\n\t"
"s_waitcnt lgkmcnt(9)\n\t"
"v_mfma_f32_16x16x32_bf16 v[32:35], v[196:199], v[184:187], v[32:35]\n\t"
"ds_read_b64_tr_b16 v[218:219], v233 offset:7680\n\t"
"v_mfma_f32_16x16x32_bf16 v[36:39], v[196:199], v[188:191], 0\n\t"
"ds_read_b64_tr_b16 v[220:221], v233 offset:13824\n\t"
"v_mfma_f32_16x16x32_bf16 v[36:39], v[192:195], v[180:183], v[36:39]\n\t"
"ds_read_b64_tr_b16 v[222:223], v233 offset:19968\n\t"
"s_mov_b32 s50, 2621440\n\t"
"s_mov_b32 s51, 0x20000\n\t"
"s_waitcnt lgkmcnt(10)\n\t"
"v_mfma_f32_16x16x32_bf16 v[40:43], v[200:203], v[176:179], 0\n\t"
"ds_read_b64_tr_b16 v[192:193], v232 offset:2048\n\t"
"s_waitcnt lgkmcnt(9)\n\t"
"v_mfma_f32_16x16x32_bf16 v[40:43], v[204:207], v[184:187], v[40:43]\n\t"
"ds_read_b64_tr_b16 v[194:195], v232 offset:8192\n\t"
"v_mfma_f32_16x16x32_bf16 v[44:47], v[204:207], v[188:191], 0\n\t"
"ds_read_b64_tr_b16 v[196:197], v232 offset:14336\n\t"
"v_mfma_f32_16x16x32_bf16 v[44:47], v[200:203], v[180:183], v[44:47]\n\t"
"ds_read_b64_tr_b16 v[198:199], v232 offset:20480\n\t"
"s_mov_b32 s52, s6\n\t"
"s_and_b32 s53, s7, 0xffff\n\t"
"s_waitcnt lgkmcnt(10)\n\t"
"v_mfma_f32_16x16x32_bf16 v[48:51], v[208:211], v[176:179], 0\n\t"
"ds_read_b64_tr_b16 v[200:201], v233 offset:2048\n\t"
"s_waitcnt lgkmcnt(9)\n\t"
"v_mfma_f32_16x16x32_bf16 v[48:51], v[212:215], v[184:187], v[48:51]\n\t"
"ds_read_b64_tr_b16 v[202:203], v233 offset:8192\n\t"
"v_mfma_f32_16x16x32_bf16 v[52:55], v[212:215], v[188:191], 0\n\t"
"ds_read_b64_tr_b16 v[204:205], v233 offset:14336\n\t"
"v_mfma_f32_16x16x32_bf16 v[52:55], v[208:211], v[180:183], v[52:55]\n\t"
"ds_read_b64_tr_b16 v[206:207], v233 offset:20480\n\t"
"s_mov_b32 s54, 2621440\n\t"
"s_mov_b32 s55, 0x20000\n\t"
"s_waitcnt lgkmcnt(10)\n\t"
"v_mfma_f32_16x16x32_bf16 v[56:59], v[216:219], v[176:179], 0\n\t"
"ds_read_b64_tr_b16 v[208:209], v232 offset:2560\n\t"
"s_waitcnt lgkmcnt(9)\n\t"
"v_mfma_f32_16x16x32_bf16 v[56:59], v[220:223], v[184:187], v[56:59]\n\t"
"ds_read_b64_tr_b16 v[210:211], v232 offset:8704\n\t"
"v_mfma_f32_16x16x32_bf16 v[60:63], v[220:223], v[188:191], 0\n\t"
"ds_read_b64_tr_b16 v[212:213], v232 offset:14848\n\t"
"v_mfma_f32_16x16x32_bf16 v[60:63], v[216:219], v[180:183], v[60:63]\n\t"
"ds_read_b64_tr_b16 v[214:215], v232 offset:20992\n\t"
"s_waitcnt lgkmcnt(10)\n\t"
"v_mfma_f32_16x16x32_bf16 v[64:67], v[192:195], v[176:179], 0\n\t"
"ds_read_b64_tr_b16 v[216:217], v233 offset:2560\n\t"
"s_waitcnt lgkmcnt(9)\n\t"
"v_mfma_f32_16x16x32_bf16 v[64:67], v[196:199], v[184:187], v[64:67]\n\t"
"ds_read_b64_tr_b16 v[218:219], v233 offset:8704\n\t"
"v_mfma_f32_16x16x32_bf16 v[68:71], v[196:199], v[188:191], 0\n\t"
"ds_read_b64_tr_b16 v[220:221], v233 offset:14848\n\t"
"v_mfma_f32_16x16x32_bf16 v[68:71], v[192:195], v[180:183], v[68:71]\n\t"
"ds_read_b64_tr_b16 v[222:223], v233 offset:20992\n\t"
"s_waitcnt lgkmcnt(10)\n\t"
"v_mfma_f32_16x16x32_bf16 v[72:75], v[200:203], v[176:179], 0\n\t"
"v_add_u32 v232, s77, v249\n\t"
"v_add_u32 v233, s77, v250\n\t"
"s_waitcnt lgkmcnt(8)\n\t"
"v_mfma_f32_16x16x32_bf16 v[72:75], v[204:207], v[184:187], v[72:75]\n\t"
"s_add_u32 s77, s77, 24640\n\t"
"s_cmp_ge_u32 s77, 147840\n\t"
"v_mfma_f32_16x16x32_bf16 v[76:79], v[204:207], v[188:191], 0\n\t"
"s_cselect_b32 s77, 0, s77\n\t"
"ds_read_b128 v[192:195], v232 offset:0\n\t"
"v_mfma_f32_16x16x32_bf16 v[76:79], v[200:203], v[180:183], v[76:79]\n\t"
"ds_read_b128 v[196:199], v233 offset:0\n\t"
"s_waitcnt lgkmcnt(8)\n\t"
"v_mfma_f32_16x16x32_bf16 v[80:83], v[208:211], v[176:179], 0\n\t"
"ds_read_b128 v[200:203], v232 offset:1024\n\t"
"s_waitcnt lgkmcnt(7)\n\t"
"v_mfma_f32_16x16x32_bf16 v[80:83], v[212:215], v[184:187], v[80:83]\n\t"
"v_mfma_f32_16x16x32_bf16 v[84:87], v[212:215], v[188:191], 0\n\t"
"ds_read_b128 v[204:207], v233 offset:1024\n\t"
"v_mfma_f32_16x16x32_bf16 v[84:87], v[208:211], v[180:183], v[84:87]\n\t"
"s_waitcnt lgkmcnt(6)\n\t"
"v_mfma_f32_16x16x32_bf16 v[88:91], v[216:219], v[176:179], 0\n\t"
"ds_read_b128 v[208:211], v232 offset:2048\n\t"
"s_waitcnt lgkmcnt(5)\n\t"
"v_mfma_f32_16x16x32_bf16 v[88:91], v[220:223], v[184:187], v[88:91]\n\t"
"v_mfma_f32_16x16x32_bf16 v[92:95], v[220:223], v[188:191], 0\n\t"
"ds_read_b128 v[212:215], v233 offset:2048\n\t"
"v_mfma_f32_16x16x32_bf16 v[92:95], v[216:219], v[180:183], v[92:95]\n\t"
"LLOOP_%=:\n\t"
"s_setprio 1\n\t"
"v_mov_b32 v236, 0xff800000\n\t"
"s_waitcnt lgkmcnt(5)\n\t"
"v_mfma_f32_16x16x32_bf16 v[144:147], v[192:195], v[96:99], v[224:227]\n\t"
"ds_read_b128 v[216:219], v232 offset:6144\n\t"
"s_waitcnt lgkmcnt(5)\n\t"
"v_mfma_f32_16x16x32_bf16 v[144:147], v[196:199], v[104:107], v[144:147]\n\t"
"s_waitcnt lgkmcnt(4)\n\t"
"v_mfma_f32_16x16x32_bf16 v[144:147], v[200:203], v[112:115], v[144:147]\n\t"
"v_mfma_f32_16x16x32_bf16 v[148:151], v[200:203], v[116:119], v[228:231]\n\t"
"ds_read_b128 v[220:223], v233 offset:6144\n\t"
"v_mfma_f32_16x16x32_bf16 v[148:151], v[196:199], v[108:111], v[148:151]\n\t"
"v_mfma_f32_16x16x32_bf16 v[148:151], v[192:195], v[100:103], v[148:151]\n\t"
"s_waitcnt lgkmcnt(4)\n\t"
"v_mfma_f32_16x16x32_bf16 v[148:151], v[204:207], v[124:127], v[148:151]\n\t"
"ds_read_b128 v[192:195], v232 offset:7168\n\t"
"s_waitcnt lgkmcnt(4)\n\t"
"v_mfma_f32_16x16x32_bf16 v[148:151], v[208:211], v[132:135], v[148:151]\n\t"
"s_waitcnt lgkmcnt(3)\n\t"
"v_mfma_f32_16x16x32_bf16 v[148:151], v[212:215], v[140:143], v[148:151]\n\t"
"ds_read_b128 v[196:199], v233 offset:7168\n\t"
"v_mfma_f32_16x16x32_bf16 v[144:147], v[212:215], v[136:139], v[144:147]\n\t"
"v_mfma_f32_16x16x32_bf16 v[144:147], v[208:211], v[128:131], v[144:147]\n\t"
"ds_read_b128 v[200:203], v232 offset:8192\n\t"
"v_mfma_f32_16x16x32_bf16 v[144:147], v[204:207], v[120:123], v[144:147]\n\t"
"s_waitcnt lgkmcnt(4)\n\t"
"v_mfma_f32_16x16x32_bf16 v[152:155], v[216:219], v[96:99], v[224:227]\n\t"
"ds_read_b128 v[204:207], v233 offset:8192\n\t"
"s_waitcnt lgkmcnt(4)\n\t"
"v_mfma_f32_16x16x32_bf16 v[152:155], v[220:223], v[104:107], v[152:155]\n\t"
"ds_read_b128 v[208:211], v232 offset:12288\n\t"
"s_waitcnt lgkmcnt(4)\n\t"
"v_mfma_f32_16x16x32_bf16 v[152:155], v[192:195], v[112:115], v[152:155]\n\t"
"ds_read_b128 v[212:215], v233 offset:12288\n\t"
"v_mfma_f32_16x16x32_bf16 v[156:159], v[192:195], v[116:119], v[228:231]\n\t"
"v_max3_f32 v236, v236, v148, v149\n\t"
"v_mfma_f32_16x16x32_bf16 v[156:159], v[220:223], v[108:111], v[156:159]\n\t"
"v_max3_f32 v236, v236, v150, v151\n\t"
"v_mfma_f32_16x16x32_bf16 v[156:159], v[216:219], v[100:103], v[156:159]\n\t"
"s_waitcnt lgkmcnt(4)\n\t"
"v_mfma_f32_16x16x32_bf16 v[156:159], v[196:199], v[124:127], v[156:159]\n\t"
"ds_read_b128 v[216:219], v232 offset:13312\n\t"
"s_waitcnt lgkmcnt(4)\n\t"
"v_mfma_f32_16x16x32_bf16 v[156:159], v[200:203], v[132:135], v[156:159]\n\t"
"ds_read_b128 v[220:223], v233 offset:13312\n\t"
"s_waitcnt lgkmcnt(4)\n\t"
"v_mfma_f32_16x16x32_bf16 v[156:159], v[204:207], v[140:143], v[156:159]\n\t"
"ds_read_b128 v[192:195], v232 offset:14336\n\t"
"v_mfma_f32_16x16x32_bf16 v[152:155], v[204:207], v[136:139], v[152:155]\n\t"
"v_max3_f32 v236, v236, v144, v145\n\t"
"v_mfma_f32_16x16x32_bf16 v[152:155], v[200:203], v[128:131], v[152:155]\n\t"
"v_max3_f32 v236, v236, v146, v147\n\t"
"v_mfma_f32_16x16x32_bf16 v[152:155], v[196:199], v[120:123], v[152:155]\n\t"
"s_waitcnt lgkmcnt(4)\n\t"
"v_mfma_f32_16x16x32_bf16 v[160:163], v[208:211], v[96:99], v[224:227]\n\t"
"ds_read_b128 v[196:199], v233 offset:14336\n\t"
"s_waitcnt lgkmcnt(4)\n\t"
"v_mfma_f32_16x16x32_bf16 v[160:163], v[212:215], v[104:107], v[160:163]\n\t"
"ds_read_b128 v[200:203], v232 offset:18432\n\t"
"s_waitcnt lgkmcnt(4)\n\t"
"v_mfma_f32_16x16x32_bf16 v[160:163], v[216:219], v[112:115], v[160:163]\n\t"
"ds_read_b128 v[204:207], v233 offset:18432\n\t"
"v_mfma_f32_16x16x32_bf16 v[164:167], v[216:219], v[116:119], v[228:231]\n\t"
"v_max3_f32 v236, v236, v156, v157\n\t"
"v_mfma_f32_16x16x32_bf16 v[164:167], v[212:215], v[108:111], v[164:167]\n\t"
"v_max3_f32 v236, v236, v158, v159\n\t"
"v_mfma_f32_16x16x32_bf16 v[164:167], v[208:211], v[100:103], v[164:167]\n\t"
"s_waitcnt lgkmcnt(4)\n\t"
"v_mfma_f32_16x16x32_bf16 v[164:167], v[220:223], v[124:127], v[164:167]\n\t"
"ds_read_b128 v[208:211], v232 offset:19456\n\t"
"s_waitcnt lgkmcnt(4)\n\t"
"v_mfma_f32_16x16x32_bf16 v[164:167], v[192:195], v[132:135], v[164:167]\n\t"
"ds_read_b128 v[212:215], v233 offset:19456\n\t"
"s_waitcnt lgkmcnt(4)\n\t"
"v_mfma_f32_16x16x32_bf16 v[164:167], v[196:199], v[140:143], v[164:167]\n\t"
"ds_read_b128 v[216:219], v232 offset:20480\n\t"
"v_mfma_f32_16x16x32_bf16 v[160:163], v[196:199], v[136:139], v[160:163]\n\t"
"v_max3_f32 v236, v236, v152, v153\n\t"
"v_mfma_f32_16x16x32_bf16 v[160:163], v[192:195], v[128:131], v[160:163]\n\t"
"v_max3_f32 v236, v236, v154, v155\n\t"
"v_mfma_f32_16x16x32_bf16 v[160:163], v[220:223], v[120:123], v[160:163]\n\t"
"s_waitcnt lgkmcnt(4)\n\t"
"v_mfma_f32_16x16x32_bf16 v[168:171], v[200:203], v[96:99], v[224:227]\n\t"
"ds_read_b128 v[220:223], v233 offset:20480\n\t"
"v_add_u32 v232, s77, v251\n\t"
"s_waitcnt lgkmcnt(4)\n\t"
"v_mfma_f32_16x16x32_bf16 v[168:171], v[204:207], v[104:107], v[168:171]\n\t"
"v_add_u32 v233, s77, v252\n\t"
"s_add_u32 s77, s77, 24640\n\t"
"s_waitcnt lgkmcnt(3)\n\t"
"v_mfma_f32_16x16x32_bf16 v[168:171], v[208:211], v[112:115], v[168:171]\n\t"
"s_cmp_ge_u32 s77, 147840\n\t"
"s_cselect_b32 s77, 0, s77\n\t"
"v_mfma_f32_16x16x32_bf16 v[172:175], v[208:211], v[116:119], v[228:231]\n\t"
"ds_read_b64_tr_b16 v[192:193], v232 offset:0\n\t"
"ds_read_b64_tr_b16 v[194:195], v232 offset:6144\n\t"
"v_mfma_f32_16x16x32_bf16 v[172:175], v[204:207], v[108:111], v[172:175]\n\t"
"ds_read_b64_tr_b16 v[196:197], v232 offset:12288\n\t"
"ds_read_b64_tr_b16 v[198:199], v232 offset:18432\n\t"
"v_mfma_f32_16x16x32_bf16 v[172:175], v[200:203], v[100:103], v[172:175]\n\t"
"v_max3_f32 v236, v236, v164, v165\n\t"
"v_max3_f32 v236, v236, v166, v167\n\t"
"s_waitcnt lgkmcnt(6)\n\t"
"v_mfma_f32_16x16x32_bf16 v[172:175], v[212:215], v[124:127], v[172:175]\n\t"
"ds_read_b64_tr_b16 v[200:201], v233 offset:0\n\t"
"ds_read_b64_tr_b16 v[202:203], v233 offset:6144\n\t"
"s_waitcnt lgkmcnt(7)\n\t"
"v_mfma_f32_16x16x32_bf16 v[172:175], v[216:219], v[132:135], v[172:175]\n\t"
"ds_read_b64_tr_b16 v[204:205], v233 offset:12288\n\t"
"s_waitcnt lgkmcnt(7)\n\t"
"v_mfma_f32_16x16x32_bf16 v[172:175], v[220:223], v[140:143], v[172:175]\n\t"
"ds_read_b64_tr_b16 v[206:207], v233 offset:18432\n\t"
"v_mfma_f32_16x16x32_bf16 v[168:171], v[220:223], v[136:139], v[168:171]\n\t"
"ds_read_b64_tr_b16 v[208:209], v232 offset:512\n\t"
"ds_read_b64_tr_b16 v[210:211], v232 offset:6656\n\t"
"v_mfma_f32_16x16x32_bf16 v[168:171], v[216:219], v[128:131], v[168:171]\n\t"
"v_max3_f32 v236, v236, v160, v161\n\t"
"v_mfma_f32_16x16x32_bf16 v[168:171], v[212:215], v[120:123], v[168:171]\n\t"
"v_max3_f32 v236, v236, v162, v163\n\t"
"ds_read_b64_tr_b16 v[212:213], v232 offset:12800\n\t"
"ds_read_b64_tr_b16 v[214:215], v232 offset:18944\n\t"
"s_setprio 0\n\t"
"s_cmp_lg_u32 s71, 0\n\t"
"s_cbranch_scc1 LB1_%=\n\t"
"s_waitcnt vmcnt(2)\n\t"
"s_barrier\n\t"
"LB1_%=:\n\t"
"s_add_u32 m0, s78, 0\n\t"
"s_add_u32 s76, s82, 0\n\t"
"buffer_load_dwordx4 v244, s[44:47], s76 offen lds\n\t"
"v_max3_f32 v236, v236, v172, v173\n\t"
"v_max3_f32 v236, v236, v174, v175\n\t"
"v_max3_f32 v236, v236, v168, v169\n\t"
"v_max3_f32 v236, v236, v170, v171\n\t"
"v_cmp_lt_f32 vcc, s80, v236\n\t"
"s_cbranch_vccnz LRARE_%=\n\t"
"LSM_%=:\n\t"
"v_exp_f32 v144, v144\n\t"
"v_exp_f32 v145, v145\n\t"
"v_add_f32 v234, v234, v144\n\t"
"v_exp_f32 v146, v146\n\t"
"v_add_f32 v234, v234, v145\n\t"
"v_exp_f32 v147, v147\n\t"
"v_add_f32 v234, v234, v146\n\t"
"v_exp_f32 v152, v152\n\t"
"v_add_f32 v234, v234, v147\n\t"
"v_exp_f32 v153, v153\n\t"
"v_add_f32 v234, v234, v152\n\t"
"v_exp_f32 v154, v154\n\t"
"v_add_f32 v234, v234, v153\n\t"
"s_add_u32 m0, s78, 1024\n\t"
"s_add_u32 s76, s82, 128\n\t"
"buffer_load_dwordx4 v244, s[44:47], s76 offen lds\n\t"
"v_exp_f32 v155, v155\n\t"
"v_add_f32 v234, v234, v154\n\t"
"v_cvt_pk_bf16_f32 v176, v144, v145\n\t"
"v_add_f32 v234, v234, v155\n\t"
"v_cvt_pk_bf16_f32 v177, v146, v147\n\t"
"v_cvt_pk_bf16_f32 v178, v152, v153\n\t"
"v_cvt_pk_bf16_f32 v179, v154, v155\n\t"
"v_exp_f32 v148, v148\n\t"
"v_exp_f32 v149, v149\n\t"
"v_add_f32 v235, v235, v148\n\t"
"v_exp_f32 v150, v150\n\t"
"v_add_f32 v235, v235, v149\n\t"
"s_add_u32 m0, s78, 2048\n\t"
"s_add_u32 s76, s82, 256\n\t"
"buffer_load_dwordx4 v244, s[44:47], s76 offen lds\n\t"
"v_exp_f32 v151, v151\n\t"
"v_add_f32 v235, v235, v150\n\t"
"v_exp_f32 v156, v156\n\t"
"v_add_f32 v235, v235, v151\n\t"
"v_exp_f32 v157, v157\n\t"
"v_add_f32 v235, v235, v156\n\t"
"v_exp_f32 v158, v158\n\t"
"v_add_f32 v235, v235, v157\n\t"
"s_add_u32 m0, s78, 3072\n\t"
"s_add_u32 s76, s82, 3072\n\t"
"buffer_load_dwordx4 v255, s[44:47], s76 offen lds\n\t"
"v_exp_f32 v159, v159\n\t"
"v_add_f32 v235, v235, v158\n\t"
"v_cvt_pk_bf16_f32 v180, v148, v149\n\t"
"v_add_f32 v235, v235, v159\n\t"
"v_cvt_pk_bf16_f32 v181, v150, v151\n\t"
"v_cvt_pk_bf16_f32 v182, v156, v157\n\t"
"v_cvt_pk_bf16_f32 v183, v158, v159\n\t"
"v_exp_f32 v160, v160\n\t"
"v_exp_f32 v161, v161\n\t"
"v_add_f32 v234, v234, v160\n\t"
"v_exp_f32 v162, v162\n\t"
"v_add_f32 v234, v234, v161\n\t"
"s_add_u32 m0, s78, 4096\n\t"
"s_add_u32 s76, s82, 3200\n\t"
"buffer_load_dwordx4 v255, s[44:47], s76 offen lds\n\t"
"v_exp_f32 v163, v163\n\t"
"v_add_f32 v234, v234, v162\n\t"
"v_exp_f32 v168, v168\n\t"
"v_add_f32 v234, v234, v163\n\t"
"v_exp_f32 v169, v169\n\t"
"v_add_f32 v234, v234, v168\n\t"
"v_exp_f32 v170, v170\n\t"
"v_add_f32 v234, v234, v169\n\t"
"s_add_u32 m0, s78, 5120\n\t"
"s_add_u32 s76, s82, 3328\n\t"
"buffer_load_dwordx4 v255, s[44:47], s76 offen lds\n\t"
"v_exp_f32 v171, v171\n\t"
"v_add_f32 v234, v234, v170\n\t"
"v_cvt_pk_bf16_f32 v184, v160, v161\n\t"
"v_add_f32 v234, v234, v171\n\t"
"v_cvt_pk_bf16_f32 v185, v162, v163\n\t"
"v_cvt_pk_bf16_f32 v186, v168, v169\n\t"
"v_cvt_pk_bf16_f32 v187, v170, v171\n\t"
"v_exp_f32 v164, v164\n\t"
"v_exp_f32 v165, v165\n\t"
"v_add_f32 v235, v235, v164\n\t"
"v_exp_f32 v166, v166\n\t"
"v_add_f32 v235, v235, v165\n\t"
"s_waitcnt vmcnt(6)\n\t"
"buffer_load_dword v242, v248, s[56:59], s22 offen offset:0 sc1\n\t"
"buffer_load_dword v245, v243, s[56:59], s23 offen offset:0 sc1\n\t"
"v_exp_f32 v167, v167\n\t"
"v_add_f32 v235, v235, v166\n\t"
"v_exp_f32 v172, v172\n\t"
"v_add_f32 v235, v235, v167\n\t"
"v_exp_f32 v173, v173\n\t"
"v_add_f32 v235, v235, v172\n\t"
"v_exp_f32 v174, v174\n\t"
"v_add_f32 v235, v235, v173\n\t"
"v_exp_f32 v175, v175\n\t"
"v_add_f32 v235, v235, v174\n\t"
"v_cvt_pk_bf16_f32 v188, v164, v165\n\t"
"v_add_f32 v235, v235, v175\n\t"
"v_cvt_pk_bf16_f32 v189, v166, v167\n\t"
"v_cvt_pk_bf16_f32 v190, v172, v173\n\t"
"v_cvt_pk_bf16_f32 v191, v174, v175\n\t"
"s_add_u32 s78, s78, 49280\n\t"
"s_cmp_ge_u32 s78, s88\n\t"
"s_cselect_b32 s76, 147840, 0\n\t"
"s_sub_u32 s78, s78, s76\n\t"
"s_cmp_eq_u32 s79, 5\n\t"
"s_cselect_b32 s78, 0x30000, s78\n\t"
"s_cselect_b32 s82, 0x10000000, s82\n\t"
"s_add_u32 s82, s82, 24576\n\t"
"s_cmp_eq_u32 s71, 0\n\t"
"s_cbranch_scc1 LB2_%=\n\t"
"s_waitcnt vmcnt(10)\n\t"
"s_barrier\n\t"
"LB2_%=:\n\t"
"s_waitcnt lgkmcnt(10)\n\t"
"v_mfma_f32_16x16x32_bf16 v[0:3], v[192:195], v[176:179], v[0:3]\n\t"
"ds_read_b64_tr_b16 v[216:217], v233 offset:512\n\t"
"s_waitcnt lgkmcnt(9)\n\t"
"v_mfma_f32_16x16x32_bf16 v[0:3], v[196:199], v[184:187], v[0:3]\n\t"
"ds_read_b64_tr_b16 v[218:219], v233 offset:6656\n\t"
"v_mfma_f32_16x16x32_bf16 v[4:7], v[196:199], v[188:191], v[4:7]\n\t"
"ds_read_b64_tr_b16 v[220:221], v233 offset:12800\n\t"
"v_mfma_f32_16x16x32_bf16 v[4:7], v[192:195], v[180:183], v[4:7]\n\t"
"ds_read_b64_tr_b16 v[222:223], v233 offset:18944\n\t"
"s_waitcnt lgkmcnt(10)\n\t"
"v_mfma_f32_16x16x32_bf16 v[8:11], v[200:203], v[176:179], v[8:11]\n\t"
"ds_read_b64_tr_b16 v[192:193], v232 offset:1024\n\t"
"s_waitcnt lgkmcnt(9)\n\t"
"v_mfma_f32_16x16x32_bf16 v[8:11], v[204:207], v[184:187], v[8:11]\n\t"
"ds_read_b64_tr_b16 v[194:195], v232 offset:7168\n\t"
"v_mfma_f32_16x16x32_bf16 v[12:15], v[204:207], v[188:191], v[12:15]\n\t"
"ds_read_b64_tr_b16 v[196:197], v232 offset:13312\n\t"
"v_mfma_f32_16x16x32_bf16 v[12:15], v[200:203], v[180:183], v[12:15]\n\t"
"ds_read_b64_tr_b16 v[198:199], v232 offset:19456\n\t"
"s_waitcnt lgkmcnt(10)\n\t"
"v_mfma_f32_16x16x32_bf16 v[16:19], v[208:211], v[176:179], v[16:19]\n\t"
"ds_read_b64_tr_b16 v[200:201], v233 offset:1024\n\t"
"s_waitcnt lgkmcnt(9)\n\t"
"v_mfma_f32_16x16x32_bf16 v[16:19], v[212:215], v[184:187], v[16:19]\n\t"
"ds_read_b64_tr_b16 v[202:203], v233 offset:7168\n\t"
"v_mfma_f32_16x16x32_bf16 v[20:23], v[212:215], v[188:191], v[20:23]\n\t"
"ds_read_b64_tr_b16 v[204:205], v233 offset:13312\n\t"
"v_mfma_f32_16x16x32_bf16 v[20:23], v[208:211], v[180:183], v[20:23]\n\t"
"ds_read_b64_tr_b16 v[206:207], v233 offset:19456\n\t"
"s_waitcnt lgkmcnt(10)\n\t"
"v_mfma_f32_16x16x32_bf16 v[24:27], v[216:219], v[176:179], v[24:27]\n\t"
"ds_read_b64_tr_b16 v[208:209], v232 offset:1536\n\t"
"s_waitcnt lgkmcnt(9)\n\t"
"v_mfma_f32_16x16x32_bf16 v[24:27], v[220:223], v[184:187], v[24:27]\n\t"
"ds_read_b64_tr_b16 v[210:211], v232 offset:7680\n\t"
"v_mfma_f32_16x16x32_bf16 v[28:31], v[220:223], v[188:191], v[28:31]\n\t"
"ds_read_b64_tr_b16 v[212:213], v232 offset:13824\n\t"
"v_mfma_f32_16x16x32_bf16 v[28:31], v[216:219], v[180:183], v[28:31]\n\t"
"ds_read_b64_tr_b16 v[214:215], v232 offset:19968\n\t"
"s_waitcnt lgkmcnt(10)\n\t"
"v_mfma_f32_16x16x32_bf16 v[32:35], v[192:195], v[176:179], v[32:35]\n\t"
"ds_read_b64_tr_b16 v[216:217], v233 offset:1536\n\t"
"s_waitcnt lgkmcnt(9)\n\t"
"v_mfma_f32_16x16x32_bf16 v[32:35], v[196:199], v[184:187], v[32:35]\n\t"
"ds_read_b64_tr_b16 v[218:219], v233 offset:7680\n\t"
"v_mfma_f32_16x16x32_bf16 v[36:39], v[196:199], v[188:191], v[36:39]\n\t"
"ds_read_b64_tr_b16 v[220:221], v233 offset:13824\n\t"
"v_mfma_f32_16x16x32_bf16 v[36:39], v[192:195], v[180:183], v[36:39]\n\t"
"ds_read_b64_tr_b16 v[222:223], v233 offset:19968\n\t"
"s_waitcnt lgkmcnt(10)\n\t"
"v_mfma_f32_16x16x32_bf16 v[40:43], v[200:203], v[176:179], v[40:43]\n\t"
"ds_read_b64_tr_b16 v[192:193], v232 offset:2048\n\t"
"s_waitcnt lgkmcnt(9)\n\t"
"v_mfma_f32_16x16x32_bf16 v[40:43], v[204:207], v[184:187], v[40:43]\n\t"
"ds_read_b64_tr_b16 v[194:195], v232 offset:8192\n\t"
"v_mfma_f32_16x16x32_bf16 v[44:47], v[204:207], v[188:191], v[44:47]\n\t"
"ds_read_b64_tr_b16 v[196:197], v232 offset:14336\n\t"
"v_mfma_f32_16x16x32_bf16 v[44:47], v[200:203], v[180:183], v[44:47]\n\t"
"ds_read_b64_tr_b16 v[198:199], v232 offset:20480\n\t"
"s_waitcnt lgkmcnt(10)\n\t"
"v_mfma_f32_16x16x32_bf16 v[48:51], v[208:211], v[176:179], v[48:51]\n\t"
"ds_read_b64_tr_b16 v[200:201], v233 offset:2048\n\t"
"s_waitcnt lgkmcnt(9)\n\t"
"v_mfma_f32_16x16x32_bf16 v[48:51], v[212:215], v[184:187], v[48:51]\n\t"
"ds_read_b64_tr_b16 v[202:203], v233 offset:8192\n\t"
"v_mfma_f32_16x16x32_bf16 v[52:55], v[212:215], v[188:191], v[52:55]\n\t"
"ds_read_b64_tr_b16 v[204:205], v233 offset:14336\n\t"
"v_mfma_f32_16x16x32_bf16 v[52:55], v[208:211], v[180:183], v[52:55]\n\t"
"ds_read_b64_tr_b16 v[206:207], v233 offset:20480\n\t"
"s_waitcnt lgkmcnt(10)\n\t"
"v_mfma_f32_16x16x32_bf16 v[56:59], v[216:219], v[176:179], v[56:59]\n\t"
"ds_read_b64_tr_b16 v[208:209], v232 offset:2560\n\t"
"s_waitcnt lgkmcnt(9)\n\t"
"v_mfma_f32_16x16x32_bf16 v[56:59], v[220:223], v[184:187], v[56:59]\n\t"
"ds_read_b64_tr_b16 v[210:211], v232 offset:8704\n\t"
"v_mfma_f32_16x16x32_bf16 v[60:63], v[220:223], v[188:191], v[60:63]\n\t"
"ds_read_b64_tr_b16 v[212:213], v232 offset:14848\n\t"
"v_mfma_f32_16x16x32_bf16 v[60:63], v[216:219], v[180:183], v[60:63]\n\t"
"ds_read_b64_tr_b16 v[214:215], v232 offset:20992\n\t"
"s_waitcnt lgkmcnt(10)\n\t"
"v_mfma_f32_16x16x32_bf16 v[64:67], v[192:195], v[176:179], v[64:67]\n\t"
"ds_read_b64_tr_b16 v[216:217], v233 offset:2560\n\t"
"s_waitcnt lgkmcnt(9)\n\t"
"v_mfma_f32_16x16x32_bf16 v[64:67], v[196:199], v[184:187], v[64:67]\n\t"
"ds_read_b64_tr_b16 v[218:219], v233 offset:8704\n\t"
"v_mfma_f32_16x16x32_bf16 v[68:71], v[196:199], v[188:191], v[68:71]\n\t"
"ds_read_b64_tr_b16 v[220:221], v233 offset:14848\n\t"
"v_mfma_f32_16x16x32_bf16 v[68:71], v[192:195], v[180:183], v[68:71]\n\t"
"ds_read_b64_tr_b16 v[222:223], v233 offset:20992\n\t"
"s_waitcnt lgkmcnt(10)\n\t"
"v_mfma_f32_16x16x32_bf16 v[72:75], v[200:203], v[176:179], v[72:75]\n\t"
"v_add_u32 v232, s77, v249\n\t"
"v_add_u32 v233, s77, v250\n\t"
"s_waitcnt lgkmcnt(8)\n\t"
"v_mfma_f32_16x16x32_bf16 v[72:75], v[204:207], v[184:187], v[72:75]\n\t"
"s_add_u32 s77, s77, 24640\n\t"
"s_cmp_ge_u32 s77, 147840\n\t"
"v_mfma_f32_16x16x32_bf16 v[76:79], v[204:207], v[188:191], v[76:79]\n\t"
"s_cselect_b32 s77, 0, s77\n\t"
"ds_read_b128 v[192:195], v232 offset:0\n\t"
"v_mfma_f32_16x16x32_bf16 v[76:79], v[200:203], v[180:183], v[76:79]\n\t"
"ds_read_b128 v[196:199], v233 offset:0\n\t"
"s_waitcnt lgkmcnt(8)\n\t"
"v_mfma_f32_16x16x32_bf16 v[80:83], v[208:211], v[176:179], v[80:83]\n\t"
"ds_read_b128 v[200:203], v232 offset:1024\n\t"
"s_waitcnt lgkmcnt(7)\n\t"
"v_mfma_f32_16x16x32_bf16 v[80:83], v[212:215], v[184:187], v[80:83]\n\t"
"v_mfma_f32_16x16x32_bf16 v[84:87], v[212:215], v[188:191], v[84:87]\n\t"
"ds_read_b128 v[204:207], v233 offset:1024\n\t"
"v_mfma_f32_16x16x32_bf16 v[84:87], v[208:211], v[180:183], v[84:87]\n\t"
"s_waitcnt lgkmcnt(6)\n\t"
"v_mfma_f32_16x16x32_bf16 v[88:91], v[216:219], v[176:179], v[88:91]\n\t"
"ds_read_b128 v[208:211], v232 offset:2048\n\t"
"s_waitcnt lgkmcnt(5)\n\t"
"v_mfma_f32_16x16x32_bf16 v[88:91], v[220:223], v[184:187], v[88:91]\n\t"
"v_mfma_f32_16x16x32_bf16 v[92:95], v[220:223], v[188:191], v[92:95]\n\t"
"ds_read_b128 v[212:215], v233 offset:2048\n\t"
"v_mfma_f32_16x16x32_bf16 v[92:95], v[216:219], v[180:183], v[92:95]\n\t"
"s_add_u32 s79, s79, 1\n\t"
"s_cmp_lt_u32 s79, 7\n\t"
"s_cbranch_scc1 LLOOP_%=\n\t"
"LLOOPL_%=:\n\t"
"s_setprio 1\n\t"
"v_mov_b32 v236, 0xff800000\n\t"
"s_waitcnt lgkmcnt(5)\n\t"
"v_mfma_f32_16x16x32_bf16 v[144:147], v[192:195], v[96:99], v[224:227]\n\t"
"ds_read_b128 v[216:219], v232 offset:6144\n\t"
"s_waitcnt lgkmcnt(5)\n\t"
"v_mfma_f32_16x16x32_bf16 v[144:147], v[196:199], v[104:107], v[144:147]\n\t"
"s_waitcnt lgkmcnt(4)\n\t"
"v_mfma_f32_16x16x32_bf16 v[144:147], v[200:203], v[112:115], v[144:147]\n\t"
"v_mfma_f32_16x16x32_bf16 v[148:151], v[200:203], v[116:119], v[228:231]\n\t"
"ds_read_b128 v[220:223], v233 offset:6144\n\t"
"v_mfma_f32_16x16x32_bf16 v[148:151], v[196:199], v[108:111], v[148:151]\n\t"
"v_mfma_f32_16x16x32_bf16 v[148:151], v[192:195], v[100:103], v[148:151]\n\t"
"s_waitcnt vmcnt(1)\n\t"
"v_cmp_eq_u32 vcc, 0, v242\n\t"
"s_cbranch_vccnz LPW_%=\n\t"
"LPWR_%=:\n\t"
"s_waitcnt vmcnt(0)\n\t"
"v_cmp_eq_u32 vcc, 0, v245\n\t"
"s_cbranch_vccnz LPH_%=\n\t"
"LPHR_%=:\n\t"
"s_waitcnt lgkmcnt(4)\n\t"
"v_mfma_f32_16x16x32_bf16 v[148:151], v[204:207], v[124:127], v[148:151]\n\t"
"ds_read_b128 v[192:195], v232 offset:7168\n\t"
"s_waitcnt lgkmcnt(4)\n\t"
"v_mfma_f32_16x16x32_bf16 v[148:151], v[208:211], v[132:135], v[148:151]\n\t"
"s_waitcnt lgkmcnt(3)\n\t"
"v_mfma_f32_16x16x32_bf16 v[148:151], v[212:215], v[140:143], v[148:151]\n\t"
"ds_read_b128 v[196:199], v233 offset:7168\n\t"
"v_mfma_f32_16x16x32_bf16 v[144:147], v[212:215], v[136:139], v[144:147]\n\t"
"v_mfma_f32_16x16x32_bf16 v[144:147], v[208:211], v[128:131], v[144:147]\n\t"
"ds_read_b128 v[200:203], v232 offset:8192\n\t"
"v_mfma_f32_16x16x32_bf16 v[144:147], v[204:207], v[120:123], v[144:147]\n\t"
"s_cmp_eq_u32 s71, 0\n\t"
"s_cbranch_scc1 LE0_%=\n\t"
"s_add_u32 m0, s12, 40960\n\t"
"s_add_u32 s76, s13, 0\n\t"
"buffer_load_dwordx4 v240, s[56:59], s76 offen sc1 lds\n\t"
"s_add_u32 m0, s12, 61440\n\t"
"s_add_u32 s76, s13, 81920\n\t"
"buffer_load_dwordx4 v240, s[56:59], s76 offen sc1 lds\n\t"
"LE0_%=:\n\t"
"s_waitcnt lgkmcnt(4)\n\t"
"v_mfma_f32_16x16x32_bf16 v[152:155], v[216:219], v[96:99], v[224:227]\n\t"
"ds_read_b128 v[204:207], v233 offset:8192\n\t"
"s_waitcnt lgkmcnt(4)\n\t"
"v_mfma_f32_16x16x32_bf16 v[152:155], v[220:223], v[104:107], v[152:155]\n\t"
"ds_read_b128 v[208:211], v232 offset:12288\n\t"
"s_waitcnt lgkmcnt(4)\n\t"
"v_mfma_f32_16x16x32_bf16 v[152:155], v[192:195], v[112:115], v[152:155]\n\t"
"ds_read_b128 v[212:215], v233 offset:12288\n\t"
"v_mfma_f32_16x16x32_bf16 v[156:159], v[192:195], v[116:119], v[228:231]\n\t"
"v_max3_f32 v236, v236, v148, v149\n\t"
"v_mfma_f32_16x16x32_bf16 v[156:159], v[220:223], v[108:111], v[156:159]\n\t"
"v_max3_f32 v236, v236, v150, v151\n\t"
"v_mfma_f32_16x16x32_bf16 v[156:159], v[216:219], v[100:103], v[156:159]\n\t"
"s_cmp_eq_u32 s71, 0\n\t"
"s_cbranch_scc1 LE1_%=\n\t"
"s_add_u32 m0, s12, 41984\n\t"
"s_add_u32 s76, s13, 1024\n\t"
"buffer_load_dwordx4 v240, s[56:59], s76 offen sc1 lds\n\t"
"s_add_u32 m0, s12, 62464\n\t"
"s_add_u32 s76, s13, 82944\n\t"
"buffer_load_dwordx4 v240, s[56:59], s76 offen sc1 lds\n\t"
"LE1_%=:\n\t"
"s_waitcnt lgkmcnt(4)\n\t"
"v_mfma_f32_16x16x32_bf16 v[156:159], v[196:199], v[124:127], v[156:159]\n\t"
"ds_read_b128 v[216:219], v232 offset:13312\n\t"
"s_waitcnt lgkmcnt(4)\n\t"
"v_mfma_f32_16x16x32_bf16 v[156:159], v[200:203], v[132:135], v[156:159]\n\t"
"ds_read_b128 v[220:223], v233 offset:13312\n\t"
"s_waitcnt lgkmcnt(4)\n\t"
"v_mfma_f32_16x16x32_bf16 v[156:159], v[204:207], v[140:143], v[156:159]\n\t"
"ds_read_b128 v[192:195], v232 offset:14336\n\t"
"v_mfma_f32_16x16x32_bf16 v[152:155], v[204:207], v[136:139], v[152:155]\n\t"
"v_max3_f32 v236, v236, v144, v145\n\t"
"v_mfma_f32_16x16x32_bf16 v[152:155], v[200:203], v[128:131], v[152:155]\n\t"
"v_max3_f32 v236, v236, v146, v147\n\t"
"v_mfma_f32_16x16x32_bf16 v[152:155], v[196:199], v[120:123], v[152:155]\n\t"
"s_cmp_eq_u32 s71, 0\n\t"
"s_cbranch_scc1 LE2_%=\n\t"
"s_add_u32 m0, s12, 43008\n\t"
"s_add_u32 s76, s13, 2048\n\t"
"buffer_load_dwordx4 v240, s[56:59], s76 offen sc1 lds\n\t"
"s_add_u32 m0, s12, 63488\n\t"
"s_add_u32 s76, s13, 83968\n\t"
"buffer_load_dwordx4 v240, s[56:59], s76 offen sc1 lds\n\t"
"LE2_%=:\n\t"
"s_waitcnt lgkmcnt(4)\n\t"
"v_mfma_f32_16x16x32_bf16 v[160:163], v[208:211], v[96:99], v[224:227]\n\t"
"ds_read_b128 v[196:199], v233 offset:14336\n\t"
"s_waitcnt lgkmcnt(4)\n\t"
"v_mfma_f32_16x16x32_bf16 v[160:163], v[212:215], v[104:107], v[160:163]\n\t"
"ds_read_b128 v[200:203], v232 offset:18432\n\t"
"s_waitcnt lgkmcnt(4)\n\t"
"v_mfma_f32_16x16x32_bf16 v[160:163], v[216:219], v[112:115], v[160:163]\n\t"
"ds_read_b128 v[204:207], v233 offset:18432\n\t"
"v_mfma_f32_16x16x32_bf16 v[164:167], v[216:219], v[116:119], v[228:231]\n\t"
"v_max3_f32 v236, v236, v156, v157\n\t"
"v_mfma_f32_16x16x32_bf16 v[164:167], v[212:215], v[108:111], v[164:167]\n\t"
"v_max3_f32 v236, v236, v158, v159\n\t"
"v_mfma_f32_16x16x32_bf16 v[164:167], v[208:211], v[100:103], v[164:167]\n\t"
"s_cmp_eq_u32 s71, 0\n\t"
"s_cbranch_scc1 LE3_%=\n\t"
"s_add_u32 m0, s12, 44032\n\t"
"s_add_u32 s76, s13, 3072\n\t"
"buffer_load_dwordx4 v240, s[56:59], s76 offen sc1 lds\n\t"
"s_add_u32 m0, s12, 64512\n\t"
"s_add_u32 s76, s13, 84992\n\t"
"buffer_load_dwordx4 v240, s[56:59], s76 offen sc1 lds\n\t"
"LE3_%=:\n\t"
"s_waitcnt lgkmcnt(4)\n\t"
"v_mfma_f32_16x16x32_bf16 v[164:167], v[220:223], v[124:127], v[164:167]\n\t"
"ds_read_b128 v[208:211], v232 offset:19456\n\t"
"s_waitcnt lgkmcnt(4)\n\t"
"v_mfma_f32_16x16x32_bf16 v[164:167], v[192:195], v[132:135], v[164:167]\n\t"
"ds_read_b128 v[212:215], v233 offset:19456\n\t"
"s_waitcnt lgkmcnt(4)\n\t"
"v_mfma_f32_16x16x32_bf16 v[164:167], v[196:199], v[140:143], v[164:167]\n\t"
"ds_read_b128 v[216:219], v232 offset:20480\n\t"
"v_mfma_f32_16x16x32_bf16 v[160:163], v[196:199], v[136:139], v[160:163]\n\t"
"v_max3_f32 v236, v236, v152, v153\n\t"
"v_mfma_f32_16x16x32_bf16 v[160:163], v[192:195], v[128:131], v[160:163]\n\t"
"v_max3_f32 v236, v236, v154, v155\n\t"
"v_mfma_f32_16x16x32_bf16 v[160:163], v[220:223], v[120:123], v[160:163]\n\t"
"s_cmp_eq_u32 s71, 0\n\t"
"s_cbranch_scc1 LE4_%=\n\t"
"s_add_u32 m0, s12, 45056\n\t"
"s_add_u32 s76, s13, 4096\n\t"
"buffer_load_dwordx4 v240, s[56:59], s76 offen sc1 lds\n\t"
"s_add_u32 m0, s12, 65536\n\t"
"s_add_u32 s76, s13, 86016\n\t"
"buffer_load_dwordx4 v240, s[56:59], s76 offen sc1 lds\n\t"
"LE4_%=:\n\t"
"s_waitcnt lgkmcnt(4)\n\t"
"v_mfma_f32_16x16x32_bf16 v[168:171], v[200:203], v[96:99], v[224:227]\n\t"
"ds_read_b128 v[220:223], v233 offset:20480\n\t"
"v_add_u32 v232, s77, v251\n\t"
"s_waitcnt lgkmcnt(4)\n\t"
"v_mfma_f32_16x16x32_bf16 v[168:171], v[204:207], v[104:107], v[168:171]\n\t"
"v_add_u32 v233, s77, v252\n\t"
"s_add_u32 s77, s77, 24640\n\t"
"s_waitcnt lgkmcnt(3)\n\t"
"v_mfma_f32_16x16x32_bf16 v[168:171], v[208:211], v[112:115], v[168:171]\n\t"
"s_cmp_ge_u32 s77, 147840\n\t"
"s_cselect_b32 s77, 0, s77\n\t"
"v_mfma_f32_16x16x32_bf16 v[172:175], v[208:211], v[116:119], v[228:231]\n\t"
"ds_read_b64_tr_b16 v[192:193], v232 offset:0\n\t"
"ds_read_b64_tr_b16 v[194:195], v232 offset:6144\n\t"
"v_mfma_f32_16x16x32_bf16 v[172:175], v[204:207], v[108:111], v[172:175]\n\t"
"ds_read_b64_tr_b16 v[196:197], v232 offset:12288\n\t"
"ds_read_b64_tr_b16 v[198:199], v232 offset:18432\n\t"
"v_mfma_f32_16x16x32_bf16 v[172:175], v[200:203], v[100:103], v[172:175]\n\t"
"v_max3_f32 v236, v236, v164, v165\n\t"
"v_max3_f32 v236, v236, v166, v167\n\t"
"s_waitcnt lgkmcnt(6)\n\t"
"v_mfma_f32_16x16x32_bf16 v[172:175], v[212:215], v[124:127], v[172:175]\n\t"
"ds_read_b64_tr_b16 v[200:201], v233 offset:0\n\t"
"ds_read_b64_tr_b16 v[202:203], v233 offset:6144\n\t"
"s_waitcnt lgkmcnt(7)\n\t"
"v_mfma_f32_16x16x32_bf16 v[172:175], v[216:219], v[132:135], v[172:175]\n\t"
"ds_read_b64_tr_b16 v[204:205], v233 offset:12288\n\t"
"s_waitcnt lgkmcnt(7)\n\t"
"v_mfma_f32_16x16x32_bf16 v[172:175], v[220:223], v[140:143], v[172:175]\n\t"
"ds_read_b64_tr_b16 v[206:207], v233 offset:18432\n\t"
"v_mfma_f32_16x16x32_bf16 v[168:171], v[220:223], v[136:139], v[168:171]\n\t"
"ds_read_b64_tr_b16 v[208:209], v232 offset:512\n\t"
"ds_read_b64_tr_b16 v[210:211], v232 offset:6656\n\t"
"v_mfma_f32_16x16x32_bf16 v[168:171], v[216:219], v[128:131], v[168:171]\n\t"
"v_max3_f32 v236, v236, v160, v161\n\t"
"v_mfma_f32_16x16x32_bf16 v[168:171], v[212:215], v[120:123], v[168:171]\n\t"
"v_max3_f32 v236, v236, v162, v163\n\t"
"ds_read_b64_tr_b16 v[212:213], v232 offset:12800\n\t"
"ds_read_b64_tr_b16 v[214:215], v232 offset:18944\n\t"
"s_setprio 0\n\t"
"s_cmp_lg_u32 s71, 0\n\t"
"s_cbranch_scc1 LB1L_%=\n\t"
"s_barrier\n\t"
"LB1L_%=:\n\t"
"s_add_u32 s87, s14, 0\n\t"
"buffer_load_dwordx2 v[96:97], v247, s[56:59], s87 offen offset:0 sc1\n\t"
"buffer_load_dwordx2 v[98:99], v247, s[56:59], s87 offen offset:512 sc1\n\t"
"v_max3_f32 v236, v236, v172, v173\n\t"
"v_max3_f32 v236, v236, v174, v175\n\t"
"v_max3_f32 v236, v236, v168, v169\n\t"
"v_max3_f32 v236, v236, v170, v171\n\t"
"v_cmp_lt_f32 vcc, s80, v236\n\t"
"s_cbranch_vccnz LRARE_%=\n\t"
"LSML_%=:\n\t"
"v_exp_f32 v144, v144\n\t"
"v_exp_f32 v145, v145\n\t"
"v_add_f32 v234, v234, v144\n\t"
"v_exp_f32 v146, v146\n\t"
"v_add_f32 v234, v234, v145\n\t"
"s_add_u32 s87, s15, 0\n\t"
"buffer_load_dwordx2 v[100:101], v247, s[56:59], s87 offen offset:0 sc1\n\t"
"buffer_load_dwordx2 v[102:103], v247, s[56:59], s87 offen offset:512 sc1\n\t"
"v_exp_f32 v147, v147\n\t"
"v_add_f32 v234, v234, v146\n\t"
"v_exp_f32 v152, v152\n\t"
"v_add_f32 v234, v234, v147\n\t"
"v_exp_f32 v153, v153\n\t"
"v_add_f32 v234, v234, v152\n\t"
"v_exp_f32 v154, v154\n\t"
"v_add_f32 v234, v234, v153\n\t"
"s_add_u32 s87, s14, 2048\n\t"
"buffer_load_dwordx2 v[104:105], v247, s[56:59], s87 offen offset:0 sc1\n\t"
"buffer_load_dwordx2 v[106:107], v247, s[56:59], s87 offen offset:512 sc1\n\t"
"v_exp_f32 v155, v155\n\t"
"v_add_f32 v234, v234, v154\n\t"
"v_cvt_pk_bf16_f32 v176, v144, v145\n\t"
"v_add_f32 v234, v234, v155\n\t"
"v_cvt_pk_bf16_f32 v177, v146, v147\n\t"
"v_cvt_pk_bf16_f32 v178, v152, v153\n\t"
"v_cvt_pk_bf16_f32 v179, v154, v155\n\t"
"v_exp_f32 v148, v148\n\t"
"v_exp_f32 v149, v149\n\t"
"v_add_f32 v235, v235, v148\n\t"
"v_exp_f32 v150, v150\n\t"
"v_add_f32 v235, v235, v149\n\t"
"s_add_u32 s87, s15, 2048\n\t"
"buffer_load_dwordx2 v[108:109], v247, s[56:59], s87 offen offset:0 sc1\n\t"
"buffer_load_dwordx2 v[110:111], v247, s[56:59], s87 offen offset:512 sc1\n\t"
"v_exp_f32 v151, v151\n\t"
"v_add_f32 v235, v235, v150\n\t"
"v_exp_f32 v156, v156\n\t"
"v_add_f32 v235, v235, v151\n\t"
"v_exp_f32 v157, v157\n\t"
"v_add_f32 v235, v235, v156\n\t"
"v_exp_f32 v158, v158\n\t"
"v_add_f32 v235, v235, v157\n\t"
"s_add_u32 s87, s14, 4096\n\t"
"buffer_load_dwordx2 v[112:113], v247, s[56:59], s87 offen offset:0 sc1\n\t"
"buffer_load_dwordx2 v[114:115], v247, s[56:59], s87 offen offset:512 sc1\n\t"
"v_exp_f32 v159, v159\n\t"
"v_add_f32 v235, v235, v158\n\t"
"v_cvt_pk_bf16_f32 v180, v148, v149\n\t"
"v_add_f32 v235, v235, v159\n\t"
"v_cvt_pk_bf16_f32 v181, v150, v151\n\t"
"v_cvt_pk_bf16_f32 v182, v156, v157\n\t"
"v_cvt_pk_bf16_f32 v183, v158, v159\n\t"
"v_exp_f32 v160, v160\n\t"
"v_exp_f32 v161, v161\n\t"
"v_add_f32 v234, v234, v160\n\t"
"v_exp_f32 v162, v162\n\t"
"v_add_f32 v234, v234, v161\n\t"
"s_add_u32 s87, s15, 4096\n\t"
"buffer_load_dwordx2 v[116:117], v247, s[56:59], s87 offen offset:0 sc1\n\t"
"buffer_load_dwordx2 v[118:119], v247, s[56:59], s87 offen offset:512 sc1\n\t"
"v_exp_f32 v163, v163\n\t"
"v_add_f32 v234, v234, v162\n\t"
"v_exp_f32 v168, v168\n\t"
"v_add_f32 v234, v234, v163\n\t"
"v_exp_f32 v169, v169\n\t"
"v_add_f32 v234, v234, v168\n\t"
"v_exp_f32 v170, v170\n\t"
"v_add_f32 v234, v234, v169\n\t"
"s_add_u32 s87, s14, 6144\n\t"
"buffer_load_dwordx2 v[120:121], v247, s[56:59], s87 offen offset:0 sc1\n\t"
"buffer_load_dwordx2 v[122:123], v247, s[56:59], s87 offen offset:512 sc1\n\t"
"v_exp_f32 v171, v171\n\t"
"v_add_f32 v234, v234, v170\n\t"
"v_cvt_pk_bf16_f32 v184, v160, v161\n\t"
"v_add_f32 v234, v234, v171\n\t"
"v_cvt_pk_bf16_f32 v185, v162, v163\n\t"
"v_cvt_pk_bf16_f32 v186, v168, v169\n\t"
"v_cvt_pk_bf16_f32 v187, v170, v171\n\t"
"v_exp_f32 v164, v164\n\t"
"v_exp_f32 v165, v165\n\t"
"v_add_f32 v235, v235, v164\n\t"
"v_exp_f32 v166, v166\n\t"
"v_add_f32 v235, v235, v165\n\t"
"s_add_u32 s87, s15, 6144\n\t"
"buffer_load_dwordx2 v[124:125], v247, s[56:59], s87 offen offset:0 sc1\n\t"
"buffer_load_dwordx2 v[126:127], v247, s[56:59], s87 offen offset:512 sc1\n\t"
"v_exp_f32 v167, v167\n\t"
"v_add_f32 v235, v235, v166\n\t"
"v_exp_f32 v172, v172\n\t"
"v_add_f32 v235, v235, v167\n\t"
"v_exp_f32 v173, v173\n\t"
"v_add_f32 v235, v235, v172\n\t"
"v_exp_f32 v174, v174\n\t"
"v_add_f32 v235, v235, v173\n\t"
"v_exp_f32 v175, v175\n\t"
"v_add_f32 v235, v235, v174\n\t"
"v_cvt_pk_bf16_f32 v188, v164, v165\n\t"
"v_add_f32 v235, v235, v175\n\t"
"v_cvt_pk_bf16_f32 v189, v166, v167\n\t"
"v_cvt_pk_bf16_f32 v190, v172, v173\n\t"
"v_cvt_pk_bf16_f32 v191, v174, v175\n\t"
"v_mov_b32 v239, v234\n\t"
"s_nop 1\n\t"
"v_permlane16_swap_b32 v234, v239\n\t"
"s_nop 0\n\t"
"v_add_f32 v234, v234, v239\n\t"
"v_mov_b32 v239, v234\n\t"
"s_nop 1\n\t"
"v_permlane32_swap_b32 v234, v239\n\t"
"s_nop 0\n\t"
"v_add_f32 v234, v234, v239\n\t"
"v_rcp_f32 v234, v234\n\t"
"v_mov_b32 v239, v235\n\t"
"s_nop 1\n\t"
"v_permlane16_swap_b32 v235, v239\n\t"
"s_nop 0\n\t"
"v_add_f32 v235, v235, v239\n\t"
"v_mov_b32 v239, v235\n\t"
"s_nop 1\n\t"
"v_permlane32_swap_b32 v235, v239\n\t"
"s_nop 0\n\t"
"v_add_f32 v235, v235, v239\n\t"
"v_rcp_f32 v235, v235\n\t"
"s_cmp_eq_u32 s71, 0\n\t"
"s_cbranch_scc1 LB2L_%=\n\t"
"s_waitcnt vmcnt(16)\n\t"
"s_barrier\n\t"
"LB2L_%=:\n\t"
"s_setprio 1\n\t"
"s_waitcnt lgkmcnt(10)\n\t"
"v_mfma_f32_16x16x32_bf16 v[0:3], v[192:195], v[176:179], v[0:3]\n\t"
"ds_read_b64_tr_b16 v[216:217], v233 offset:512\n\t"
"ds_read_b64_tr_b16 v[218:219], v233 offset:6656\n\t"
"s_add_u32 m0, s12, 81920\n\t"
"s_waitcnt lgkmcnt(10)\n\t"
"v_mfma_f32_16x16x32_bf16 v[0:3], v[196:199], v[184:187], v[0:3]\n\t"
"s_add_u32 s76, s13, 163840\n\t"
"buffer_load_dwordx4 v240, s[56:59], s76 offen sc1 lds\n\t"
"v_mfma_f32_16x16x32_bf16 v[4:7], v[196:199], v[188:191], v[4:7]\n\t"
"ds_read_b64_tr_b16 v[220:221], v233 offset:12800\n\t"
"ds_read_b64_tr_b16 v[222:223], v233 offset:18944\n\t"
"s_add_u32 m0, s12, 82944\n\t"
"v_mfma_f32_16x16x32_bf16 v[4:7], v[192:195], v[180:183], v[4:7]\n\t"
"s_add_u32 s76, s13, 164864\n\t"
"buffer_load_dwordx4 v240, s[56:59], s76 offen sc1 lds\n\t"
"s_waitcnt lgkmcnt(10)\n\t"
"v_mfma_f32_16x16x32_bf16 v[8:11], v[200:203], v[176:179], v[8:11]\n\t"
"ds_read_b64_tr_b16 v[192:193], v232 offset:1024\n\t"
"ds_read_b64_tr_b16 v[194:195], v232 offset:7168\n\t"
"s_add_u32 m0, s12, 83968\n\t"
"s_waitcnt lgkmcnt(10)\n\t"
"v_mfma_f32_16x16x32_bf16 v[8:11], v[204:207], v[184:187], v[8:11]\n\t"
"s_add_u32 s76, s13, 165888\n\t"
"buffer_load_dwordx4 v240, s[56:59], s76 offen sc1 lds\n\t"
"v_mfma_f32_16x16x32_bf16 v[12:15], v[204:207], v[188:191], v[12:15]\n\t"
"ds_read_b64_tr_b16 v[196:197], v232 offset:13312\n\t"
"ds_read_b64_tr_b16 v[198:199], v232 offset:19456\n\t"
"s_add_u32 m0, s12, 84992\n\t"
"v_mfma_f32_16x16x32_bf16 v[12:15], v[200:203], v[180:183], v[12:15]\n\t"
"s_add_u32 s76, s13, 166912\n\t"
"buffer_load_dwordx4 v240, s[56:59], s76 offen sc1 lds\n\t"
"s_waitcnt lgkmcnt(10)\n\t"
"v_mfma_f32_16x16x32_bf16 v[16:19], v[208:211], v[176:179], v[16:19]\n\t"
"ds_read_b64_tr_b16 v[200:201], v233 offset:1024\n\t"
"ds_read_b64_tr_b16 v[202:203], v233 offset:7168\n\t"
"s_waitcnt lgkmcnt(10)\n\t"
"v_mfma_f32_16x16x32_bf16 v[16:19], v[212:215], v[184:187], v[16:19]\n\t"
"s_add_u32 m0, s12, 86016\n\t"
"s_add_u32 s76, s13, 167936\n\t"
"v_mfma_f32_16x16x32_bf16 v[20:23], v[212:215], v[188:191], v[20:23]\n\t"
"buffer_load_dwordx4 v240, s[56:59], s76 offen sc1 lds\n\t"
"ds_read_b64_tr_b16 v[204:205], v233 offset:13312\n\t"
"v_mfma_f32_16x16x32_bf16 v[20:23], v[208:211], v[180:183], v[20:23]\n\t"
"ds_read_b64_tr_b16 v[206:207], v233 offset:19456\n\t"
"s_waitcnt lgkmcnt(10)\n\t"
"v_mfma_f32_16x16x32_bf16 v[24:27], v[216:219], v[176:179], v[24:27]\n\t"
"ds_read_b64_tr_b16 v[208:209], v232 offset:1536\n\t"
"ds_read_b64_tr_b16 v[210:211], v232 offset:7680\n\t"
"ds_read_b64_tr_b16 v[212:213], v232 offset:13824\n\t"
"ds_read_b64_tr_b16 v[214:215], v232 offset:19968\n\t"
"v_mul_f32 v0, v0, v234\n\t"
"s_waitcnt lgkmcnt(12)\n\t"
"v_mfma_f32_16x16x32_bf16 v[24:27], v[220:223], v[184:187], v[24:27]\n\t"
"v_mul_f32 v1, v1, v234\n\t"
"v_mul_f32 v2, v2, v234\n\t"
"v_mul_f32 v3, v3, v234\n\t"
"v_mul_f32 v8, v8, v234\n\t"
"v_mfma_f32_16x16x32_bf16 v[28:31], v[220:223], v[188:191], v[28:31]\n\t"
"v_mul_f32 v9, v9, v234\n\t"
"v_mul_f32 v10, v10, v234\n\t"
"v_mul_f32 v11, v11, v234\n\t"
"v_cvt_pk_bf16_f32 v144, v0, v1\n\t"
"v_cvt_pk_bf16_f32 v145, v2, v3\n\t"
"v_mfma_f32_16x16x32_bf16 v[28:31], v[216:219], v[180:183], v[28:31]\n\t"
"v_cvt_pk_bf16_f32 v146, v8, v9\n\t"
"v_cvt_pk_bf16_f32 v147, v10, v11\n\t"
"s_add_u32 s87, s67, 0\n\t"
"buffer_store_dwordx4 v[144:147], v240, s[56:59], s87 offen offset:0 sc1\n\t"
"s_waitcnt lgkmcnt(10)\n\t"
"v_mfma_f32_16x16x32_bf16 v[32:35], v[192:195], v[176:179], v[32:35]\n\t"
"ds_read_b64_tr_b16 v[216:217], v233 offset:1536\n\t"
"ds_read_b64_tr_b16 v[218:219], v233 offset:7680\n\t"
"ds_read_b64_tr_b16 v[220:221], v233 offset:13824\n\t"
"ds_read_b64_tr_b16 v[222:223], v233 offset:19968\n\t"
"v_mul_f32 v4, v4, v235\n\t"
"s_waitcnt lgkmcnt(12)\n\t"
"v_mfma_f32_16x16x32_bf16 v[32:35], v[196:199], v[184:187], v[32:35]\n\t"
"v_mul_f32 v5, v5, v235\n\t"
"v_mul_f32 v6, v6, v235\n\t"
"v_mul_f32 v7, v7, v235\n\t"
"v_mul_f32 v12, v12, v235\n\t"
"v_mfma_f32_16x16x32_bf16 v[36:39], v[196:199], v[188:191], v[36:39]\n\t"
"v_mul_f32 v13, v13, v235\n\t"
"v_mul_f32 v14, v14, v235\n\t"
"v_mul_f32 v15, v15, v235\n\t"
"v_cvt_pk_bf16_f32 v148, v4, v5\n\t"
"v_cvt_pk_bf16_f32 v149, v6, v7\n\t"
"v_mfma_f32_16x16x32_bf16 v[36:39], v[192:195], v[180:183], v[36:39]\n\t"
"v_cvt_pk_bf16_f32 v150, v12, v13\n\t"
"v_cvt_pk_bf16_f32 v151, v14, v15\n\t"
"s_add_u32 s87, s67, 245760\n\t"
"buffer_store_dwordx4 v[148:151], v240, s[56:59], s87 offen offset:0 sc1\n\t"
"s_waitcnt lgkmcnt(10)\n\t"
"v_mfma_f32_16x16x32_bf16 v[40:43], v[200:203], v[176:179], v[40:43]\n\t"
"ds_read_b64_tr_b16 v[192:193], v232 offset:2048\n\t"
"ds_read_b64_tr_b16 v[194:195], v232 offset:8192\n\t"
"ds_read_b64_tr_b16 v[196:197], v232 offset:14336\n\t"
"ds_read_b64_tr_b16 v[198:199], v232 offset:20480\n\t"
"v_mul_f32 v16, v16, v234\n\t"
"s_waitcnt lgkmcnt(12)\n\t"
"v_mfma_f32_16x16x32_bf16 v[40:43], v[204:207], v[184:187], v[40:43]\n\t"
"v_mul_f32 v17, v17, v234\n\t"
"v_mul_f32 v18, v18, v234\n\t"
"v_mul_f32 v19, v19, v234\n\t"
"v_mul_f32 v24, v24, v234\n\t"
"v_mfma_f32_16x16x32_bf16 v[44:47], v[204:207], v[188:191], v[44:47]\n\t"
"v_mul_f32 v25, v25, v234\n\t"
"v_mul_f32 v26, v26, v234\n\t"
"v_mul_f32 v27, v27, v234\n\t"
"v_cvt_pk_bf16_f32 v152, v16, v17\n\t"
"v_cvt_pk_bf16_f32 v153, v18, v19\n\t"
"v_mfma_f32_16x16x32_bf16 v[44:47], v[200:203], v[180:183], v[44:47]\n\t"
"v_cvt_pk_bf16_f32 v154, v24, v25\n\t"
"v_cvt_pk_bf16_f32 v155, v26, v27\n\t"
"s_add_u32 s87, s67, 1024\n\t"
"buffer_store_dwordx4 v[152:155], v240, s[56:59], s87 offen offset:0 sc1\n\t"
"s_waitcnt lgkmcnt(10)\n\t"
"v_mfma_f32_16x16x32_bf16 v[48:51], v[208:211], v[176:179], v[48:51]\n\t"
"ds_read_b64_tr_b16 v[200:201], v233 offset:2048\n\t"
"ds_read_b64_tr_b16 v[202:203], v233 offset:8192\n\t"
"ds_read_b64_tr_b16 v[204:205], v233 offset:14336\n\t"
"ds_read_b64_tr_b16 v[206:207], v233 offset:20480\n\t"
"v_mul_f32 v20, v20, v235\n\t"
"s_waitcnt lgkmcnt(12)\n\t"
"v_mfma_f32_16x16x32_bf16 v[48:51], v[212:215], v[184:187], v[48:51]\n\t"
"v_mul_f32 v21, v21, v235\n\t"
"v_mul_f32 v22, v22, v235\n\t"
"v_mul_f32 v23, v23, v235\n\t"
"v_mul_f32 v28, v28, v235\n\t"
"v_mfma_f32_16x16x32_bf16 v[52:55], v[212:215], v[188:191], v[52:55]\n\t"
"v_mul_f32 v29, v29, v235\n\t"
"v_mul_f32 v30, v30, v235\n\t"
"v_mul_f32 v31, v31, v235\n\t"
"v_cvt_pk_bf16_f32 v156, v20, v21\n\t"
"v_cvt_pk_bf16_f32 v157, v22, v23\n\t"
"v_mfma_f32_16x16x32_bf16 v[52:55], v[208:211], v[180:183], v[52:55]\n\t"
"v_cvt_pk_bf16_f32 v158, v28, v29\n\t"
"v_cvt_pk_bf16_f32 v159, v30, v31\n\t"
"s_add_u32 s87, s67, 246784\n\t"
"buffer_store_dwordx4 v[156:159], v240, s[56:59], s87 offen offset:0 sc1\n\t"
"s_waitcnt lgkmcnt(10)\n\t"
"v_mfma_f32_16x16x32_bf16 v[56:59], v[216:219], v[176:179], v[56:59]\n\t"
"ds_read_b64_tr_b16 v[208:209], v232 offset:2560\n\t"
"ds_read_b64_tr_b16 v[210:211], v232 offset:8704\n\t"
"ds_read_b64_tr_b16 v[212:213], v232 offset:14848\n\t"
"ds_read_b64_tr_b16 v[214:215], v232 offset:20992\n\t"
"v_mul_f32 v32, v32, v234\n\t"
"s_waitcnt lgkmcnt(12)\n\t"
"v_mfma_f32_16x16x32_bf16 v[56:59], v[220:223], v[184:187], v[56:59]\n\t"
"v_mul_f32 v33, v33, v234\n\t"
"v_mul_f32 v34, v34, v234\n\t"
"v_mul_f32 v35, v35, v234\n\t"
"v_mul_f32 v40, v40, v234\n\t"
"v_mfma_f32_16x16x32_bf16 v[60:63], v[220:223], v[188:191], v[60:63]\n\t"
"v_mul_f32 v41, v41, v234\n\t"
"v_mul_f32 v42, v42, v234\n\t"
"v_mul_f32 v43, v43, v234\n\t"
"v_cvt_pk_bf16_f32 v160, v32, v33\n\t"
"v_cvt_pk_bf16_f32 v161, v34, v35\n\t"
"v_mfma_f32_16x16x32_bf16 v[60:63], v[216:219], v[180:183], v[60:63]\n\t"
"v_cvt_pk_bf16_f32 v162, v40, v41\n\t"
"v_cvt_pk_bf16_f32 v163, v42, v43\n\t"
"s_add_u32 s87, s67, 2048\n\t"
"buffer_store_dwordx4 v[160:163], v240, s[56:59], s87 offen offset:0 sc1\n\t"
"s_waitcnt lgkmcnt(10)\n\t"
"v_mfma_f32_16x16x32_bf16 v[64:67], v[192:195], v[176:179], v[64:67]\n\t"
"ds_read_b64_tr_b16 v[216:217], v233 offset:2560\n\t"
"ds_read_b64_tr_b16 v[218:219], v233 offset:8704\n\t"
"ds_read_b64_tr_b16 v[220:221], v233 offset:14848\n\t"
"ds_read_b64_tr_b16 v[222:223], v233 offset:20992\n\t"
"v_mul_f32 v36, v36, v235\n\t"
"s_waitcnt lgkmcnt(12)\n\t"
"v_mfma_f32_16x16x32_bf16 v[64:67], v[196:199], v[184:187], v[64:67]\n\t"
"v_mul_f32 v37, v37, v235\n\t"
"v_mul_f32 v38, v38, v235\n\t"
"v_mul_f32 v39, v39, v235\n\t"
"v_mul_f32 v44, v44, v235\n\t"
"v_mfma_f32_16x16x32_bf16 v[68:71], v[196:199], v[188:191], v[68:71]\n\t"
"v_mul_f32 v45, v45, v235\n\t"
"v_mul_f32 v46, v46, v235\n\t"
"v_mul_f32 v47, v47, v235\n\t"
"v_cvt_pk_bf16_f32 v164, v36, v37\n\t"
"v_cvt_pk_bf16_f32 v165, v38, v39\n\t"
"v_mfma_f32_16x16x32_bf16 v[68:71], v[192:195], v[180:183], v[68:71]\n\t"
"v_cvt_pk_bf16_f32 v166, v44, v45\n\t"
"v_cvt_pk_bf16_f32 v167, v46, v47\n\t"
"s_add_u32 s87, s67, 247808\n\t"
"buffer_store_dwordx4 v[164:167], v240, s[56:59], s87 offen offset:0 sc1\n\t"
"s_waitcnt lgkmcnt(10)\n\t"
"v_mfma_f32_16x16x32_bf16 v[72:75], v[200:203], v[176:179], v[72:75]\n\t"
"v_mul_f32 v48, v48, v234\n\t"
"v_mul_f32 v49, v49, v234\n\t"
"v_mul_f32 v50, v50, v234\n\t"
"v_mul_f32 v51, v51, v234\n\t"
"s_waitcnt lgkmcnt(8)\n\t"
"v_mfma_f32_16x16x32_bf16 v[72:75], v[204:207], v[184:187], v[72:75]\n\t"
"v_mul_f32 v56, v56, v234\n\t"
"v_mul_f32 v57, v57, v234\n\t"
"v_mul_f32 v58, v58, v234\n\t"
"v_mfma_f32_16x16x32_bf16 v[76:79], v[204:207], v[188:191], v[76:79]\n\t"
"v_mul_f32 v59, v59, v234\n\t"
"v_cvt_pk_bf16_f32 v168, v48, v49\n\t"
"v_cvt_pk_bf16_f32 v169, v50, v51\n\t"
"v_cvt_pk_bf16_f32 v170, v56, v57\n\t"
"v_mfma_f32_16x16x32_bf16 v[76:79], v[200:203], v[180:183], v[76:79]\n\t"
"v_cvt_pk_bf16_f32 v171, v58, v59\n\t"
"s_add_u32 s87, s67, 3072\n\t"
"buffer_store_dwordx4 v[168:171], v240, s[56:59], s87 offen offset:0 sc1\n\t"
"s_waitcnt lgkmcnt(6)\n\t"
"v_mfma_f32_16x16x32_bf16 v[80:83], v[208:211], v[176:179], v[80:83]\n\t"
"v_mul_f32 v52, v52, v235\n\t"
"v_mul_f32 v53, v53, v235\n\t"
"v_mul_f32 v54, v54, v235\n\t"
"v_mul_f32 v55, v55, v235\n\t"
"s_waitcnt lgkmcnt(4)\n\t"
"v_mfma_f32_16x16x32_bf16 v[80:83], v[212:215], v[184:187], v[80:83]\n\t"
"v_mul_f32 v60, v60, v235\n\t"
"v_mul_f32 v61, v61, v235\n\t"
"v_mul_f32 v62, v62, v235\n\t"
"v_mfma_f32_16x16x32_bf16 v[84:87], v[212:215], v[188:191], v[84:87]\n\t"
"v_mul_f32 v63, v63, v235\n\t"
"v_cvt_pk_bf16_f32 v172, v52, v53\n\t"
"v_cvt_pk_bf16_f32 v173, v54, v55\n\t"
"v_cvt_pk_bf16_f32 v174, v60, v61\n\t"
"v_mfma_f32_16x16x32_bf16 v[84:87], v[208:211], v[180:183], v[84:87]\n\t"
"v_cvt_pk_bf16_f32 v175, v62, v63\n\t"
"s_add_u32 s87, s67, 248832\n\t"
"buffer_store_dwordx4 v[172:175], v240, s[56:59], s87 offen offset:0 sc1\n\t"
"s_waitcnt lgkmcnt(2)\n\t"
"v_mfma_f32_16x16x32_bf16 v[88:91], v[216:219], v[176:179], v[88:91]\n\t"
"v_mul_f32 v64, v64, v234\n\t"
"v_mul_f32 v65, v65, v234\n\t"
"v_mul_f32 v66, v66, v234\n\t"
"v_mul_f32 v67, v67, v234\n\t"
"s_waitcnt lgkmcnt(0)\n\t"
"v_mfma_f32_16x16x32_bf16 v[88:91], v[220:223], v[184:187], v[88:91]\n\t"
"v_mul_f32 v72, v72, v234\n\t"
"v_mul_f32 v73, v73, v234\n\t"
"v_mul_f32 v74, v74, v234\n\t"
"v_mfma_f32_16x16x32_bf16 v[92:95], v[220:223], v[188:191], v[92:95]\n\t"
"v_mul_f32 v75, v75, v234\n\t"
"v_cvt_pk_bf16_f32 v144, v64, v65\n\t"
"v_cvt_pk_bf16_f32 v145, v66, v67\n\t"
"v_cvt_pk_bf16_f32 v146, v72, v73\n\t"
"v_mfma_f32_16x16x32_bf16 v[92:95], v[216:219], v[180:183], v[92:95]\n\t"
"v_cvt_pk_bf16_f32 v147, v74, v75\n\t"
"s_add_u32 s87, s67, 4096\n\t"
"buffer_store_dwordx4 v[144:147], v240, s[56:59], s87 offen offset:0 sc1\n\t"
"v_mul_f32 v68, v68, v235\n\t"
"v_mul_f32 v69, v69, v235\n\t"
"v_mul_f32 v70, v70, v235\n\t"
"v_mul_f32 v71, v71, v235\n\t"
"v_mul_f32 v76, v76, v235\n\t"
"v_mul_f32 v77, v77, v235\n\t"
"v_mul_f32 v78, v78, v235\n\t"
"v_mul_f32 v79, v79, v235\n\t"
"v_cvt_pk_bf16_f32 v148, v68, v69\n\t"
"v_cvt_pk_bf16_f32 v149, v70, v71\n\t"
"v_cvt_pk_bf16_f32 v150, v76, v77\n\t"
"v_cvt_pk_bf16_f32 v151, v78, v79\n\t"
"s_add_u32 s87, s67, 249856\n\t"
"buffer_store_dwordx4 v[148:151], v240, s[56:59], s87 offen offset:0 sc1\n\t"
"v_mul_f32 v80, v80, v234\n\t"
"v_mul_f32 v81, v81, v234\n\t"
"v_mul_f32 v82, v82, v234\n\t"
"v_mul_f32 v83, v83, v234\n\t"
"v_mul_f32 v88, v88, v234\n\t"
"v_mul_f32 v89, v89, v234\n\t"
"v_mul_f32 v90, v90, v234\n\t"
"v_mul_f32 v91, v91, v234\n\t"
"v_cvt_pk_bf16_f32 v152, v80, v81\n\t"
"v_cvt_pk_bf16_f32 v153, v82, v83\n\t"
"v_cvt_pk_bf16_f32 v154, v88, v89\n\t"
"v_cvt_pk_bf16_f32 v155, v90, v91\n\t"
"s_add_u32 s87, s67, 5120\n\t"
"buffer_store_dwordx4 v[152:155], v240, s[56:59], s87 offen offset:0 sc1\n\t"
"v_mul_f32 v84, v84, v235\n\t"
"v_mul_f32 v85, v85, v235\n\t"
"v_mul_f32 v86, v86, v235\n\t"
"v_mul_f32 v87, v87, v235\n\t"
"v_mul_f32 v92, v92, v235\n\t"
"v_mul_f32 v93, v93, v235\n\t"
"v_mul_f32 v94, v94, v235\n\t"
"v_mul_f32 v95, v95, v235\n\t"
"v_cvt_pk_bf16_f32 v156, v84, v85\n\t"
"v_cvt_pk_bf16_f32 v157, v86, v87\n\t"
"v_cvt_pk_bf16_f32 v158, v92, v93\n\t"
"v_cvt_pk_bf16_f32 v159, v94, v95\n\t"
"s_add_u32 s87, s67, 250880\n\t"
"buffer_store_dwordx4 v[156:159], v240, s[56:59], s87 offen offset:0 sc1\n\t"
"s_setprio 0\n\t"
"v_add_u32 v246, 40960, v253\n\t"
"ds_read_b128 v[192:195], v246 offset:0\n\t"
"ds_read_b128 v[196:199], v246 offset:20480\n\t"
"ds_read_b128 v[200:203], v246 offset:1024\n\t"
"ds_read_b128 v[204:207], v246 offset:21504\n\t"
"ds_read_b128 v[208:211], v246 offset:2048\n\t"
"ds_read_b128 v[212:215], v246 offset:22528\n\t"
"s_waitcnt vmcnt(25) lgkmcnt(4)\n\t"
"v_mfma_f32_16x16x32_bf16 v[0:3], v[192:195], v[96:99], 0\n\t"
"ds_read_b128 v[216:219], v246 offset:3072\n\t"
"s_add_u32 s87, s14, 8192\n\t"
"v_mfma_f32_16x16x32_bf16 v[0:3], v[196:199], v[104:107], v[0:3]\n\t"
"buffer_load_dwordx2 v[128:129], v247, s[56:59], s87 offen offset:0 sc1\n\t"
"buffer_load_dwordx2 v[130:131], v247, s[56:59], s87 offen offset:512 sc1\n\t"
"v_mfma_f32_16x16x32_bf16 v[4:7], v[196:199], v[108:111], 0\n\t"
"ds_read_b128 v[220:223], v246 offset:23552\n\t"
"s_add_u32 s87, s15, 8192\n\t"
"v_mfma_f32_16x16x32_bf16 v[4:7], v[192:195], v[100:103], v[4:7]\n\t"
"buffer_load_dwordx2 v[132:133], v247, s[56:59], s87 offen offset:0 sc1\n\t"
"buffer_load_dwordx2 v[134:135], v247, s[56:59], s87 offen offset:512 sc1\n\t"
"s_waitcnt lgkmcnt(4)\n\t"
"v_mfma_f32_16x16x32_bf16 v[8:11], v[200:203], v[96:99], 0\n\t"
"ds_read_b128 v[192:195], v246 offset:4096\n\t"
"s_add_u32 s87, s14, 10240\n\t"
"v_mfma_f32_16x16x32_bf16 v[8:11], v[204:207], v[104:107], v[8:11]\n\t"
"buffer_load_dwordx2 v[136:137], v247, s[56:59], s87 offen offset:0 sc1\n\t"
"buffer_load_dwordx2 v[138:139], v247, s[56:59], s87 offen offset:512 sc1\n\t"
"v_mfma_f32_16x16x32_bf16 v[12:15], v[204:207], v[108:111], 0\n\t"
"ds_read_b128 v[196:199], v246 offset:24576\n\t"
"s_add_u32 s87, s15, 10240\n\t"
"v_mfma_f32_16x16x32_bf16 v[12:15], v[200:203], v[100:103], v[12:15]\n\t"
"buffer_load_dwordx2 v[140:141], v247, s[56:59], s87 offen offset:0 sc1\n\t"
"buffer_load_dwordx2 v[142:143], v247, s[56:59], s87 offen offset:512 sc1\n\t"
"s_waitcnt lgkmcnt(4)\n\t"
"v_mfma_f32_16x16x32_bf16 v[16:19], v[208:211], v[96:99], 0\n\t"
"ds_read_b128 v[200:203], v246 offset:5120\n\t"
"s_add_u32 m0, s12, 122880\n\t"
"v_mfma_f32_16x16x32_bf16 v[16:19], v[212:215], v[104:107], v[16:19]\n\t"
"s_add_u32 s76, s13, 327680\n\t"
"buffer_load_dwordx4 v240, s[56:59], s76 offen sc1 lds\n\t"
"v_mfma_f32_16x16x32_bf16 v[20:23], v[212:215], v[108:111], 0\n\t"
"ds_read_b128 v[204:207], v246 offset:25600\n\t"
"s_add_u32 m0, s12, 123904\n\t"
"v_mfma_f32_16x16x32_bf16 v[20:23], v[208:211], v[100:103], v[20:23]\n\t"
"s_add_u32 s76, s13, 328704\n\t"
"buffer_load_dwordx4 v240, s[56:59], s76 offen sc1 lds\n\t"
"s_waitcnt lgkmcnt(4)\n\t"
"v_mfma_f32_16x16x32_bf16 v[24:27], v[216:219], v[96:99], 0\n\t"
"ds_read_b128 v[208:211], v246 offset:6144\n\t"
"s_add_u32 m0, s12, 124928\n\t"
"v_mfma_f32_16x16x32_bf16 v[24:27], v[220:223], v[104:107], v[24:27]\n\t"
"s_add_u32 s76, s13, 329728\n\t"
"buffer_load_dwordx4 v240, s[56:59], s76 offen sc1 lds\n\t"
"v_mfma_f32_16x16x32_bf16 v[28:31], v[220:223], v[108:111], 0\n\t"
"ds_read_b128 v[212:215], v246 offset:26624\n\t"
"s_add_u32 m0, s12, 125952\n\t"
"v_mfma_f32_16x16x32_bf16 v[28:31], v[216:219], v[100:103], v[28:31]\n\t"
"s_add_u32 s76, s13, 330752\n\t"
"buffer_load_dwordx4 v240, s[56:59], s76 offen sc1 lds\n\t"
"s_waitcnt lgkmcnt(4)\n\t"
"v_mfma_f32_16x16x32_bf16 v[32:35], v[192:195], v[96:99], 0\n\t"
"ds_read_b128 v[216:219], v246 offset:7168\n\t"
"s_add_u32 m0, s12, 126976\n\t"
"v_mfma_f32_16x16x32_bf16 v[32:35], v[196:199], v[104:107], v[32:35]\n\t"
"s_add_u32 s76, s13, 331776\n\t"
"v_mfma_f32_16x16x32_bf16 v[36:39], v[196:199], v[108:111], 0\n\t"
"buffer_load_dwordx4 v240, s[56:59], s76 offen sc1 lds\n\t"
"v_mfma_f32_16x16x32_bf16 v[36:39], v[192:195], v[100:103], v[36:39]\n\t"
"ds_read_b128 v[220:223], v246 offset:27648\n\t"
"s_waitcnt lgkmcnt(4)\n\t"
"v_mfma_f32_16x16x32_bf16 v[40:43], v[200:203], v[96:99], 0\n\t"
"ds_read_b128 v[192:195], v246 offset:8192\n\t"
"v_mfma_f32_16x16x32_bf16 v[40:43], v[204:207], v[104:107], v[40:43]\n\t"
"v_mfma_f32_16x16x32_bf16 v[44:47], v[204:207], v[108:111], 0\n\t"
"ds_read_b128 v[196:199], v246 offset:28672\n\t"
"v_mfma_f32_16x16x32_bf16 v[44:47], v[200:203], v[100:103], v[44:47]\n\t"
"s_waitcnt lgkmcnt(4)\n\t"
"v_mfma_f32_16x16x32_bf16 v[48:51], v[208:211], v[96:99], 0\n\t"
"ds_read_b128 v[200:203], v246 offset:9216\n\t"
"v_mfma_f32_16x16x32_bf16 v[48:51], v[212:215], v[104:107], v[48:51]\n\t"
"v_mfma_f32_16x16x32_bf16 v[52:55], v[212:215], v[108:111], 0\n\t"
"ds_read_b128 v[204:207], v246 offset:29696\n\t"
"v_mfma_f32_16x16x32_bf16 v[52:55], v[208:211], v[100:103], v[52:55]\n\t"
"s_waitcnt vmcnt(13)\n\t"
"s_mov_b64 exec, 1\n\t"
"buffer_store_dword v241, off, s[56:59], s21 sc1\n\t"
"s_mov_b64 exec, -1\n\t"
"s_barrier\n\t"
"v_add_u32 v246, 81920, v253\n\t"
"s_waitcnt lgkmcnt(4)\n\t"
"v_mfma_f32_16x16x32_bf16 v[56:59], v[216:219], v[96:99], 0\n\t"
"ds_read_b128 v[208:211], v246 offset:0\n\t"
"s_add_u32 m0, s12, 0\n\t"
"v_mfma_f32_16x16x32_bf16 v[56:59], v[220:223], v[104:107], v[56:59]\n\t"
"s_add_u32 s76, s20, 0\n\t"
"v_mfma_f32_16x16x32_bf16 v[60:63], v[220:223], v[108:111], 0\n\t"
"buffer_load_dwordx4 v240, s[56:59], s76 offen sc1 lds\n\t"
"v_mfma_f32_16x16x32_bf16 v[60:63], v[216:219], v[100:103], v[60:63]\n\t"
"ds_read_b128 v[212:215], v246 offset:20480\n\t"
"s_waitcnt lgkmcnt(4)\n\t"
"v_mfma_f32_16x16x32_bf16 v[64:67], v[192:195], v[96:99], 0\n\t"
"ds_read_b128 v[216:219], v246 offset:1024\n\t"
"s_add_u32 m0, s12, 1024\n\t"
"v_mfma_f32_16x16x32_bf16 v[64:67], v[196:199], v[104:107], v[64:67]\n\t"
"s_add_u32 s76, s20, 1024\n\t"
"v_mfma_f32_16x16x32_bf16 v[68:71], v[196:199], v[108:111], 0\n\t"
"buffer_load_dwordx4 v240, s[56:59], s76 offen sc1 lds\n\t"
"v_mfma_f32_16x16x32_bf16 v[68:71], v[192:195], v[100:103], v[68:71]\n\t"
"ds_read_b128 v[220:223], v246 offset:21504\n\t"
"s_waitcnt lgkmcnt(4)\n\t"
"v_mfma_f32_16x16x32_bf16 v[72:75], v[200:203], v[96:99], 0\n\t"
"ds_read_b128 v[192:195], v246 offset:2048\n\t"
"s_add_u32 m0, s12, 2048\n\t"
"v_mfma_f32_16x16x32_bf16 v[72:75], v[204:207], v[104:107], v[72:75]\n\t"
"s_add_u32 s76, s20, 2048\n\t"
"v_mfma_f32_16x16x32_bf16 v[76:79], v[204:207], v[108:111], 0\n\t"
"buffer_load_dwordx4 v240, s[56:59], s76 offen sc1 lds\n\t"
"v_mfma_f32_16x16x32_bf16 v[76:79], v[200:203], v[100:103], v[76:79]\n\t"
"ds_read_b128 v[196:199], v246 offset:22528\n\t"
"s_waitcnt lgkmcnt(4)\n\t"
"v_mfma_f32_16x16x32_bf16 v[0:3], v[208:211], v[112:115], v[0:3]\n\t"
"ds_read_b128 v[200:203], v246 offset:3072\n\t"
"s_add_u32 m0, s12, 3072\n\t"
"v_mfma_f32_16x16x32_bf16 v[0:3], v[212:215], v[120:123], v[0:3]\n\t"
"s_add_u32 s76, s20, 3072\n\t"
"v_mfma_f32_16x16x32_bf16 v[4:7], v[212:215], v[124:127], v[4:7]\n\t"
"buffer_load_dwordx4 v240, s[56:59], s76 offen sc1 lds\n\t"
"v_mfma_f32_16x16x32_bf16 v[4:7], v[208:211], v[116:119], v[4:7]\n\t"
"ds_read_b128 v[204:207], v246 offset:23552\n\t"
"s_waitcnt lgkmcnt(4)\n\t"
"v_mfma_f32_16x16x32_bf16 v[8:11], v[216:219], v[112:115], v[8:11]\n\t"
"ds_read_b128 v[208:211], v246 offset:4096\n\t"
"s_add_u32 m0, s12, 4096\n\t"
"v_mfma_f32_16x16x32_bf16 v[8:11], v[220:223], v[120:123], v[8:11]\n\t"
"s_add_u32 s76, s20, 4096\n\t"
"v_mfma_f32_16x16x32_bf16 v[12:15], v[220:223], v[124:127], v[12:15]\n\t"
"buffer_load_dwordx4 v240, s[56:59], s76 offen sc1 lds\n\t"
"v_mfma_f32_16x16x32_bf16 v[12:15], v[216:219], v[116:119], v[12:15]\n\t"
"ds_read_b128 v[212:215], v246 offset:24576\n\t"
"s_waitcnt lgkmcnt(4)\n\t"
"v_mfma_f32_16x16x32_bf16 v[16:19], v[192:195], v[112:115], v[16:19]\n\t"
"ds_read_b128 v[216:219], v246 offset:5120\n\t"
"v_mfma_f32_16x16x32_bf16 v[16:19], v[196:199], v[120:123], v[16:19]\n\t"
"v_mfma_f32_16x16x32_bf16 v[20:23], v[196:199], v[124:127], v[20:23]\n\t"
"ds_read_b128 v[220:223], v246 offset:25600\n\t"
"v_mfma_f32_16x16x32_bf16 v[20:23], v[192:195], v[116:119], v[20:23]\n\t"
"s_waitcnt lgkmcnt(4)\n\t"
"v_mfma_f32_16x16x32_bf16 v[24:27], v[200:203], v[112:115], v[24:27]\n\t"
"ds_read_b128 v[192:195], v246 offset:6144\n\t"
"v_mfma_f32_16x16x32_bf16 v[24:27], v[204:207], v[120:123], v[24:27]\n\t"
"v_mfma_f32_16x16x32_bf16 v[28:31], v[204:207], v[124:127], v[28:31]\n\t"
"ds_read_b128 v[196:199], v246 offset:26624\n\t"
"v_mfma_f32_16x16x32_bf16 v[28:31], v[200:203], v[116:119], v[28:31]\n\t"
"s_waitcnt lgkmcnt(4)\n\t"
"v_mfma_f32_16x16x32_bf16 v[32:35], v[208:211], v[112:115], v[32:35]\n\t"
"ds_read_b128 v[200:203], v246 offset:7168\n\t"
"v_mfma_f32_16x16x32_bf16 v[32:35], v[212:215], v[120:123], v[32:35]\n\t"
"v_mfma_f32_16x16x32_bf16 v[36:39], v[212:215], v[124:127], v[36:39]\n\t"
"ds_read_b128 v[204:207], v246 offset:27648\n\t"
"v_mfma_f32_16x16x32_bf16 v[36:39], v[208:211], v[116:119], v[36:39]\n\t"
"s_waitcnt lgkmcnt(4)\n\t"
"v_mfma_f32_16x16x32_bf16 v[40:43], v[216:219], v[112:115], v[40:43]\n\t"
"ds_read_b128 v[208:211], v246 offset:8192\n\t"
"v_mfma_f32_16x16x32_bf16 v[40:43], v[220:223], v[120:123], v[40:43]\n\t"
"v_mfma_f32_16x16x32_bf16 v[44:47], v[220:223], v[124:127], v[44:47]\n\t"
"ds_read_b128 v[212:215], v246 offset:28672\n\t"
"v_mfma_f32_16x16x32_bf16 v[44:47], v[216:219], v[116:119], v[44:47]\n\t"
"s_waitcnt lgkmcnt(4)\n\t"
"v_mfma_f32_16x16x32_bf16 v[48:51], v[192:195], v[112:115], v[48:51]\n\t"
"ds_read_b128 v[216:219], v246 offset:9216\n\t"
"v_mfma_f32_16x16x32_bf16 v[48:51], v[196:199], v[120:123], v[48:51]\n\t"
"v_mfma_f32_16x16x32_bf16 v[52:55], v[196:199], v[124:127], v[52:55]\n\t"
"ds_read_b128 v[220:223], v246 offset:29696\n\t"
"v_mfma_f32_16x16x32_bf16 v[52:55], v[192:195], v[116:119], v[52:55]\n\t"
"s_waitcnt vmcnt(6)\n\t"
"s_barrier\n\t"
"v_add_u32 v246, 122880, v253\n\t"
"s_waitcnt lgkmcnt(4)\n\t"
"v_mfma_f32_16x16x32_bf16 v[56:59], v[200:203], v[112:115], v[56:59]\n\t"
"ds_read_b128 v[192:195], v246 offset:0\n\t"
"s_add_u32 m0, s12, 40960\n\t"
"v_mfma_f32_16x16x32_bf16 v[56:59], v[204:207], v[120:123], v[56:59]\n\t"
"s_add_u32 s76, s20, 163840\n\t"
"v_mfma_f32_16x16x32_bf16 v[60:63], v[204:207], v[124:127], v[60:63]\n\t"
"buffer_load_dwordx4 v240, s[56:59], s76 offen sc1 lds\n\t"
"v_mfma_f32_16x16x32_bf16 v[60:63], v[200:203], v[116:119], v[60:63]\n\t"
"ds_read_b128 v[196:199], v246 offset:20480\n\t"
"s_waitcnt lgkmcnt(4)\n\t"
"v_mfma_f32_16x16x32_bf16 v[64:67], v[208:211], v[112:115], v[64:67]\n\t"
"ds_read_b128 v[200:203], v246 offset:1024\n\t"
"s_add_u32 m0, s12, 41984\n\t"
"v_mfma_f32_16x16x32_bf16 v[64:67], v[212:215], v[120:123], v[64:67]\n\t"
"s_add_u32 s76, s20, 164864\n\t"
"v_mfma_f32_16x16x32_bf16 v[68:71], v[212:215], v[124:127], v[68:71]\n\t"
"buffer_load_dwordx4 v240, s[56:59], s76 offen sc1 lds\n\t"
"v_mfma_f32_16x16x32_bf16 v[68:71], v[208:211], v[116:119], v[68:71]\n\t"
"ds_read_b128 v[204:207], v246 offset:21504\n\t"
"s_waitcnt lgkmcnt(4)\n\t"
"v_mfma_f32_16x16x32_bf16 v[72:75], v[216:219], v[112:115], v[72:75]\n\t"
"ds_read_b128 v[208:211], v246 offset:2048\n\t"
"s_add_u32 m0, s12, 43008\n\t"
"v_mfma_f32_16x16x32_bf16 v[72:75], v[220:223], v[120:123], v[72:75]\n\t"
"s_add_u32 s76, s20, 165888\n\t"
"v_mfma_f32_16x16x32_bf16 v[76:79], v[220:223], v[124:127], v[76:79]\n\t"
"buffer_load_dwordx4 v240, s[56:59], s76 offen sc1 lds\n\t"
"v_mfma_f32_16x16x32_bf16 v[76:79], v[216:219], v[116:119], v[76:79]\n\t"
"ds_read_b128 v[212:215], v246 offset:22528\n\t"
"s_waitcnt lgkmcnt(4)\n\t"
"v_mfma_f32_16x16x32_bf16 v[0:3], v[192:195], v[128:131], v[0:3]\n\t"
"ds_read_b128 v[216:219], v246 offset:3072\n\t"
"s_add_u32 m0, s12, 44032\n\t"
"v_mfma_f32_16x16x32_bf16 v[0:3], v[196:199], v[136:139], v[0:3]\n\t"
"s_add_u32 s76, s20, 166912\n\t"
"v_mfma_f32_16x16x32_bf16 v[4:7], v[196:199], v[140:143], v[4:7]\n\t"
"buffer_load_dwordx4 v240, s[56:59], s76 offen sc1 lds\n\t"
"v_mfma_f32_16x16x32_bf16 v[4:7], v[192:195], v[132:135], v[4:7]\n\t"
"ds_read_b128 v[220:223], v246 offset:23552\n\t"
"s_waitcnt lgkmcnt(4)\n\t"
"v_mfma_f32_16x16x32_bf16 v[8:11], v[200:203], v[128:131], v[8:11]\n\t"
"ds_read_b128 v[192:195], v246 offset:4096\n\t"
"s_add_u32 m0, s12, 45056\n\t"
"v_mfma_f32_16x16x32_bf16 v[8:11], v[204:207], v[136:139], v[8:11]\n\t"
"s_add_u32 s76, s20, 167936\n\t"
"v_mfma_f32_16x16x32_bf16 v[12:15], v[204:207], v[140:143], v[12:15]\n\t"
"buffer_load_dwordx4 v240, s[56:59], s76 offen sc1 lds\n\t"
"v_mfma_f32_16x16x32_bf16 v[12:15], v[200:203], v[132:135], v[12:15]\n\t"
"ds_read_b128 v[196:199], v246 offset:24576\n\t"
"s_waitcnt lgkmcnt(4)\n\t"
"v_mfma_f32_16x16x32_bf16 v[16:19], v[208:211], v[128:131], v[16:19]\n\t"
"ds_read_b128 v[200:203], v246 offset:5120\n\t"
"v_mfma_f32_16x16x32_bf16 v[16:19], v[212:215], v[136:139], v[16:19]\n\t"
"v_mfma_f32_16x16x32_bf16 v[20:23], v[212:215], v[140:143], v[20:23]\n\t"
"ds_read_b128 v[204:207], v246 offset:25600\n\t"
"v_mfma_f32_16x16x32_bf16 v[20:23], v[208:211], v[132:135], v[20:23]\n\t"
"s_waitcnt lgkmcnt(4)\n\t"
"v_mfma_f32_16x16x32_bf16 v[24:27], v[216:219], v[128:131], v[24:27]\n\t"
"ds_read_b128 v[208:211], v246 offset:6144\n\t"
"v_mfma_f32_16x16x32_bf16 v[24:27], v[220:223], v[136:139], v[24:27]\n\t"
"v_mfma_f32_16x16x32_bf16 v[28:31], v[220:223], v[140:143], v[28:31]\n\t"
"ds_read_b128 v[212:215], v246 offset:26624\n\t"
"v_mfma_f32_16x16x32_bf16 v[28:31], v[216:219], v[132:135], v[28:31]\n\t"
"s_mov_b32 s83, 0x20000\n\t"
"s_branch LPOF_%=\n\t"
"LPO_%=:\n\t"
"s_sleep 2\n\t"
"LPOF_%=:\n\t"
"s_sub_u32 s83, s83, 1\n\t"
"s_cbranch_scc1 LPOX_%=\n\t"
"buffer_load_dword v245, v243, s[56:59], s23 offen offset:2048 sc1\n\t"
"s_waitcnt vmcnt(0)\n\t"
"v_cmp_eq_u32 vcc, 0, v245\n\t"
"s_cbranch_vccnz LPO_%=\n\t"
"LPOX_%=:\n\t"
"s_add_u32 s87, s34, 0\n\t"
"buffer_load_dwordx4 v[96:99], v240, s[56:59], s87 offen offset:0 sc1\n\t"
"s_add_u32 s87, s35, 0\n\t"
"buffer_load_dwordx4 v[100:103], v240, s[56:59], s87 offen offset:0 sc1\n\t"
"s_add_u32 s87, s34, 2048\n\t"
"buffer_load_dwordx4 v[104:107], v240, s[56:59], s87 offen offset:0 sc1\n\t"
"s_add_u32 s87, s35, 2048\n\t"
"buffer_load_dwordx4 v[108:111], v240, s[56:59], s87 offen offset:0 sc1\n\t"
"s_waitcnt lgkmcnt(4)\n\t"
"v_mfma_f32_16x16x32_bf16 v[32:35], v[192:195], v[128:131], v[32:35]\n\t"
"ds_read_b128 v[216:219], v246 offset:7168\n\t"
"s_add_u32 s87, s34, 4096\n\t"
"v_mfma_f32_16x16x32_bf16 v[32:35], v[196:199], v[136:139], v[32:35]\n\t"
"buffer_load_dwordx4 v[112:115], v240, s[56:59], s87 offen offset:0 sc1\n\t"
"v_mfma_f32_16x16x32_bf16 v[36:39], v[196:199], v[140:143], v[36:39]\n\t"
"ds_read_b128 v[220:223], v246 offset:27648\n\t"
"s_add_u32 s87, s35, 4096\n\t"
"v_mfma_f32_16x16x32_bf16 v[36:39], v[192:195], v[132:135], v[36:39]\n\t"
"buffer_load_dwordx4 v[116:119], v240, s[56:59], s87 offen offset:0 sc1\n\t"
"s_waitcnt lgkmcnt(4)\n\t"
"v_mfma_f32_16x16x32_bf16 v[40:43], v[200:203], v[128:131], v[40:43]\n\t"
"ds_read_b128 v[192:195], v246 offset:8192\n\t"
"s_add_u32 s87, s34, 6144\n\t"
"v_mfma_f32_16x16x32_bf16 v[40:43], v[204:207], v[136:139], v[40:43]\n\t"
"buffer_load_dwordx4 v[120:123], v240, s[56:59], s87 offen offset:0 sc1\n\t"
"v_mfma_f32_16x16x32_bf16 v[44:47], v[204:207], v[140:143], v[44:47]\n\t"
"ds_read_b128 v[196:199], v246 offset:28672\n\t"
"s_add_u32 s87, s35, 6144\n\t"
"v_mfma_f32_16x16x32_bf16 v[44:47], v[200:203], v[132:135], v[44:47]\n\t"
"buffer_load_dwordx4 v[124:127], v240, s[56:59], s87 offen offset:0 sc1\n\t"
"s_waitcnt lgkmcnt(4)\n\t"
"v_mfma_f32_16x16x32_bf16 v[48:51], v[208:211], v[128:131], v[48:51]\n\t"
"ds_read_b128 v[200:203], v246 offset:9216\n\t"
"v_mfma_f32_16x16x32_bf16 v[48:51], v[212:215], v[136:139], v[48:51]\n\t"
"v_mfma_f32_16x16x32_bf16 v[52:55], v[212:215], v[140:143], v[52:55]\n\t"
"ds_read_b128 v[204:207], v246 offset:29696\n\t"
"v_mfma_f32_16x16x32_bf16 v[52:55], v[208:211], v[132:135], v[52:55]\n\t"
"s_barrier\n\t"
"v_add_u32 v246, 0, v253\n\t"
"s_waitcnt lgkmcnt(4)\n\t"
"v_mfma_f32_16x16x32_bf16 v[56:59], v[216:219], v[128:131], v[56:59]\n\t"
"ds_read_b128 v[208:211], v246 offset:0\n\t"
"s_add_u32 m0, s12, 81920\n\t"
"v_mfma_f32_16x16x32_bf16 v[56:59], v[220:223], v[136:139], v[56:59]\n\t"
"s_add_u32 s76, s20, 327680\n\t"
"v_mfma_f32_16x16x32_bf16 v[60:63], v[220:223], v[140:143], v[60:63]\n\t"
"buffer_load_dwordx4 v240, s[56:59], s76 offen sc1 lds\n\t"
"v_mfma_f32_16x16x32_bf16 v[60:63], v[216:219], v[132:135], v[60:63]\n\t"
"ds_read_b128 v[212:215], v246 offset:20480\n\t"
"s_waitcnt lgkmcnt(4)\n\t"
"v_mfma_f32_16x16x32_bf16 v[64:67], v[192:195], v[128:131], v[64:67]\n\t"
"ds_read_b128 v[216:219], v246 offset:1024\n\t"
"s_add_u32 m0, s12, 82944\n\t"
"v_mfma_f32_16x16x32_bf16 v[64:67], v[196:199], v[136:139], v[64:67]\n\t"
"s_add_u32 s76, s20, 328704\n\t"
"v_mfma_f32_16x16x32_bf16 v[68:71], v[196:199], v[140:143], v[68:71]\n\t"
"buffer_load_dwordx4 v240, s[56:59], s76 offen sc1 lds\n\t"
"v_mfma_f32_16x16x32_bf16 v[68:71], v[192:195], v[132:135], v[68:71]\n\t"
"ds_read_b128 v[220:223], v246 offset:21504\n\t"
"s_waitcnt lgkmcnt(4)\n\t"
"v_mfma_f32_16x16x32_bf16 v[72:75], v[200:203], v[128:131], v[72:75]\n\t"
"ds_read_b128 v[192:195], v246 offset:2048\n\t"
"s_add_u32 m0, s12, 83968\n\t"
"v_mfma_f32_16x16x32_bf16 v[72:75], v[204:207], v[136:139], v[72:75]\n\t"
"s_add_u32 s76, s20, 329728\n\t"
"v_mfma_f32_16x16x32_bf16 v[76:79], v[204:207], v[140:143], v[76:79]\n\t"
"buffer_load_dwordx4 v240, s[56:59], s76 offen sc1 lds\n\t"
"v_mfma_f32_16x16x32_bf16 v[76:79], v[200:203], v[132:135], v[76:79]\n\t"
"ds_read_b128 v[196:199], v246 offset:22528\n\t"
"s_waitcnt vmcnt(7) lgkmcnt(4)\n\t"
"v_mfma_f32_16x16x32_bf16 v[144:147], v[208:211], v[96:99], 0\n\t"
"ds_read_b128 v[200:203], v246 offset:3072\n\t"
"s_add_u32 m0, s12, 84992\n\t"
"v_mfma_f32_16x16x32_bf16 v[144:147], v[212:215], v[104:107], v[144:147]\n\t"
"s_add_u32 s76, s20, 330752\n\t"
"buffer_load_dwordx4 v240, s[56:59], s76 offen sc1 lds\n\t"
"v_mfma_f32_16x16x32_bf16 v[148:151], v[212:215], v[108:111], 0\n\t"
"ds_read_b128 v[204:207], v246 offset:23552\n\t"
"s_add_u32 s87, s34, 8192\n\t"
"v_mfma_f32_16x16x32_bf16 v[148:151], v[208:211], v[100:103], v[148:151]\n\t"
"buffer_load_dwordx4 v[128:131], v240, s[56:59], s87 offen offset:0 sc1\n\t"
"s_waitcnt lgkmcnt(4)\n\t"
"v_mfma_f32_16x16x32_bf16 v[152:155], v[216:219], v[96:99], 0\n\t"
"ds_read_b128 v[208:211], v246 offset:4096\n\t"
"s_add_u32 m0, s12, 86016\n\t"
"v_mfma_f32_16x16x32_bf16 v[152:155], v[220:223], v[104:107], v[152:155]\n\t"
"s_add_u32 s76, s20, 331776\n\t"
"buffer_load_dwordx4 v240, s[56:59], s76 offen sc1 lds\n\t"
"v_mfma_f32_16x16x32_bf16 v[156:159], v[220:223], v[108:111], 0\n\t"
"ds_read_b128 v[212:215], v246 offset:24576\n\t"
"s_add_u32 s87, s35, 8192\n\t"
"v_mfma_f32_16x16x32_bf16 v[156:159], v[216:219], v[100:103], v[156:159]\n\t"
"buffer_load_dwordx4 v[132:135], v240, s[56:59], s87 offen offset:0 sc1\n\t"
"s_waitcnt lgkmcnt(4)\n\t"
"v_mfma_f32_16x16x32_bf16 v[160:163], v[192:195], v[96:99], 0\n\t"
"ds_read_b128 v[216:219], v246 offset:5120\n\t"
"v_mfma_f32_16x16x32_bf16 v[160:163], v[196:199], v[104:107], v[160:163]\n\t"
"v_mfma_f32_16x16x32_bf16 v[164:167], v[196:199], v[108:111], 0\n\t"
"ds_read_b128 v[220:223], v246 offset:25600\n\t"
"v_mfma_f32_16x16x32_bf16 v[164:167], v[192:195], v[100:103], v[164:167]\n\t"
"s_waitcnt lgkmcnt(4)\n\t"
"v_mfma_f32_16x16x32_bf16 v[168:171], v[200:203], v[96:99], 0\n\t"
"ds_read_b128 v[192:195], v246 offset:6144\n\t"
"v_mfma_f32_16x16x32_bf16 v[168:171], v[204:207], v[104:107], v[168:171]\n\t"
"v_mfma_f32_16x16x32_bf16 v[172:175], v[204:207], v[108:111], 0\n\t"
"ds_read_b128 v[196:199], v246 offset:26624\n\t"
"v_mfma_f32_16x16x32_bf16 v[172:175], v[200:203], v[100:103], v[172:175]\n\t"
"s_waitcnt lgkmcnt(4)\n\t"
"v_mfma_f32_16x16x32_bf16 v[176:179], v[208:211], v[96:99], 0\n\t"
"ds_read_b128 v[200:203], v246 offset:7168\n\t"
"v_mfma_f32_16x16x32_bf16 v[176:179], v[212:215], v[104:107], v[176:179]\n\t"
"ds_read_b128 v[204:207], v246 offset:27648\n\t"
"v_mfma_f32_16x16x32_bf16 v[180:183], v[212:215], v[108:111], 0\n\t"
"s_add_u32 s87, s34, 10240\n\t"
"v_mfma_f32_16x16x32_bf16 v[180:183], v[208:211], v[100:103], v[180:183]\n\t"
"buffer_load_dwordx4 v[136:139], v240, s[56:59], s87 offen offset:0 sc1\n\t"
"s_waitcnt lgkmcnt(4)\n\t"
"v_mfma_f32_16x16x32_bf16 v[184:187], v[216:219], v[96:99], 0\n\t"
"ds_read_b128 v[208:211], v246 offset:8192\n\t"
"v_mfma_f32_16x16x32_bf16 v[184:187], v[220:223], v[104:107], v[184:187]\n\t"
"ds_read_b128 v[212:215], v246 offset:28672\n\t"
"v_mfma_f32_16x16x32_bf16 v[188:191], v[220:223], v[108:111], 0\n\t"
"s_add_u32 s87, s35, 10240\n\t"
"v_mfma_f32_16x16x32_bf16 v[188:191], v[216:219], v[100:103], v[188:191]\n\t"
"buffer_load_dwordx4 v[140:143], v240, s[56:59], s87 offen offset:0 sc1\n\t"
"s_waitcnt lgkmcnt(4)\n\t"
"v_mfma_f32_16x16x32_bf16 v[80:83], v[192:195], v[96:99], 0\n\t"
"ds_read_b128 v[216:219], v246 offset:9216\n\t"
"v_mfma_f32_16x16x32_bf16 v[80:83], v[196:199], v[104:107], v[80:83]\n\t"
"v_mfma_f32_16x16x32_bf16 v[84:87], v[196:199], v[108:111], 0\n\t"
"ds_read_b128 v[220:223], v246 offset:29696\n\t"
"v_mfma_f32_16x16x32_bf16 v[84:87], v[192:195], v[100:103], v[84:87]\n\t"
"s_barrier\n\t"
"v_add_u32 v246, 40960, v253\n\t"
"s_waitcnt lgkmcnt(4)\n\t"
"v_mfma_f32_16x16x32_bf16 v[88:91], v[200:203], v[96:99], 0\n\t"
"ds_read_b128 v[192:195], v246 offset:0\n\t"
"s_add_u32 m0, s12, 122880\n\t"
"v_mfma_f32_16x16x32_bf16 v[88:91], v[204:207], v[104:107], v[88:91]\n\t"
"s_add_u32 s76, s20, 491520\n\t"
"v_mfma_f32_16x16x32_bf16 v[92:95], v[204:207], v[108:111], 0\n\t"
"buffer_load_dwordx4 v240, s[56:59], s76 offen sc1 lds\n\t"
"v_mfma_f32_16x16x32_bf16 v[92:95], v[200:203], v[100:103], v[92:95]\n\t"
"ds_read_b128 v[196:199], v246 offset:20480\n\t"
"s_waitcnt lgkmcnt(4)\n\t"
"v_mfma_f32_16x16x32_bf16 v[224:227], v[208:211], v[96:99], 0\n\t"
"ds_read_b128 v[200:203], v246 offset:1024\n\t"
"s_add_u32 m0, s12, 123904\n\t"
"v_mfma_f32_16x16x32_bf16 v[224:227], v[212:215], v[104:107], v[224:227]\n\t"
"s_add_u32 s76, s20, 492544\n\t"
"v_mfma_f32_16x16x32_bf16 v[228:231], v[212:215], v[108:111], 0\n\t"
"buffer_load_dwordx4 v240, s[56:59], s76 offen sc1 lds\n\t"
"v_mfma_f32_16x16x32_bf16 v[228:231], v[208:211], v[100:103], v[228:231]\n\t"
"ds_read_b128 v[204:207], v246 offset:21504\n\t"
"s_waitcnt lgkmcnt(4)\n\t"
"v_mfma_f32_16x16x32_bf16 v[232:235], v[216:219], v[96:99], 0\n\t"
"ds_read_b128 v[208:211], v246 offset:2048\n\t"
"s_add_u32 m0, s12, 124928\n\t"
"v_mfma_f32_16x16x32_bf16 v[232:235], v[220:223], v[104:107], v[232:235]\n\t"
"s_add_u32 s76, s20, 493568\n\t"
"v_mfma_f32_16x16x32_bf16 v[236:239], v[220:223], v[108:111], 0\n\t"
"buffer_load_dwordx4 v240, s[56:59], s76 offen sc1 lds\n\t"
"v_mfma_f32_16x16x32_bf16 v[236:239], v[216:219], v[100:103], v[236:239]\n\t"
"ds_read_b128 v[212:215], v246 offset:22528\n\t"
"s_waitcnt vmcnt(12) lgkmcnt(4)\n\t"
"v_mfma_f32_16x16x32_bf16 v[144:147], v[192:195], v[112:115], v[144:147]\n\t"
"ds_read_b128 v[216:219], v246 offset:3072\n\t"
"s_add_u32 m0, s12, 125952\n\t"
"v_mfma_f32_16x16x32_bf16 v[144:147], v[196:199], v[120:123], v[144:147]\n\t"
"s_add_u32 s76, s20, 494592\n\t"
"buffer_load_dwordx4 v240, s[56:59], s76 offen sc1 lds\n\t"
"v_mfma_f32_16x16x32_bf16 v[148:151], v[196:199], v[124:127], v[148:151]\n\t"
"ds_read_b128 v[220:223], v246 offset:23552\n\t"
"s_add_u32 s87, s34, 12288\n\t"
"v_mfma_f32_16x16x32_bf16 v[148:151], v[192:195], v[116:119], v[148:151]\n\t"
"buffer_load_dwordx4 v[96:99], v240, s[56:59], s87 offen offset:0 sc1\n\t"
"s_waitcnt lgkmcnt(4)\n\t"
"v_mfma_f32_16x16x32_bf16 v[152:155], v[200:203], v[112:115], v[152:155]\n\t"
"ds_read_b128 v[192:195], v246 offset:4096\n\t"
"s_add_u32 m0, s12, 126976\n\t"
"v_mfma_f32_16x16x32_bf16 v[152:155], v[204:207], v[120:123], v[152:155]\n\t"
"s_add_u32 s76, s20, 495616\n\t"
"buffer_load_dwordx4 v240, s[56:59], s76 offen sc1 lds\n\t"
"v_mfma_f32_16x16x32_bf16 v[156:159], v[204:207], v[124:127], v[156:159]\n\t"
"ds_read_b128 v[196:199], v246 offset:24576\n\t"
"s_add_u32 s87, s35, 12288\n\t"
"v_mfma_f32_16x16x32_bf16 v[156:159], v[200:203], v[116:119], v[156:159]\n\t"
"buffer_load_dwordx4 v[100:103], v240, s[56:59], s87 offen offset:0 sc1\n\t"
"s_waitcnt lgkmcnt(4)\n\t"
"v_mfma_f32_16x16x32_bf16 v[160:163], v[208:211], v[112:115], v[160:163]\n\t"
"ds_read_b128 v[200:203], v246 offset:5120\n\t"
"v_mfma_f32_16x16x32_bf16 v[160:163], v[212:215], v[120:123], v[160:163]\n\t"
"v_mfma_f32_16x16x32_bf16 v[164:167], v[212:215], v[124:127], v[164:167]\n\t"
"ds_read_b128 v[204:207], v246 offset:25600\n\t"
"v_mfma_f32_16x16x32_bf16 v[164:167], v[208:211], v[116:119], v[164:167]\n\t"
"s_waitcnt lgkmcnt(4)\n\t"
"v_mfma_f32_16x16x32_bf16 v[168:171], v[216:219], v[112:115], v[168:171]\n\t"
"ds_read_b128 v[208:211], v246 offset:6144\n\t"
"v_mfma_f32_16x16x32_bf16 v[168:171], v[220:223], v[120:123], v[168:171]\n\t"
"v_mfma_f32_16x16x32_bf16 v[172:175], v[220:223], v[124:127], v[172:175]\n\t"
"ds_read_b128 v[212:215], v246 offset:26624\n\t"
"v_mfma_f32_16x16x32_bf16 v[172:175], v[216:219], v[116:119], v[172:175]\n\t"
"s_waitcnt lgkmcnt(4)\n\t"
"v_mfma_f32_16x16x32_bf16 v[176:179], v[192:195], v[112:115], v[176:179]\n\t"
"ds_read_b128 v[216:219], v246 offset:7168\n\t"
"v_mfma_f32_16x16x32_bf16 v[176:179], v[196:199], v[120:123], v[176:179]\n\t"
"ds_read_b128 v[220:223], v246 offset:27648\n\t"
"v_mfma_f32_16x16x32_bf16 v[180:183], v[196:199], v[124:127], v[180:183]\n\t"
"s_add_u32 s87, s34, 14336\n\t"
"v_mfma_f32_16x16x32_bf16 v[180:183], v[192:195], v[116:119], v[180:183]\n\t"
"buffer_load_dwordx4 v[104:107], v240, s[56:59], s87 offen offset:0 sc1\n\t"
"s_waitcnt lgkmcnt(4)\n\t"
"v_mfma_f32_16x16x32_bf16 v[184:187], v[200:203], v[112:115], v[184:187]\n\t"
"ds_read_b128 v[192:195], v246 offset:8192\n\t"
"v_mfma_f32_16x16x32_bf16 v[184:187], v[204:207], v[120:123], v[184:187]\n\t"
"ds_read_b128 v[196:199], v246 offset:28672\n\t"
"v_mfma_f32_16x16x32_bf16 v[188:191], v[204:207], v[124:127], v[188:191]\n\t"
"s_add_u32 s87, s35, 14336\n\t"
"v_mfma_f32_16x16x32_bf16 v[188:191], v[200:203], v[116:119], v[188:191]\n\t"
"buffer_load_dwordx4 v[108:111], v240, s[56:59], s87 offen offset:0 sc1\n\t"
"s_waitcnt lgkmcnt(4)\n\t"
"v_mfma_f32_16x16x32_bf16 v[80:83], v[208:211], v[112:115], v[80:83]\n\t"
"ds_read_b128 v[200:203], v246 offset:9216\n\t"
"v_mfma_f32_16x16x32_bf16 v[80:83], v[212:215], v[120:123], v[80:83]\n\t"
"v_mfma_f32_16x16x32_bf16 v[84:87], v[212:215], v[124:127], v[84:87]\n\t"
"ds_read_b128 v[204:207], v246 offset:29696\n\t"
"v_mfma_f32_16x16x32_bf16 v[84:87], v[208:211], v[116:119], v[84:87]\n\t"
"s_waitcnt vmcnt(12)\n\t"
"s_barrier\n\t"
"v_add_u32 v246, 81920, v253\n\t"
"s_waitcnt lgkmcnt(4)\n\t"
"v_mfma_f32_16x16x32_bf16 v[88:91], v[216:219], v[112:115], v[88:91]\n\t"
"ds_read_b128 v[208:211], v246 offset:0\n\t"
"s_add_u32 m0, s12, 0\n\t"
"v_mfma_f32_16x16x32_bf16 v[88:91], v[220:223], v[120:123], v[88:91]\n\t"
"s_add_u32 s76, s20, 655360\n\t"
"v_mfma_f32_16x16x32_bf16 v[92:95], v[220:223], v[124:127], v[92:95]\n\t"
"buffer_load_dwordx4 v240, s[56:59], s76 offen sc1 lds\n\t"
"v_mfma_f32_16x16x32_bf16 v[92:95], v[216:219], v[116:119], v[92:95]\n\t"
"ds_read_b128 v[212:215], v246 offset:20480\n\t"
"s_waitcnt lgkmcnt(4)\n\t"
"v_mfma_f32_16x16x32_bf16 v[224:227], v[192:195], v[112:115], v[224:227]\n\t"
"ds_read_b128 v[216:219], v246 offset:1024\n\t"
"s_add_u32 m0, s12, 1024\n\t"
"v_mfma_f32_16x16x32_bf16 v[224:227], v[196:199], v[120:123], v[224:227]\n\t"
"s_add_u32 s76, s20, 656384\n\t"
"v_mfma_f32_16x16x32_bf16 v[228:231], v[196:199], v[124:127], v[228:231]\n\t"
"buffer_load_dwordx4 v240, s[56:59], s76 offen sc1 lds\n\t"
"v_mfma_f32_16x16x32_bf16 v[228:231], v[192:195], v[116:119], v[228:231]\n\t"
"ds_read_b128 v[220:223], v246 offset:21504\n\t"
"s_waitcnt lgkmcnt(4)\n\t"
"v_mfma_f32_16x16x32_bf16 v[232:235], v[200:203], v[112:115], v[232:235]\n\t"
"ds_read_b128 v[192:195], v246 offset:2048\n\t"
"s_add_u32 m0, s12, 2048\n\t"
"v_mfma_f32_16x16x32_bf16 v[232:235], v[204:207], v[120:123], v[232:235]\n\t"
"s_add_u32 s76, s20, 657408\n\t"
"v_mfma_f32_16x16x32_bf16 v[236:239], v[204:207], v[124:127], v[236:239]\n\t"
"buffer_load_dwordx4 v240, s[56:59], s76 offen sc1 lds\n\t"
"v_mfma_f32_16x16x32_bf16 v[236:239], v[200:203], v[116:119], v[236:239]\n\t"
"ds_read_b128 v[196:199], v246 offset:22528\n\t"
"s_waitcnt vmcnt(12) lgkmcnt(4)\n\t"
"v_mfma_f32_16x16x32_bf16 v[144:147], v[208:211], v[128:131], v[144:147]\n\t"
"ds_read_b128 v[200:203], v246 offset:3072\n\t"
"s_add_u32 m0, s12, 3072\n\t"
"v_mfma_f32_16x16x32_bf16 v[144:147], v[212:215], v[136:139], v[144:147]\n\t"
"s_add_u32 s76, s20, 658432\n\t"
"buffer_load_dwordx4 v240, s[56:59], s76 offen sc1 lds\n\t"
"v_mfma_f32_16x16x32_bf16 v[148:151], v[212:215], v[140:143], v[148:151]\n\t"
"ds_read_b128 v[204:207], v246 offset:23552\n\t"
"s_add_u32 s87, s34, 16384\n\t"
"v_mfma_f32_16x16x32_bf16 v[148:151], v[208:211], v[132:135], v[148:151]\n\t"
"buffer_load_dwordx4 v[112:115], v240, s[56:59], s87 offen offset:0 sc1\n\t"
"s_waitcnt lgkmcnt(4)\n\t"
"v_mfma_f32_16x16x32_bf16 v[152:155], v[216:219], v[128:131], v[152:155]\n\t"
"ds_read_b128 v[208:211], v246 offset:4096\n\t"
"s_add_u32 m0, s12, 4096\n\t"
"v_mfma_f32_16x16x32_bf16 v[152:155], v[220:223], v[136:139], v[152:155]\n\t"
"s_add_u32 s76, s20, 659456\n\t"
"buffer_load_dwordx4 v240, s[56:59], s76 offen sc1 lds\n\t"
"v_mfma_f32_16x16x32_bf16 v[156:159], v[220:223], v[140:143], v[156:159]\n\t"
"ds_read_b128 v[212:215], v246 offset:24576\n\t"
"s_add_u32 s87, s35, 16384\n\t"
"v_mfma_f32_16x16x32_bf16 v[156:159], v[216:219], v[132:135], v[156:159]\n\t"
"buffer_load_dwordx4 v[116:119], v240, s[56:59], s87 offen offset:0 sc1\n\t"
"s_waitcnt lgkmcnt(4)\n\t"
"v_mfma_f32_16x16x32_bf16 v[160:163], v[192:195], v[128:131], v[160:163]\n\t"
"ds_read_b128 v[216:219], v246 offset:5120\n\t"
"v_mfma_f32_16x16x32_bf16 v[160:163], v[196:199], v[136:139], v[160:163]\n\t"
"v_mfma_f32_16x16x32_bf16 v[164:167], v[196:199], v[140:143], v[164:167]\n\t"
"ds_read_b128 v[220:223], v246 offset:25600\n\t"
"v_mfma_f32_16x16x32_bf16 v[164:167], v[192:195], v[132:135], v[164:167]\n\t"
"s_waitcnt lgkmcnt(4)\n\t"
"v_mfma_f32_16x16x32_bf16 v[168:171], v[200:203], v[128:131], v[168:171]\n\t"
"ds_read_b128 v[192:195], v246 offset:6144\n\t"
"v_mfma_f32_16x16x32_bf16 v[168:171], v[204:207], v[136:139], v[168:171]\n\t"
"v_mfma_f32_16x16x32_bf16 v[172:175], v[204:207], v[140:143], v[172:175]\n\t"
"ds_read_b128 v[196:199], v246 offset:26624\n\t"
"v_mfma_f32_16x16x32_bf16 v[172:175], v[200:203], v[132:135], v[172:175]\n\t"
"s_waitcnt lgkmcnt(4)\n\t"
"v_mfma_f32_16x16x32_bf16 v[176:179], v[208:211], v[128:131], v[176:179]\n\t"
"ds_read_b128 v[200:203], v246 offset:7168\n\t"
"v_mfma_f32_16x16x32_bf16 v[176:179], v[212:215], v[136:139], v[176:179]\n\t"
"ds_read_b128 v[204:207], v246 offset:27648\n\t"
"v_mfma_f32_16x16x32_bf16 v[180:183], v[212:215], v[140:143], v[180:183]\n\t"
"s_add_u32 s87, s34, 18432\n\t"
"v_mfma_f32_16x16x32_bf16 v[180:183], v[208:211], v[132:135], v[180:183]\n\t"
"buffer_load_dwordx4 v[120:123], v240, s[56:59], s87 offen offset:0 sc1\n\t"
"s_waitcnt lgkmcnt(4)\n\t"
"v_mfma_f32_16x16x32_bf16 v[184:187], v[216:219], v[128:131], v[184:187]\n\t"
"ds_read_b128 v[208:211], v246 offset:8192\n\t"
"v_mfma_f32_16x16x32_bf16 v[184:187], v[220:223], v[136:139], v[184:187]\n\t"
"ds_read_b128 v[212:215], v246 offset:28672\n\t"
"v_mfma_f32_16x16x32_bf16 v[188:191], v[220:223], v[140:143], v[188:191]\n\t"
"s_add_u32 s87, s35, 18432\n\t"
"v_mfma_f32_16x16x32_bf16 v[188:191], v[216:219], v[132:135], v[188:191]\n\t"
"buffer_load_dwordx4 v[124:127], v240, s[56:59], s87 offen offset:0 sc1\n\t"
"s_waitcnt lgkmcnt(4)\n\t"
"v_mfma_f32_16x16x32_bf16 v[80:83], v[192:195], v[128:131], v[80:83]\n\t"
"ds_read_b128 v[216:219], v246 offset:9216\n\t"
"v_mfma_f32_16x16x32_bf16 v[80:83], v[196:199], v[136:139], v[80:83]\n\t"
"v_mfma_f32_16x16x32_bf16 v[84:87], v[196:199], v[140:143], v[84:87]\n\t"
"ds_read_b128 v[220:223], v246 offset:29696\n\t"
"v_mfma_f32_16x16x32_bf16 v[84:87], v[192:195], v[132:135], v[84:87]\n\t"
"s_waitcnt vmcnt(12)\n\t"
"s_barrier\n\t"
"v_add_u32 v246, 122880, v253\n\t"
"s_waitcnt lgkmcnt(4)\n\t"
"v_mfma_f32_16x16x32_bf16 v[88:91], v[200:203], v[128:131], v[88:91]\n\t"
"ds_read_b128 v[192:195], v246 offset:0\n\t"
"s_add_u32 m0, s12, 40960\n\t"
"v_mfma_f32_16x16x32_bf16 v[88:91], v[204:207], v[136:139], v[88:91]\n\t"
"s_add_u32 s76, s20, 819200\n\t"
"v_mfma_f32_16x16x32_bf16 v[92:95], v[204:207], v[140:143], v[92:95]\n\t"
"buffer_load_dwordx4 v240, s[56:59], s76 offen sc1 lds\n\t"
"v_mfma_f32_16x16x32_bf16 v[92:95], v[200:203], v[132:135], v[92:95]\n\t"
"ds_read_b128 v[196:199], v246 offset:20480\n\t"
"s_waitcnt lgkmcnt(4)\n\t"
"v_mfma_f32_16x16x32_bf16 v[224:227], v[208:211], v[128:131], v[224:227]\n\t"
"ds_read_b128 v[200:203], v246 offset:1024\n\t"
"s_add_u32 m0, s12, 41984\n\t"
"v_mfma_f32_16x16x32_bf16 v[224:227], v[212:215], v[136:139], v[224:227]\n\t"
"s_add_u32 s76, s20, 820224\n\t"
"v_mfma_f32_16x16x32_bf16 v[228:231], v[212:215], v[140:143], v[228:231]\n\t"
"buffer_load_dwordx4 v240, s[56:59], s76 offen sc1 lds\n\t"
"v_mfma_f32_16x16x32_bf16 v[228:231], v[208:211], v[132:135], v[228:231]\n\t"
"ds_read_b128 v[204:207], v246 offset:21504\n\t"
"s_waitcnt lgkmcnt(4)\n\t"
"v_mfma_f32_16x16x32_bf16 v[232:235], v[216:219], v[128:131], v[232:235]\n\t"
"ds_read_b128 v[208:211], v246 offset:2048\n\t"
"s_add_u32 m0, s12, 43008\n\t"
"v_mfma_f32_16x16x32_bf16 v[232:235], v[220:223], v[136:139], v[232:235]\n\t"
"s_add_u32 s76, s20, 821248\n\t"
"v_mfma_f32_16x16x32_bf16 v[236:239], v[220:223], v[140:143], v[236:239]\n\t"
"buffer_load_dwordx4 v240, s[56:59], s76 offen sc1 lds\n\t"
"v_mfma_f32_16x16x32_bf16 v[236:239], v[216:219], v[132:135], v[236:239]\n\t"
"ds_read_b128 v[212:215], v246 offset:22528\n\t"
"s_waitcnt vmcnt(12) lgkmcnt(4)\n\t"
"v_mfma_f32_16x16x32_bf16 v[144:147], v[192:195], v[96:99], v[144:147]\n\t"
"ds_read_b128 v[216:219], v246 offset:3072\n\t"
"s_add_u32 m0, s12, 44032\n\t"
"v_mfma_f32_16x16x32_bf16 v[144:147], v[196:199], v[104:107], v[144:147]\n\t"
"s_add_u32 s76, s20, 822272\n\t"
"buffer_load_dwordx4 v240, s[56:59], s76 offen sc1 lds\n\t"
"v_mfma_f32_16x16x32_bf16 v[148:151], v[196:199], v[108:111], v[148:151]\n\t"
"ds_read_b128 v[220:223], v246 offset:23552\n\t"
"s_add_u32 s87, s34, 20480\n\t"
"v_mfma_f32_16x16x32_bf16 v[148:151], v[192:195], v[100:103], v[148:151]\n\t"
"buffer_load_dwordx4 v[128:131], v240, s[56:59], s87 offen offset:0 sc1\n\t"
"s_waitcnt lgkmcnt(4)\n\t"
"v_mfma_f32_16x16x32_bf16 v[152:155], v[200:203], v[96:99], v[152:155]\n\t"
"ds_read_b128 v[192:195], v246 offset:4096\n\t"
"s_add_u32 m0, s12, 45056\n\t"
"v_mfma_f32_16x16x32_bf16 v[152:155], v[204:207], v[104:107], v[152:155]\n\t"
"s_add_u32 s76, s20, 823296\n\t"
"buffer_load_dwordx4 v240, s[56:59], s76 offen sc1 lds\n\t"
"v_mfma_f32_16x16x32_bf16 v[156:159], v[204:207], v[108:111], v[156:159]\n\t"
"ds_read_b128 v[196:199], v246 offset:24576\n\t"
"s_add_u32 s87, s35, 20480\n\t"
"v_mfma_f32_16x16x32_bf16 v[156:159], v[200:203], v[100:103], v[156:159]\n\t"
"buffer_load_dwordx4 v[132:135], v240, s[56:59], s87 offen offset:0 sc1\n\t"
"s_waitcnt lgkmcnt(4)\n\t"
"v_mfma_f32_16x16x32_bf16 v[160:163], v[208:211], v[96:99], v[160:163]\n\t"
"ds_read_b128 v[200:203], v246 offset:5120\n\t"
"v_mfma_f32_16x16x32_bf16 v[160:163], v[212:215], v[104:107], v[160:163]\n\t"
"v_mfma_f32_16x16x32_bf16 v[164:167], v[212:215], v[108:111], v[164:167]\n\t"
"ds_read_b128 v[204:207], v246 offset:25600\n\t"
"v_mfma_f32_16x16x32_bf16 v[164:167], v[208:211], v[100:103], v[164:167]\n\t"
"s_waitcnt lgkmcnt(4)\n\t"
"v_mfma_f32_16x16x32_bf16 v[168:171], v[216:219], v[96:99], v[168:171]\n\t"
"ds_read_b128 v[208:211], v246 offset:6144\n\t"
"v_mfma_f32_16x16x32_bf16 v[168:171], v[220:223], v[104:107], v[168:171]\n\t"
"v_mfma_f32_16x16x32_bf16 v[172:175], v[220:223], v[108:111], v[172:175]\n\t"
"ds_read_b128 v[212:215], v246 offset:26624\n\t"
"v_mfma_f32_16x16x32_bf16 v[172:175], v[216:219], v[100:103], v[172:175]\n\t"
"s_waitcnt lgkmcnt(4)\n\t"
"v_mfma_f32_16x16x32_bf16 v[176:179], v[192:195], v[96:99], v[176:179]\n\t"
"ds_read_b128 v[216:219], v246 offset:7168\n\t"
"v_mfma_f32_16x16x32_bf16 v[176:179], v[196:199], v[104:107], v[176:179]\n\t"
"ds_read_b128 v[220:223], v246 offset:27648\n\t"
"v_mfma_f32_16x16x32_bf16 v[180:183], v[196:199], v[108:111], v[180:183]\n\t"
"s_add_u32 s87, s34, 22528\n\t"
"v_mfma_f32_16x16x32_bf16 v[180:183], v[192:195], v[100:103], v[180:183]\n\t"
"buffer_load_dwordx4 v[136:139], v240, s[56:59], s87 offen offset:0 sc1\n\t"
"s_waitcnt lgkmcnt(4)\n\t"
"v_mfma_f32_16x16x32_bf16 v[184:187], v[200:203], v[96:99], v[184:187]\n\t"
"ds_read_b128 v[192:195], v246 offset:8192\n\t"
"v_mfma_f32_16x16x32_bf16 v[184:187], v[204:207], v[104:107], v[184:187]\n\t"
"ds_read_b128 v[196:199], v246 offset:28672\n\t"
"v_mfma_f32_16x16x32_bf16 v[188:191], v[204:207], v[108:111], v[188:191]\n\t"
"s_add_u32 s87, s35, 22528\n\t"
"v_mfma_f32_16x16x32_bf16 v[188:191], v[200:203], v[100:103], v[188:191]\n\t"
"buffer_load_dwordx4 v[140:143], v240, s[56:59], s87 offen offset:0 sc1\n\t"
"s_waitcnt lgkmcnt(4)\n\t"
"v_mfma_f32_16x16x32_bf16 v[80:83], v[208:211], v[96:99], v[80:83]\n\t"
"ds_read_b128 v[200:203], v246 offset:9216\n\t"
"v_mfma_f32_16x16x32_bf16 v[80:83], v[212:215], v[104:107], v[80:83]\n\t"
"v_mfma_f32_16x16x32_bf16 v[84:87], v[212:215], v[108:111], v[84:87]\n\t"
"ds_read_b128 v[204:207], v246 offset:29696\n\t"
"v_mfma_f32_16x16x32_bf16 v[84:87], v[208:211], v[100:103], v[84:87]\n\t"
"s_waitcnt vmcnt(12)\n\t"
"s_barrier\n\t"
"v_add_u32 v246, 0, v253\n\t"
"s_waitcnt lgkmcnt(4)\n\t"
"v_mfma_f32_16x16x32_bf16 v[88:91], v[216:219], v[96:99], v[88:91]\n\t"
"ds_read_b128 v[208:211], v246 offset:0\n\t"
"s_add_u32 m0, s12, 81920\n\t"
"v_mfma_f32_16x16x32_bf16 v[88:91], v[220:223], v[104:107], v[88:91]\n\t"
"s_add_u32 s76, s20, 983040\n\t"
"v_mfma_f32_16x16x32_bf16 v[92:95], v[220:223], v[108:111], v[92:95]\n\t"
"buffer_load_dwordx4 v240, s[56:59], s76 offen sc1 lds\n\t"
"v_mfma_f32_16x16x32_bf16 v[92:95], v[216:219], v[100:103], v[92:95]\n\t"
"ds_read_b128 v[212:215], v246 offset:20480\n\t"
"s_waitcnt lgkmcnt(4)\n\t"
"v_mfma_f32_16x16x32_bf16 v[224:227], v[192:195], v[96:99], v[224:227]\n\t"
"ds_read_b128 v[216:219], v246 offset:1024\n\t"
"s_add_u32 m0, s12, 82944\n\t"
"v_mfma_f32_16x16x32_bf16 v[224:227], v[196:199], v[104:107], v[224:227]\n\t"
"s_add_u32 s76, s20, 984064\n\t"
"v_mfma_f32_16x16x32_bf16 v[228:231], v[196:199], v[108:111], v[228:231]\n\t"
"buffer_load_dwordx4 v240, s[56:59], s76 offen sc1 lds\n\t"
"v_mfma_f32_16x16x32_bf16 v[228:231], v[192:195], v[100:103], v[228:231]\n\t"
"ds_read_b128 v[220:223], v246 offset:21504\n\t"
"s_waitcnt lgkmcnt(4)\n\t"
"v_mfma_f32_16x16x32_bf16 v[232:235], v[200:203], v[96:99], v[232:235]\n\t"
"ds_read_b128 v[192:195], v246 offset:2048\n\t"
"s_add_u32 m0, s12, 83968\n\t"
"v_mfma_f32_16x16x32_bf16 v[232:235], v[204:207], v[104:107], v[232:235]\n\t"
"s_add_u32 s76, s20, 985088\n\t"
"v_mfma_f32_16x16x32_bf16 v[236:239], v[204:207], v[108:111], v[236:239]\n\t"
"buffer_load_dwordx4 v240, s[56:59], s76 offen sc1 lds\n\t"
"v_mfma_f32_16x16x32_bf16 v[236:239], v[200:203], v[100:103], v[236:239]\n\t"
"ds_read_b128 v[196:199], v246 offset:22528\n\t"
"s_waitcnt vmcnt(12) lgkmcnt(4)\n\t"
"v_mfma_f32_16x16x32_bf16 v[144:147], v[208:211], v[112:115], v[144:147]\n\t"
"ds_read_b128 v[200:203], v246 offset:3072\n\t"
"s_add_u32 m0, s12, 84992\n\t"
"v_mfma_f32_16x16x32_bf16 v[144:147], v[212:215], v[120:123], v[144:147]\n\t"
"s_add_u32 s76, s20, 986112\n\t"
"buffer_load_dwordx4 v240, s[56:59], s76 offen sc1 lds\n\t"
"v_mfma_f32_16x16x32_bf16 v[148:151], v[212:215], v[124:127], v[148:151]\n\t"
"ds_read_b128 v[204:207], v246 offset:23552\n\t"
"s_add_u32 s87, s34, 24576\n\t"
"v_mfma_f32_16x16x32_bf16 v[148:151], v[208:211], v[116:119], v[148:151]\n\t"
"buffer_load_dwordx4 v[96:99], v240, s[56:59], s87 offen offset:0 sc1\n\t"
"s_waitcnt lgkmcnt(4)\n\t"
"v_mfma_f32_16x16x32_bf16 v[152:155], v[216:219], v[112:115], v[152:155]\n\t"
"ds_read_b128 v[208:211], v246 offset:4096\n\t"
"s_add_u32 m0, s12, 86016\n\t"
"v_mfma_f32_16x16x32_bf16 v[152:155], v[220:223], v[120:123], v[152:155]\n\t"
"s_add_u32 s76, s20, 987136\n\t"
"buffer_load_dwordx4 v240, s[56:59], s76 offen sc1 lds\n\t"
"v_mfma_f32_16x16x32_bf16 v[156:159], v[220:223], v[124:127], v[156:159]\n\t"
"ds_read_b128 v[212:215], v246 offset:24576\n\t"
"s_add_u32 s87, s35, 24576\n\t"
"v_mfma_f32_16x16x32_bf16 v[156:159], v[216:219], v[116:119], v[156:159]\n\t"
"buffer_load_dwordx4 v[100:103], v240, s[56:59], s87 offen offset:0 sc1\n\t"
"s_waitcnt lgkmcnt(4)\n\t"
"v_mfma_f32_16x16x32_bf16 v[160:163], v[192:195], v[112:115], v[160:163]\n\t"
"ds_read_b128 v[216:219], v246 offset:5120\n\t"
"v_mfma_f32_16x16x32_bf16 v[160:163], v[196:199], v[120:123], v[160:163]\n\t"
"v_mfma_f32_16x16x32_bf16 v[164:167], v[196:199], v[124:127], v[164:167]\n\t"
"ds_read_b128 v[220:223], v246 offset:25600\n\t"
"v_mfma_f32_16x16x32_bf16 v[164:167], v[192:195], v[116:119], v[164:167]\n\t"
"s_waitcnt lgkmcnt(4)\n\t"
"v_mfma_f32_16x16x32_bf16 v[168:171], v[200:203], v[112:115], v[168:171]\n\t"
"ds_read_b128 v[192:195], v246 offset:6144\n\t"
"v_mfma_f32_16x16x32_bf16 v[168:171], v[204:207], v[120:123], v[168:171]\n\t"
"v_mfma_f32_16x16x32_bf16 v[172:175], v[204:207], v[124:127], v[172:175]\n\t"
"ds_read_b128 v[196:199], v246 offset:26624\n\t"
"v_mfma_f32_16x16x32_bf16 v[172:175], v[200:203], v[116:119], v[172:175]\n\t"
"s_waitcnt lgkmcnt(4)\n\t"
"v_mfma_f32_16x16x32_bf16 v[176:179], v[208:211], v[112:115], v[176:179]\n\t"
"ds_read_b128 v[200:203], v246 offset:7168\n\t"
"v_mfma_f32_16x16x32_bf16 v[176:179], v[212:215], v[120:123], v[176:179]\n\t"
"ds_read_b128 v[204:207], v246 offset:27648\n\t"
"v_mfma_f32_16x16x32_bf16 v[180:183], v[212:215], v[124:127], v[180:183]\n\t"
"s_add_u32 s87, s34, 26624\n\t"
"v_mfma_f32_16x16x32_bf16 v[180:183], v[208:211], v[116:119], v[180:183]\n\t"
"buffer_load_dwordx4 v[104:107], v240, s[56:59], s87 offen offset:0 sc1\n\t"
"s_waitcnt lgkmcnt(4)\n\t"
"v_mfma_f32_16x16x32_bf16 v[184:187], v[216:219], v[112:115], v[184:187]\n\t"
"ds_read_b128 v[208:211], v246 offset:8192\n\t"
"v_mfma_f32_16x16x32_bf16 v[184:187], v[220:223], v[120:123], v[184:187]\n\t"
"ds_read_b128 v[212:215], v246 offset:28672\n\t"
"v_mfma_f32_16x16x32_bf16 v[188:191], v[220:223], v[124:127], v[188:191]\n\t"
"s_add_u32 s87, s35, 26624\n\t"
"v_mfma_f32_16x16x32_bf16 v[188:191], v[216:219], v[116:119], v[188:191]\n\t"
"buffer_load_dwordx4 v[108:111], v240, s[56:59], s87 offen offset:0 sc1\n\t"
"s_waitcnt lgkmcnt(4)\n\t"
"v_mfma_f32_16x16x32_bf16 v[80:83], v[192:195], v[112:115], v[80:83]\n\t"
"ds_read_b128 v[216:219], v246 offset:9216\n\t"
"v_mfma_f32_16x16x32_bf16 v[80:83], v[196:199], v[120:123], v[80:83]\n\t"
"v_mfma_f32_16x16x32_bf16 v[84:87], v[196:199], v[124:127], v[84:87]\n\t"
"ds_read_b128 v[220:223], v246 offset:29696\n\t"
"v_mfma_f32_16x16x32_bf16 v[84:87], v[192:195], v[116:119], v[84:87]\n\t"
"s_waitcnt vmcnt(12)\n\t"
"s_barrier\n\t"
"v_add_u32 v246, 40960, v253\n\t"
"s_waitcnt lgkmcnt(4)\n\t"
"v_mfma_f32_16x16x32_bf16 v[88:91], v[200:203], v[112:115], v[88:91]\n\t"
"ds_read_b128 v[192:195], v246 offset:0\n\t"
"s_add_u32 m0, s12, 122880\n\t"
"v_mfma_f32_16x16x32_bf16 v[88:91], v[204:207], v[120:123], v[88:91]\n\t"
"s_add_u32 s76, s20, 1146880\n\t"
"v_mfma_f32_16x16x32_bf16 v[92:95], v[204:207], v[124:127], v[92:95]\n\t"
"buffer_load_dwordx4 v240, s[56:59], s76 offen sc1 lds\n\t"
"v_mfma_f32_16x16x32_bf16 v[92:95], v[200:203], v[116:119], v[92:95]\n\t"
"ds_read_b128 v[196:199], v246 offset:20480\n\t"
"s_waitcnt lgkmcnt(4)\n\t"
"v_mfma_f32_16x16x32_bf16 v[224:227], v[208:211], v[112:115], v[224:227]\n\t"
"ds_read_b128 v[200:203], v246 offset:1024\n\t"
"s_add_u32 m0, s12, 123904\n\t"
"v_mfma_f32_16x16x32_bf16 v[224:227], v[212:215], v[120:123], v[224:227]\n\t"
"s_add_u32 s76, s20, 1147904\n\t"
"v_mfma_f32_16x16x32_bf16 v[228:231], v[212:215], v[124:127], v[228:231]\n\t"
"buffer_load_dwordx4 v240, s[56:59], s76 offen sc1 lds\n\t"
"v_mfma_f32_16x16x32_bf16 v[228:231], v[208:211], v[116:119], v[228:231]\n\t"
"ds_read_b128 v[204:207], v246 offset:21504\n\t"
"s_waitcnt lgkmcnt(4)\n\t"
"v_mfma_f32_16x16x32_bf16 v[232:235], v[216:219], v[112:115], v[232:235]\n\t"
"ds_read_b128 v[208:211], v246 offset:2048\n\t"
"s_add_u32 m0, s12, 124928\n\t"
"v_mfma_f32_16x16x32_bf16 v[232:235], v[220:223], v[120:123], v[232:235]\n\t"
"s_add_u32 s76, s20, 1148928\n\t"
"v_mfma_f32_16x16x32_bf16 v[236:239], v[220:223], v[124:127], v[236:239]\n\t"
"buffer_load_dwordx4 v240, s[56:59], s76 offen sc1 lds\n\t"
"v_mfma_f32_16x16x32_bf16 v[236:239], v[216:219], v[116:119], v[236:239]\n\t"
"ds_read_b128 v[212:215], v246 offset:22528\n\t"
"s_waitcnt vmcnt(12) lgkmcnt(4)\n\t"
"v_mfma_f32_16x16x32_bf16 v[144:147], v[192:195], v[128:131], v[144:147]\n\t"
"ds_read_b128 v[216:219], v246 offset:3072\n\t"
"s_add_u32 m0, s12, 125952\n\t"
"v_mfma_f32_16x16x32_bf16 v[144:147], v[196:199], v[136:139], v[144:147]\n\t"
"s_add_u32 s76, s20, 1149952\n\t"
"buffer_load_dwordx4 v240, s[56:59], s76 offen sc1 lds\n\t"
"v_mfma_f32_16x16x32_bf16 v[148:151], v[196:199], v[140:143], v[148:151]\n\t"
"ds_read_b128 v[220:223], v246 offset:23552\n\t"
"s_add_u32 s87, s34, 28672\n\t"
"v_mfma_f32_16x16x32_bf16 v[148:151], v[192:195], v[132:135], v[148:151]\n\t"
"buffer_load_dwordx4 v[112:115], v240, s[56:59], s87 offen offset:0 sc1\n\t"
"s_waitcnt lgkmcnt(4)\n\t"
"v_mfma_f32_16x16x32_bf16 v[152:155], v[200:203], v[128:131], v[152:155]\n\t"
"ds_read_b128 v[192:195], v246 offset:4096\n\t"
"s_add_u32 m0, s12, 126976\n\t"
"v_mfma_f32_16x16x32_bf16 v[152:155], v[204:207], v[136:139], v[152:155]\n\t"
"s_add_u32 s76, s20, 1150976\n\t"
"buffer_load_dwordx4 v240, s[56:59], s76 offen sc1 lds\n\t"
"v_mfma_f32_16x16x32_bf16 v[156:159], v[204:207], v[140:143], v[156:159]\n\t"
"ds_read_b128 v[196:199], v246 offset:24576\n\t"
"s_add_u32 s87, s35, 28672\n\t"
"v_mfma_f32_16x16x32_bf16 v[156:159], v[200:203], v[132:135], v[156:159]\n\t"
"buffer_load_dwordx4 v[116:119], v240, s[56:59], s87 offen offset:0 sc1\n\t"
"s_waitcnt lgkmcnt(4)\n\t"
"v_mfma_f32_16x16x32_bf16 v[160:163], v[208:211], v[128:131], v[160:163]\n\t"
"ds_read_b128 v[200:203], v246 offset:5120\n\t"
"v_mfma_f32_16x16x32_bf16 v[160:163], v[212:215], v[136:139], v[160:163]\n\t"
"v_mfma_f32_16x16x32_bf16 v[164:167], v[212:215], v[140:143], v[164:167]\n\t"
"ds_read_b128 v[204:207], v246 offset:25600\n\t"
"v_mfma_f32_16x16x32_bf16 v[164:167], v[208:211], v[132:135], v[164:167]\n\t"
"s_waitcnt lgkmcnt(4)\n\t"
"v_mfma_f32_16x16x32_bf16 v[168:171], v[216:219], v[128:131], v[168:171]\n\t"
"ds_read_b128 v[208:211], v246 offset:6144\n\t"
"v_mfma_f32_16x16x32_bf16 v[168:171], v[220:223], v[136:139], v[168:171]\n\t"
"v_mfma_f32_16x16x32_bf16 v[172:175], v[220:223], v[140:143], v[172:175]\n\t"
"ds_read_b128 v[212:215], v246 offset:26624\n\t"
"v_mfma_f32_16x16x32_bf16 v[172:175], v[216:219], v[132:135], v[172:175]\n\t"
"s_waitcnt lgkmcnt(4)\n\t"
"v_mfma_f32_16x16x32_bf16 v[176:179], v[192:195], v[128:131], v[176:179]\n\t"
"ds_read_b128 v[216:219], v246 offset:7168\n\t"
"v_mfma_f32_16x16x32_bf16 v[176:179], v[196:199], v[136:139], v[176:179]\n\t"
"ds_read_b128 v[220:223], v246 offset:27648\n\t"
"v_mfma_f32_16x16x32_bf16 v[180:183], v[196:199], v[140:143], v[180:183]\n\t"
"s_add_u32 s87, s34, 30720\n\t"
"v_mfma_f32_16x16x32_bf16 v[180:183], v[192:195], v[132:135], v[180:183]\n\t"
"buffer_load_dwordx4 v[120:123], v240, s[56:59], s87 offen offset:0 sc1\n\t"
"s_waitcnt lgkmcnt(4)\n\t"
"v_mfma_f32_16x16x32_bf16 v[184:187], v[200:203], v[128:131], v[184:187]\n\t"
"ds_read_b128 v[192:195], v246 offset:8192\n\t"
"v_mfma_f32_16x16x32_bf16 v[184:187], v[204:207], v[136:139], v[184:187]\n\t"
"ds_read_b128 v[196:199], v246 offset:28672\n\t"
"v_mfma_f32_16x16x32_bf16 v[188:191], v[204:207], v[140:143], v[188:191]\n\t"
"s_add_u32 s87, s35, 30720\n\t"
"v_mfma_f32_16x16x32_bf16 v[188:191], v[200:203], v[132:135], v[188:191]\n\t"
"buffer_load_dwordx4 v[124:127], v240, s[56:59], s87 offen offset:0 sc1\n\t"
"s_waitcnt lgkmcnt(4)\n\t"
"v_mfma_f32_16x16x32_bf16 v[80:83], v[208:211], v[128:131], v[80:83]\n\t"
"ds_read_b128 v[200:203], v246 offset:9216\n\t"
"v_mfma_f32_16x16x32_bf16 v[80:83], v[212:215], v[136:139], v[80:83]\n\t"
"v_mfma_f32_16x16x32_bf16 v[84:87], v[212:215], v[140:143], v[84:87]\n\t"
"ds_read_b128 v[204:207], v246 offset:29696\n\t"
"v_mfma_f32_16x16x32_bf16 v[84:87], v[208:211], v[132:135], v[84:87]\n\t"
"s_waitcnt vmcnt(12)\n\t"
"s_barrier\n\t"
"v_add_u32 v246, 81920, v253\n\t"
"s_waitcnt lgkmcnt(4)\n\t"
"v_mfma_f32_16x16x32_bf16 v[88:91], v[216:219], v[128:131], v[88:91]\n\t"
"ds_read_b128 v[208:211], v246 offset:0\n\t"
"s_add_u32 m0, s12, 0\n\t"
"v_mfma_f32_16x16x32_bf16 v[88:91], v[220:223], v[136:139], v[88:91]\n\t"
"s_add_u32 s76, s20, 1310720\n\t"
"v_mfma_f32_16x16x32_bf16 v[92:95], v[220:223], v[140:143], v[92:95]\n\t"
"buffer_load_dwordx4 v240, s[56:59], s76 offen sc1 lds\n\t"
"v_mfma_f32_16x16x32_bf16 v[92:95], v[216:219], v[132:135], v[92:95]\n\t"
"ds_read_b128 v[212:215], v246 offset:20480\n\t"
"s_waitcnt lgkmcnt(4)\n\t"
"v_mfma_f32_16x16x32_bf16 v[224:227], v[192:195], v[128:131], v[224:227]\n\t"
"ds_read_b128 v[216:219], v246 offset:1024\n\t"
"s_add_u32 m0, s12, 1024\n\t"
"v_mfma_f32_16x16x32_bf16 v[224:227], v[196:199], v[136:139], v[224:227]\n\t"
"s_add_u32 s76, s20, 1311744\n\t"
"v_mfma_f32_16x16x32_bf16 v[228:231], v[196:199], v[140:143], v[228:231]\n\t"
"buffer_load_dwordx4 v240, s[56:59], s76 offen sc1 lds\n\t"
"v_mfma_f32_16x16x32_bf16 v[228:231], v[192:195], v[132:135], v[228:231]\n\t"
"ds_read_b128 v[220:223], v246 offset:21504\n\t"
"s_waitcnt lgkmcnt(4)\n\t"
"v_mfma_f32_16x16x32_bf16 v[232:235], v[200:203], v[128:131], v[232:235]\n\t"
"ds_read_b128 v[192:195], v246 offset:2048\n\t"
"s_add_u32 m0, s12, 2048\n\t"
"v_mfma_f32_16x16x32_bf16 v[232:235], v[204:207], v[136:139], v[232:235]\n\t"
"s_add_u32 s76, s20, 1312768\n\t"
"v_mfma_f32_16x16x32_bf16 v[236:239], v[204:207], v[140:143], v[236:239]\n\t"
"buffer_load_dwordx4 v240, s[56:59], s76 offen sc1 lds\n\t"
"v_mfma_f32_16x16x32_bf16 v[236:239], v[200:203], v[132:135], v[236:239]\n\t"
"ds_read_b128 v[196:199], v246 offset:22528\n\t"
"s_waitcnt vmcnt(12) lgkmcnt(4)\n\t"
"v_mfma_f32_16x16x32_bf16 v[144:147], v[208:211], v[96:99], v[144:147]\n\t"
"ds_read_b128 v[200:203], v246 offset:3072\n\t"
"s_add_u32 m0, s12, 3072\n\t"
"v_mfma_f32_16x16x32_bf16 v[144:147], v[212:215], v[104:107], v[144:147]\n\t"
"s_add_u32 s76, s20, 1313792\n\t"
"buffer_load_dwordx4 v240, s[56:59], s76 offen sc1 lds\n\t"
"v_mfma_f32_16x16x32_bf16 v[148:151], v[212:215], v[108:111], v[148:151]\n\t"
"ds_read_b128 v[204:207], v246 offset:23552\n\t"
"s_add_u32 s87, s34, 32768\n\t"
"v_mfma_f32_16x16x32_bf16 v[148:151], v[208:211], v[100:103], v[148:151]\n\t"
"buffer_load_dwordx4 v[128:131], v240, s[56:59], s87 offen offset:0 sc1\n\t"
"s_waitcnt lgkmcnt(4)\n\t"
"v_mfma_f32_16x16x32_bf16 v[152:155], v[216:219], v[96:99], v[152:155]\n\t"
"ds_read_b128 v[208:211], v246 offset:4096\n\t"
"s_add_u32 m0, s12, 4096\n\t"
"v_mfma_f32_16x16x32_bf16 v[152:155], v[220:223], v[104:107], v[152:155]\n\t"
"s_add_u32 s76, s20, 1314816\n\t"
"buffer_load_dwordx4 v240, s[56:59], s76 offen sc1 lds\n\t"
"v_mfma_f32_16x16x32_bf16 v[156:159], v[220:223], v[108:111], v[156:159]\n\t"
"ds_read_b128 v[212:215], v246 offset:24576\n\t"
"s_add_u32 s87, s35, 32768\n\t"
"v_mfma_f32_16x16x32_bf16 v[156:159], v[216:219], v[100:103], v[156:159]\n\t"
"buffer_load_dwordx4 v[132:135], v240, s[56:59], s87 offen offset:0 sc1\n\t"
"s_waitcnt lgkmcnt(4)\n\t"
"v_mfma_f32_16x16x32_bf16 v[160:163], v[192:195], v[96:99], v[160:163]\n\t"
"ds_read_b128 v[216:219], v246 offset:5120\n\t"
"v_mfma_f32_16x16x32_bf16 v[160:163], v[196:199], v[104:107], v[160:163]\n\t"
"v_mfma_f32_16x16x32_bf16 v[164:167], v[196:199], v[108:111], v[164:167]\n\t"
"ds_read_b128 v[220:223], v246 offset:25600\n\t"
"v_mfma_f32_16x16x32_bf16 v[164:167], v[192:195], v[100:103], v[164:167]\n\t"
"s_waitcnt lgkmcnt(4)\n\t"
"v_mfma_f32_16x16x32_bf16 v[168:171], v[200:203], v[96:99], v[168:171]\n\t"
"ds_read_b128 v[192:195], v246 offset:6144\n\t"
"v_mfma_f32_16x16x32_bf16 v[168:171], v[204:207], v[104:107], v[168:171]\n\t"
"v_mfma_f32_16x16x32_bf16 v[172:175], v[204:207], v[108:111], v[172:175]\n\t"
"ds_read_b128 v[196:199], v246 offset:26624\n\t"
"v_mfma_f32_16x16x32_bf16 v[172:175], v[200:203], v[100:103], v[172:175]\n\t"
"s_waitcnt lgkmcnt(4)\n\t"
"v_mfma_f32_16x16x32_bf16 v[176:179], v[208:211], v[96:99], v[176:179]\n\t"
"ds_read_b128 v[200:203], v246 offset:7168\n\t"
"v_mfma_f32_16x16x32_bf16 v[176:179], v[212:215], v[104:107], v[176:179]\n\t"
"ds_read_b128 v[204:207], v246 offset:27648\n\t"
"v_mfma_f32_16x16x32_bf16 v[180:183], v[212:215], v[108:111], v[180:183]\n\t"
"s_add_u32 s87, s34, 34816\n\t"
"v_mfma_f32_16x16x32_bf16 v[180:183], v[208:211], v[100:103], v[180:183]\n\t"
"buffer_load_dwordx4 v[136:139], v240, s[56:59], s87 offen offset:0 sc1\n\t"
"s_waitcnt lgkmcnt(4)\n\t"
"v_mfma_f32_16x16x32_bf16 v[184:187], v[216:219], v[96:99], v[184:187]\n\t"
"ds_read_b128 v[208:211], v246 offset:8192\n\t"
"v_mfma_f32_16x16x32_bf16 v[184:187], v[220:223], v[104:107], v[184:187]\n\t"
"ds_read_b128 v[212:215], v246 offset:28672\n\t"
"v_mfma_f32_16x16x32_bf16 v[188:191], v[220:223], v[108:111], v[188:191]\n\t"
"s_add_u32 s87, s35, 34816\n\t"
"v_mfma_f32_16x16x32_bf16 v[188:191], v[216:219], v[100:103], v[188:191]\n\t"
"buffer_load_dwordx4 v[140:143], v240, s[56:59], s87 offen offset:0 sc1\n\t"
"s_waitcnt lgkmcnt(4)\n\t"
"v_mfma_f32_16x16x32_bf16 v[80:83], v[192:195], v[96:99], v[80:83]\n\t"
"ds_read_b128 v[216:219], v246 offset:9216\n\t"
"v_mfma_f32_16x16x32_bf16 v[80:83], v[196:199], v[104:107], v[80:83]\n\t"
"v_mfma_f32_16x16x32_bf16 v[84:87], v[196:199], v[108:111], v[84:87]\n\t"
"ds_read_b128 v[220:223], v246 offset:29696\n\t"
"v_mfma_f32_16x16x32_bf16 v[84:87], v[192:195], v[100:103], v[84:87]\n\t"
"s_waitcnt vmcnt(12)\n\t"
"s_barrier\n\t"
"v_add_u32 v246, 122880, v253\n\t"
"s_waitcnt lgkmcnt(4)\n\t"
"v_mfma_f32_16x16x32_bf16 v[88:91], v[200:203], v[96:99], v[88:91]\n\t"
"ds_read_b128 v[192:195], v246 offset:0\n\t"
"s_add_u32 m0, s12, 40960\n\t"
"v_mfma_f32_16x16x32_bf16 v[88:91], v[204:207], v[104:107], v[88:91]\n\t"
"s_add_u32 s76, s20, 1474560\n\t"
"v_mfma_f32_16x16x32_bf16 v[92:95], v[204:207], v[108:111], v[92:95]\n\t"
"buffer_load_dwordx4 v240, s[56:59], s76 offen sc1 lds\n\t"
"v_mfma_f32_16x16x32_bf16 v[92:95], v[200:203], v[100:103], v[92:95]\n\t"
"ds_read_b128 v[196:199], v246 offset:20480\n\t"
"s_waitcnt lgkmcnt(4)\n\t"
"v_mfma_f32_16x16x32_bf16 v[224:227], v[208:211], v[96:99], v[224:227]\n\t"
"ds_read_b128 v[200:203], v246 offset:1024\n\t"
"s_add_u32 m0, s12, 41984\n\t"
"v_mfma_f32_16x16x32_bf16 v[224:227], v[212:215], v[104:107], v[224:227]\n\t"
"s_add_u32 s76, s20, 1475584\n\t"
"v_mfma_f32_16x16x32_bf16 v[228:231], v[212:215], v[108:111], v[228:231]\n\t"
"buffer_load_dwordx4 v240, s[56:59], s76 offen sc1 lds\n\t"
"v_mfma_f32_16x16x32_bf16 v[228:231], v[208:211], v[100:103], v[228:231]\n\t"
"ds_read_b128 v[204:207], v246 offset:21504\n\t"
"s_waitcnt lgkmcnt(4)\n\t"
"v_mfma_f32_16x16x32_bf16 v[232:235], v[216:219], v[96:99], v[232:235]\n\t"
"ds_read_b128 v[208:211], v246 offset:2048\n\t"
"s_add_u32 m0, s12, 43008\n\t"
"v_mfma_f32_16x16x32_bf16 v[232:235], v[220:223], v[104:107], v[232:235]\n\t"
"s_add_u32 s76, s20, 1476608\n\t"
"v_mfma_f32_16x16x32_bf16 v[236:239], v[220:223], v[108:111], v[236:239]\n\t"
"buffer_load_dwordx4 v240, s[56:59], s76 offen sc1 lds\n\t"
"v_mfma_f32_16x16x32_bf16 v[236:239], v[216:219], v[100:103], v[236:239]\n\t"
"ds_read_b128 v[212:215], v246 offset:22528\n\t"
"s_waitcnt vmcnt(12) lgkmcnt(4)\n\t"
"v_mfma_f32_16x16x32_bf16 v[144:147], v[192:195], v[112:115], v[144:147]\n\t"
"ds_read_b128 v[216:219], v246 offset:3072\n\t"
"s_add_u32 m0, s12, 44032\n\t"
"v_mfma_f32_16x16x32_bf16 v[144:147], v[196:199], v[120:123], v[144:147]\n\t"
"s_add_u32 s76, s20, 1477632\n\t"
"buffer_load_dwordx4 v240, s[56:59], s76 offen sc1 lds\n\t"
"v_mfma_f32_16x16x32_bf16 v[148:151], v[196:199], v[124:127], v[148:151]\n\t"
"ds_read_b128 v[220:223], v246 offset:23552\n\t"
"s_add_u32 s87, s34, 36864\n\t"
"v_mfma_f32_16x16x32_bf16 v[148:151], v[192:195], v[116:119], v[148:151]\n\t"
"buffer_load_dwordx4 v[96:99], v240, s[56:59], s87 offen offset:0 sc1\n\t"
"s_waitcnt lgkmcnt(4)\n\t"
"v_mfma_f32_16x16x32_bf16 v[152:155], v[200:203], v[112:115], v[152:155]\n\t"
"ds_read_b128 v[192:195], v246 offset:4096\n\t"
"s_add_u32 m0, s12, 45056\n\t"
"v_mfma_f32_16x16x32_bf16 v[152:155], v[204:207], v[120:123], v[152:155]\n\t"
"s_add_u32 s76, s20, 1478656\n\t"
"buffer_load_dwordx4 v240, s[56:59], s76 offen sc1 lds\n\t"
"v_mfma_f32_16x16x32_bf16 v[156:159], v[204:207], v[124:127], v[156:159]\n\t"
"ds_read_b128 v[196:199], v246 offset:24576\n\t"
"s_add_u32 s87, s35, 36864\n\t"
"v_mfma_f32_16x16x32_bf16 v[156:159], v[200:203], v[116:119], v[156:159]\n\t"
"buffer_load_dwordx4 v[100:103], v240, s[56:59], s87 offen offset:0 sc1\n\t"
"s_waitcnt lgkmcnt(4)\n\t"
"v_mfma_f32_16x16x32_bf16 v[160:163], v[208:211], v[112:115], v[160:163]\n\t"
"ds_read_b128 v[200:203], v246 offset:5120\n\t"
"v_mfma_f32_16x16x32_bf16 v[160:163], v[212:215], v[120:123], v[160:163]\n\t"
"v_mfma_f32_16x16x32_bf16 v[164:167], v[212:215], v[124:127], v[164:167]\n\t"
"ds_read_b128 v[204:207], v246 offset:25600\n\t"
"v_mfma_f32_16x16x32_bf16 v[164:167], v[208:211], v[116:119], v[164:167]\n\t"
"s_waitcnt lgkmcnt(4)\n\t"
"v_mfma_f32_16x16x32_bf16 v[168:171], v[216:219], v[112:115], v[168:171]\n\t"
"ds_read_b128 v[208:211], v246 offset:6144\n\t"
"v_mfma_f32_16x16x32_bf16 v[168:171], v[220:223], v[120:123], v[168:171]\n\t"
"v_mfma_f32_16x16x32_bf16 v[172:175], v[220:223], v[124:127], v[172:175]\n\t"
"ds_read_b128 v[212:215], v246 offset:26624\n\t"
"v_mfma_f32_16x16x32_bf16 v[172:175], v[216:219], v[116:119], v[172:175]\n\t"
"s_waitcnt lgkmcnt(4)\n\t"
"v_mfma_f32_16x16x32_bf16 v[176:179], v[192:195], v[112:115], v[176:179]\n\t"
"ds_read_b128 v[216:219], v246 offset:7168\n\t"
"v_mfma_f32_16x16x32_bf16 v[176:179], v[196:199], v[120:123], v[176:179]\n\t"
"ds_read_b128 v[220:223], v246 offset:27648\n\t"
"v_mfma_f32_16x16x32_bf16 v[180:183], v[196:199], v[124:127], v[180:183]\n\t"
"s_add_u32 s87, s34, 38912\n\t"
"v_mfma_f32_16x16x32_bf16 v[180:183], v[192:195], v[116:119], v[180:183]\n\t"
"buffer_load_dwordx4 v[104:107], v240, s[56:59], s87 offen offset:0 sc1\n\t"
"s_waitcnt lgkmcnt(4)\n\t"
"v_mfma_f32_16x16x32_bf16 v[184:187], v[200:203], v[112:115], v[184:187]\n\t"
"ds_read_b128 v[192:195], v246 offset:8192\n\t"
"v_mfma_f32_16x16x32_bf16 v[184:187], v[204:207], v[120:123], v[184:187]\n\t"
"ds_read_b128 v[196:199], v246 offset:28672\n\t"
"v_mfma_f32_16x16x32_bf16 v[188:191], v[204:207], v[124:127], v[188:191]\n\t"
"s_add_u32 s87, s35, 38912\n\t"
"v_mfma_f32_16x16x32_bf16 v[188:191], v[200:203], v[116:119], v[188:191]\n\t"
"buffer_load_dwordx4 v[108:111], v240, s[56:59], s87 offen offset:0 sc1\n\t"
"s_waitcnt lgkmcnt(4)\n\t"
"v_mfma_f32_16x16x32_bf16 v[80:83], v[208:211], v[112:115], v[80:83]\n\t"
"ds_read_b128 v[200:203], v246 offset:9216\n\t"
"v_mfma_f32_16x16x32_bf16 v[80:83], v[212:215], v[120:123], v[80:83]\n\t"
"v_mfma_f32_16x16x32_bf16 v[84:87], v[212:215], v[124:127], v[84:87]\n\t"
"ds_read_b128 v[204:207], v246 offset:29696\n\t"
"v_mfma_f32_16x16x32_bf16 v[84:87], v[208:211], v[116:119], v[84:87]\n\t"
"s_waitcnt vmcnt(12)\n\t"
"s_barrier\n\t"
"v_add_u32 v246, 0, v253\n\t"
"s_waitcnt lgkmcnt(4)\n\t"
"v_mfma_f32_16x16x32_bf16 v[88:91], v[216:219], v[112:115], v[88:91]\n\t"
"ds_read_b128 v[208:211], v246 offset:0\n\t"
"s_add_u32 m0, s12, 81920\n\t"
"v_mfma_f32_16x16x32_bf16 v[88:91], v[220:223], v[120:123], v[88:91]\n\t"
"s_add_u32 s76, s20, 1638400\n\t"
"v_mfma_f32_16x16x32_bf16 v[92:95], v[220:223], v[124:127], v[92:95]\n\t"
"buffer_load_dwordx4 v240, s[56:59], s76 offen sc1 lds\n\t"
"v_mfma_f32_16x16x32_bf16 v[92:95], v[216:219], v[116:119], v[92:95]\n\t"
"ds_read_b128 v[212:215], v246 offset:20480\n\t"
"s_waitcnt lgkmcnt(4)\n\t"
"v_mfma_f32_16x16x32_bf16 v[224:227], v[192:195], v[112:115], v[224:227]\n\t"
"ds_read_b128 v[216:219], v246 offset:1024\n\t"
"s_add_u32 m0, s12, 82944\n\t"
"v_mfma_f32_16x16x32_bf16 v[224:227], v[196:199], v[120:123], v[224:227]\n\t"
"s_add_u32 s76, s20, 1639424\n\t"
"v_mfma_f32_16x16x32_bf16 v[228:231], v[196:199], v[124:127], v[228:231]\n\t"
"buffer_load_dwordx4 v240, s[56:59], s76 offen sc1 lds\n\t"
"v_mfma_f32_16x16x32_bf16 v[228:231], v[192:195], v[116:119], v[228:231]\n\t"
"ds_read_b128 v[220:223], v246 offset:21504\n\t"
"s_waitcnt lgkmcnt(4)\n\t"
"v_mfma_f32_16x16x32_bf16 v[232:235], v[200:203], v[112:115], v[232:235]\n\t"
"ds_read_b128 v[192:195], v246 offset:2048\n\t"
"s_add_u32 m0, s12, 83968\n\t"
"v_mfma_f32_16x16x32_bf16 v[232:235], v[204:207], v[120:123], v[232:235]\n\t"
"s_add_u32 s76, s20, 1640448\n\t"
"v_mfma_f32_16x16x32_bf16 v[236:239], v[204:207], v[124:127], v[236:239]\n\t"
"buffer_load_dwordx4 v240, s[56:59], s76 offen sc1 lds\n\t"
"v_mfma_f32_16x16x32_bf16 v[236:239], v[200:203], v[116:119], v[236:239]\n\t"
"ds_read_b128 v[196:199], v246 offset:22528\n\t"
"s_waitcnt vmcnt(12) lgkmcnt(4)\n\t"
"v_mfma_f32_16x16x32_bf16 v[144:147], v[208:211], v[128:131], v[144:147]\n\t"
"ds_read_b128 v[200:203], v246 offset:3072\n\t"
"s_add_u32 m0, s12, 84992\n\t"
"v_mfma_f32_16x16x32_bf16 v[144:147], v[212:215], v[136:139], v[144:147]\n\t"
"s_add_u32 s76, s20, 1641472\n\t"
"buffer_load_dwordx4 v240, s[56:59], s76 offen sc1 lds\n\t"
"v_mfma_f32_16x16x32_bf16 v[148:151], v[212:215], v[140:143], v[148:151]\n\t"
"ds_read_b128 v[204:207], v246 offset:23552\n\t"
"s_add_u32 s87, s34, 40960\n\t"
"v_mfma_f32_16x16x32_bf16 v[148:151], v[208:211], v[132:135], v[148:151]\n\t"
"buffer_load_dwordx4 v[112:115], v240, s[56:59], s87 offen offset:0 sc1\n\t"
"s_waitcnt lgkmcnt(4)\n\t"
"v_mfma_f32_16x16x32_bf16 v[152:155], v[216:219], v[128:131], v[152:155]\n\t"
"ds_read_b128 v[208:211], v246 offset:4096\n\t"
"s_add_u32 m0, s12, 86016\n\t"
"v_mfma_f32_16x16x32_bf16 v[152:155], v[220:223], v[136:139], v[152:155]\n\t"
"s_add_u32 s76, s20, 1642496\n\t"
"buffer_load_dwordx4 v240, s[56:59], s76 offen sc1 lds\n\t"
"v_mfma_f32_16x16x32_bf16 v[156:159], v[220:223], v[140:143], v[156:159]\n\t"
"ds_read_b128 v[212:215], v246 offset:24576\n\t"
"s_add_u32 s87, s35, 40960\n\t"
"v_mfma_f32_16x16x32_bf16 v[156:159], v[216:219], v[132:135], v[156:159]\n\t"
"buffer_load_dwordx4 v[116:119], v240, s[56:59], s87 offen offset:0 sc1\n\t"
"s_waitcnt lgkmcnt(4)\n\t"
"v_mfma_f32_16x16x32_bf16 v[160:163], v[192:195], v[128:131], v[160:163]\n\t"
"ds_read_b128 v[216:219], v246 offset:5120\n\t"
"v_mfma_f32_16x16x32_bf16 v[160:163], v[196:199], v[136:139], v[160:163]\n\t"
"v_mfma_f32_16x16x32_bf16 v[164:167], v[196:199], v[140:143], v[164:167]\n\t"
"ds_read_b128 v[220:223], v246 offset:25600\n\t"
"v_mfma_f32_16x16x32_bf16 v[164:167], v[192:195], v[132:135], v[164:167]\n\t"
"s_waitcnt lgkmcnt(4)\n\t"
"v_mfma_f32_16x16x32_bf16 v[168:171], v[200:203], v[128:131], v[168:171]\n\t"
"ds_read_b128 v[192:195], v246 offset:6144\n\t"
"v_mfma_f32_16x16x32_bf16 v[168:171], v[204:207], v[136:139], v[168:171]\n\t"
"v_mfma_f32_16x16x32_bf16 v[172:175], v[204:207], v[140:143], v[172:175]\n\t"
"ds_read_b128 v[196:199], v246 offset:26624\n\t"
"v_mfma_f32_16x16x32_bf16 v[172:175], v[200:203], v[132:135], v[172:175]\n\t"
"s_waitcnt lgkmcnt(4)\n\t"
"v_mfma_f32_16x16x32_bf16 v[176:179], v[208:211], v[128:131], v[176:179]\n\t"
"ds_read_b128 v[200:203], v246 offset:7168\n\t"
"v_mfma_f32_16x16x32_bf16 v[176:179], v[212:215], v[136:139], v[176:179]\n\t"
"ds_read_b128 v[204:207], v246 offset:27648\n\t"
"v_mfma_f32_16x16x32_bf16 v[180:183], v[212:215], v[140:143], v[180:183]\n\t"
"s_add_u32 s87, s34, 43008\n\t"
"v_mfma_f32_16x16x32_bf16 v[180:183], v[208:211], v[132:135], v[180:183]\n\t"
"buffer_load_dwordx4 v[120:123], v240, s[56:59], s87 offen offset:0 sc1\n\t"
"s_waitcnt lgkmcnt(4)\n\t"
"v_mfma_f32_16x16x32_bf16 v[184:187], v[216:219], v[128:131], v[184:187]\n\t"
"ds_read_b128 v[208:211], v246 offset:8192\n\t"
"v_mfma_f32_16x16x32_bf16 v[184:187], v[220:223], v[136:139], v[184:187]\n\t"
"ds_read_b128 v[212:215], v246 offset:28672\n\t"
"v_mfma_f32_16x16x32_bf16 v[188:191], v[220:223], v[140:143], v[188:191]\n\t"
"s_add_u32 s87, s35, 43008\n\t"
"v_mfma_f32_16x16x32_bf16 v[188:191], v[216:219], v[132:135], v[188:191]\n\t"
"buffer_load_dwordx4 v[124:127], v240, s[56:59], s87 offen offset:0 sc1\n\t"
"s_waitcnt lgkmcnt(4)\n\t"
"v_mfma_f32_16x16x32_bf16 v[80:83], v[192:195], v[128:131], v[80:83]\n\t"
"ds_read_b128 v[216:219], v246 offset:9216\n\t"
"v_mfma_f32_16x16x32_bf16 v[80:83], v[196:199], v[136:139], v[80:83]\n\t"
"v_mfma_f32_16x16x32_bf16 v[84:87], v[196:199], v[140:143], v[84:87]\n\t"
"ds_read_b128 v[220:223], v246 offset:29696\n\t"
"v_mfma_f32_16x16x32_bf16 v[84:87], v[192:195], v[132:135], v[84:87]\n\t"
"s_waitcnt vmcnt(12)\n\t"
"s_barrier\n\t"
"v_add_u32 v246, 40960, v253\n\t"
"s_waitcnt lgkmcnt(4)\n\t"
"v_mfma_f32_16x16x32_bf16 v[88:91], v[200:203], v[128:131], v[88:91]\n\t"
"ds_read_b128 v[192:195], v246 offset:0\n\t"
"s_add_u32 m0, s12, 122880\n\t"
"v_mfma_f32_16x16x32_bf16 v[88:91], v[204:207], v[136:139], v[88:91]\n\t"
"s_add_u32 s76, s20, 1802240\n\t"
"v_mfma_f32_16x16x32_bf16 v[92:95], v[204:207], v[140:143], v[92:95]\n\t"
"buffer_load_dwordx4 v240, s[56:59], s76 offen sc1 lds\n\t"
"v_mfma_f32_16x16x32_bf16 v[92:95], v[200:203], v[132:135], v[92:95]\n\t"
"ds_read_b128 v[196:199], v246 offset:20480\n\t"
"s_waitcnt lgkmcnt(4)\n\t"
"v_mfma_f32_16x16x32_bf16 v[224:227], v[208:211], v[128:131], v[224:227]\n\t"
"ds_read_b128 v[200:203], v246 offset:1024\n\t"
"s_add_u32 m0, s12, 123904\n\t"
"v_mfma_f32_16x16x32_bf16 v[224:227], v[212:215], v[136:139], v[224:227]\n\t"
"s_add_u32 s76, s20, 1803264\n\t"
"v_mfma_f32_16x16x32_bf16 v[228:231], v[212:215], v[140:143], v[228:231]\n\t"
"buffer_load_dwordx4 v240, s[56:59], s76 offen sc1 lds\n\t"
"v_mfma_f32_16x16x32_bf16 v[228:231], v[208:211], v[132:135], v[228:231]\n\t"
"ds_read_b128 v[204:207], v246 offset:21504\n\t"
"s_waitcnt lgkmcnt(4)\n\t"
"v_mfma_f32_16x16x32_bf16 v[232:235], v[216:219], v[128:131], v[232:235]\n\t"
"ds_read_b128 v[208:211], v246 offset:2048\n\t"
"s_add_u32 m0, s12, 124928\n\t"
"v_mfma_f32_16x16x32_bf16 v[232:235], v[220:223], v[136:139], v[232:235]\n\t"
"s_add_u32 s76, s20, 1804288\n\t"
"v_mfma_f32_16x16x32_bf16 v[236:239], v[220:223], v[140:143], v[236:239]\n\t"
"buffer_load_dwordx4 v240, s[56:59], s76 offen sc1 lds\n\t"
"v_mfma_f32_16x16x32_bf16 v[236:239], v[216:219], v[132:135], v[236:239]\n\t"
"ds_read_b128 v[212:215], v246 offset:22528\n\t"
"s_waitcnt vmcnt(12) lgkmcnt(4)\n\t"
"v_mfma_f32_16x16x32_bf16 v[144:147], v[192:195], v[96:99], v[144:147]\n\t"
"ds_read_b128 v[216:219], v246 offset:3072\n\t"
"s_add_u32 m0, s12, 125952\n\t"
"s_add_u32 s76, s20, 1805312\n\t"
"v_mfma_f32_16x16x32_bf16 v[144:147], v[196:199], v[104:107], v[144:147]\n\t"
"buffer_load_dwordx4 v240, s[56:59], s76 offen sc1 lds\n\t"
"v_add_u32 v248, s8, v240\n\t"
"v_mfma_f32_16x16x32_bf16 v[148:151], v[196:199], v[108:111], v[148:151]\n\t"
"ds_read_b128 v[220:223], v246 offset:23552\n\t"
"s_add_u32 s87, s34, 45056\n\t"
"v_mfma_f32_16x16x32_bf16 v[148:151], v[192:195], v[100:103], v[148:151]\n\t"
"buffer_load_dwordx4 v[128:131], v240, s[56:59], s87 offen offset:0 sc1\n\t"
"v_add_u32 v249, s9, v240\n\t"
"s_waitcnt lgkmcnt(4)\n\t"
"v_mfma_f32_16x16x32_bf16 v[152:155], v[200:203], v[96:99], v[152:155]\n\t"
"ds_read_b128 v[192:195], v246 offset:4096\n\t"
"s_add_u32 m0, s12, 126976\n\t"
"s_add_u32 s76, s20, 1806336\n\t"
"v_mfma_f32_16x16x32_bf16 v[152:155], v[204:207], v[104:107], v[152:155]\n\t"
"buffer_load_dwordx4 v240, s[56:59], s76 offen sc1 lds\n\t"
"v_bfe_u32 v252, v240, 4, 4\n\t"
"v_mfma_f32_16x16x32_bf16 v[156:159], v[204:207], v[108:111], v[156:159]\n\t"
"ds_read_b128 v[196:199], v246 offset:24576\n\t"
"s_add_u32 s87, s35, 45056\n\t"
"v_mfma_f32_16x16x32_bf16 v[156:159], v[200:203], v[100:103], v[156:159]\n\t"
"buffer_load_dwordx4 v[132:135], v240, s[56:59], s87 offen offset:0 sc1\n\t"
"v_mul_u32_u24 v252, 1280, v252\n\t"
"s_waitcnt lgkmcnt(4)\n\t"
"v_mfma_f32_16x16x32_bf16 v[160:163], v[208:211], v[96:99], v[160:163]\n\t"
"ds_read_b128 v[200:203], v246 offset:5120\n\t"
"v_mfma_f32_16x16x32_bf16 v[160:163], v[212:215], v[104:107], v[160:163]\n\t"
"v_lshrrev_b32 v250, 4, v240\n\t"
"v_mfma_f32_16x16x32_bf16 v[164:167], v[212:215], v[108:111], v[164:167]\n\t"
"ds_read_b128 v[204:207], v246 offset:25600\n\t"
"v_mfma_f32_16x16x32_bf16 v[164:167], v[208:211], v[100:103], v[164:167]\n\t"
"v_and_b32 v250, 48, v250\n\t"
"s_waitcnt lgkmcnt(4)\n\t"
"v_mfma_f32_16x16x32_bf16 v[168:171], v[216:219], v[96:99], v[168:171]\n\t"
"ds_read_b128 v[208:211], v246 offset:6144\n\t"
"v_mfma_f32_16x16x32_bf16 v[168:171], v[220:223], v[104:107], v[168:171]\n\t"
"v_add_u32 v252, v252, v250\n\t"
"v_mfma_f32_16x16x32_bf16 v[172:175], v[220:223], v[108:111], v[172:175]\n\t"
"ds_read_b128 v[212:215], v246 offset:26624\n\t"
"v_mfma_f32_16x16x32_bf16 v[172:175], v[216:219], v[100:103], v[172:175]\n\t"
"v_add_u32 v250, s29, v252\n\t"
"s_waitcnt lgkmcnt(4)\n\t"
"v_mfma_f32_16x16x32_bf16 v[176:179], v[192:195], v[96:99], v[176:179]\n\t"
"ds_read_b128 v[216:219], v246 offset:7168\n\t"
"v_add_u32 v243, s31, v240\n\t"
"v_mfma_f32_16x16x32_bf16 v[176:179], v[196:199], v[104:107], v[176:179]\n\t"
"ds_read_b128 v[220:223], v246 offset:27648\n\t"
"v_mfma_f32_16x16x32_bf16 v[180:183], v[196:199], v[108:111], v[180:183]\n\t"
"s_add_u32 s87, s34, 47104\n\t"
"buffer_load_dwordx4 v[136:139], v240, s[56:59], s87 offen offset:0 sc1\n\t"
"v_mfma_f32_16x16x32_bf16 v[180:183], v[192:195], v[100:103], v[180:183]\n\t"
"v_add_u32 v251, s30, v252\n\t"
"s_waitcnt lgkmcnt(4)\n\t"
"v_mfma_f32_16x16x32_bf16 v[184:187], v[200:203], v[96:99], v[184:187]\n\t"
"ds_read_b128 v[192:195], v246 offset:8192\n\t"
"v_add_u32 v244, s32, v240\n\t"
"v_mfma_f32_16x16x32_bf16 v[184:187], v[204:207], v[104:107], v[184:187]\n\t"
"ds_read_b128 v[196:199], v246 offset:28672\n\t"
"v_mfma_f32_16x16x32_bf16 v[188:191], v[204:207], v[108:111], v[188:191]\n\t"
"s_add_u32 s87, s35, 47104\n\t"
"v_mfma_f32_16x16x32_bf16 v[188:191], v[200:203], v[100:103], v[188:191]\n\t"
"buffer_load_dwordx4 v[140:143], v240, s[56:59], s87 offen offset:0 sc1\n\t"
"s_waitcnt lgkmcnt(4)\n\t"
"v_mfma_f32_16x16x32_bf16 v[80:83], v[208:211], v[96:99], v[80:83]\n\t"
"ds_read_b128 v[200:203], v246 offset:9216\n\t"
"v_mfma_f32_16x16x32_bf16 v[80:83], v[212:215], v[104:107], v[80:83]\n\t"
"v_mfma_f32_16x16x32_bf16 v[84:87], v[212:215], v[108:111], v[84:87]\n\t"
"ds_read_b128 v[204:207], v246 offset:29696\n\t"
"v_mfma_f32_16x16x32_bf16 v[84:87], v[208:211], v[100:103], v[84:87]\n\t"
"s_waitcnt vmcnt(12)\n\t"
"s_barrier\n\t"
"v_add_u32 v246, 81920, v253\n\t"
"s_waitcnt lgkmcnt(4)\n\t"
"v_mfma_f32_16x16x32_bf16 v[88:91], v[216:219], v[96:99], v[88:91]\n\t"
"ds_read_b128 v[208:211], v246 offset:0\n\t"
"v_mfma_f32_16x16x32_bf16 v[88:91], v[220:223], v[104:107], v[88:91]\n\t"
"v_mfma_f32_16x16x32_bf16 v[92:95], v[220:223], v[108:111], v[92:95]\n\t"
"ds_read_b128 v[212:215], v246 offset:20480\n\t"
"v_mfma_f32_16x16x32_bf16 v[92:95], v[216:219], v[100:103], v[92:95]\n\t"
"s_waitcnt lgkmcnt(4)\n\t"
"v_mfma_f32_16x16x32_bf16 v[224:227], v[192:195], v[96:99], v[224:227]\n\t"
"ds_read_b128 v[216:219], v246 offset:1024\n\t"
"v_mfma_f32_16x16x32_bf16 v[224:227], v[196:199], v[104:107], v[224:227]\n\t"
"v_mfma_f32_16x16x32_bf16 v[228:231], v[196:199], v[108:111], v[228:231]\n\t"
"ds_read_b128 v[220:223], v246 offset:21504\n\t"
"v_mfma_f32_16x16x32_bf16 v[228:231], v[192:195], v[100:103], v[228:231]\n\t"
"s_waitcnt lgkmcnt(4)\n\t"
"v_mfma_f32_16x16x32_bf16 v[232:235], v[200:203], v[96:99], v[232:235]\n\t"
"ds_read_b128 v[192:195], v246 offset:2048\n\t"
"v_mfma_f32_16x16x32_bf16 v[232:235], v[204:207], v[104:107], v[232:235]\n\t"
"v_mfma_f32_16x16x32_bf16 v[236:239], v[204:207], v[108:111], v[236:239]\n\t"
"ds_read_b128 v[196:199], v246 offset:22528\n\t"
"v_mfma_f32_16x16x32_bf16 v[236:239], v[200:203], v[100:103], v[236:239]\n\t"
"s_waitcnt vmcnt(9) lgkmcnt(4)\n\t"
"v_mfma_f32_16x16x32_bf16 v[144:147], v[208:211], v[112:115], v[144:147]\n\t"
"ds_read_b128 v[200:203], v246 offset:3072\n\t"
"v_mfma_f32_16x16x32_bf16 v[144:147], v[212:215], v[120:123], v[144:147]\n\t"
"v_mfma_f32_16x16x32_bf16 v[148:151], v[212:215], v[124:127], v[148:151]\n\t"
"ds_read_b128 v[204:207], v246 offset:23552\n\t"
"v_mfma_f32_16x16x32_bf16 v[148:151], v[208:211], v[116:119], v[148:151]\n\t"
"s_waitcnt lgkmcnt(4)\n\t"
"v_mfma_f32_16x16x32_bf16 v[152:155], v[216:219], v[112:115], v[152:155]\n\t"
"ds_read_b128 v[208:211], v246 offset:4096\n\t"
"v_mfma_f32_16x16x32_bf16 v[152:155], v[220:223], v[120:123], v[152:155]\n\t"
"buffer_load_dwordx4 v[96:99], v250, s[48:51], 0 offen offset:0\n\t"
"v_mfma_f32_16x16x32_bf16 v[156:159], v[220:223], v[124:127], v[156:159]\n\t"
"ds_read_b128 v[212:215], v246 offset:24576\n\t"
"v_mfma_f32_16x16x32_bf16 v[156:159], v[216:219], v[116:119], v[156:159]\n\t"
"s_waitcnt lgkmcnt(4)\n\t"
"v_mfma_f32_16x16x32_bf16 v[160:163], v[192:195], v[112:115], v[160:163]\n\t"
"ds_read_b128 v[216:219], v246 offset:5120\n\t"
"v_mfma_f32_16x16x32_bf16 v[160:163], v[196:199], v[120:123], v[160:163]\n\t"
"buffer_load_dwordx4 v[100:103], v251, s[48:51], 0 offen offset:0\n\t"
"v_mfma_f32_16x16x32_bf16 v[164:167], v[196:199], v[124:127], v[164:167]\n\t"
"ds_read_b128 v[220:223], v246 offset:25600\n\t"
"v_mfma_f32_16x16x32_bf16 v[164:167], v[192:195], v[116:119], v[164:167]\n\t"
"s_waitcnt lgkmcnt(4)\n\t"
"v_mfma_f32_16x16x32_bf16 v[168:171], v[200:203], v[112:115], v[168:171]\n\t"
"ds_read_b128 v[192:195], v246 offset:6144\n\t"
"v_mfma_f32_16x16x32_bf16 v[168:171], v[204:207], v[120:123], v[168:171]\n\t"
"v_mfma_f32_16x16x32_bf16 v[172:175], v[204:207], v[124:127], v[172:175]\n\t"
"ds_read_b128 v[196:199], v246 offset:26624\n\t"
"v_mfma_f32_16x16x32_bf16 v[172:175], v[200:203], v[116:119], v[172:175]\n\t"
"s_waitcnt lgkmcnt(4)\n\t"
"v_mfma_f32_16x16x32_bf16 v[176:179], v[208:211], v[112:115], v[176:179]\n\t"
"ds_read_b128 v[200:203], v246 offset:7168\n\t"
"v_mfma_f32_16x16x32_bf16 v[176:179], v[212:215], v[120:123], v[176:179]\n\t"
"v_mfma_f32_16x16x32_bf16 v[180:183], v[212:215], v[124:127], v[180:183]\n\t"
"ds_read_b128 v[204:207], v246 offset:27648\n\t"
"v_mfma_f32_16x16x32_bf16 v[180:183], v[208:211], v[116:119], v[180:183]\n\t"
"s_waitcnt lgkmcnt(4)\n\t"
"v_mfma_f32_16x16x32_bf16 v[184:187], v[216:219], v[112:115], v[184:187]\n\t"
"ds_read_b128 v[208:211], v246 offset:8192\n\t"
"v_mfma_f32_16x16x32_bf16 v[184:187], v[220:223], v[120:123], v[184:187]\n\t"
"v_mfma_f32_16x16x32_bf16 v[188:191], v[220:223], v[124:127], v[188:191]\n\t"
"ds_read_b128 v[212:215], v246 offset:28672\n\t"
"v_mfma_f32_16x16x32_bf16 v[188:191], v[216:219], v[116:119], v[188:191]\n\t"
"s_waitcnt lgkmcnt(4)\n\t"
"v_mfma_f32_16x16x32_bf16 v[80:83], v[192:195], v[112:115], v[80:83]\n\t"
"ds_read_b128 v[216:219], v246 offset:9216\n\t"
"v_mfma_f32_16x16x32_bf16 v[80:83], v[196:199], v[120:123], v[80:83]\n\t"
"v_mfma_f32_16x16x32_bf16 v[84:87], v[196:199], v[124:127], v[84:87]\n\t"
"ds_read_b128 v[220:223], v246 offset:29696\n\t"
"v_mfma_f32_16x16x32_bf16 v[84:87], v[192:195], v[116:119], v[84:87]\n\t"
"s_waitcnt vmcnt(5)\n\t"
"s_barrier\n\t"
"v_add_u32 v246, 122880, v253\n\t"
"s_waitcnt lgkmcnt(4)\n\t"
"v_mfma_f32_16x16x32_bf16 v[88:91], v[200:203], v[112:115], v[88:91]\n\t"
"ds_read_b128 v[192:195], v246 offset:0\n\t"
"v_mfma_f32_16x16x32_bf16 v[88:91], v[204:207], v[120:123], v[88:91]\n\t"
"ds_read_b128 v[196:199], v246 offset:20480\n\t"
"v_mfma_f32_16x16x32_bf16 v[92:95], v[204:207], v[124:127], v[92:95]\n\t"
"ds_write_b128 v248, v[4:7] offset:0\n\t"
"v_mfma_f32_16x16x32_bf16 v[92:95], v[200:203], v[116:119], v[92:95]\n\t"
"s_waitcnt lgkmcnt(5)\n\t"
"v_mfma_f32_16x16x32_bf16 v[224:227], v[208:211], v[112:115], v[224:227]\n\t"
"ds_read_b128 v[200:203], v246 offset:1024\n\t"
"v_mfma_f32_16x16x32_bf16 v[224:227], v[212:215], v[120:123], v[224:227]\n\t"
"ds_write_b128 v248, v[12:15] offset:1024\n\t"
"v_mfma_f32_16x16x32_bf16 v[228:231], v[212:215], v[124:127], v[228:231]\n\t"
"ds_read_b128 v[204:207], v246 offset:21504\n\t"
"v_mfma_f32_16x16x32_bf16 v[228:231], v[208:211], v[116:119], v[228:231]\n\t"
"ds_write_b128 v248, v[20:23] offset:2048\n\t"
"s_waitcnt lgkmcnt(7)\n\t"
"v_mfma_f32_16x16x32_bf16 v[232:235], v[216:219], v[112:115], v[232:235]\n\t"
"ds_read_b128 v[208:211], v246 offset:2048\n\t"
"v_mfma_f32_16x16x32_bf16 v[232:235], v[220:223], v[120:123], v[232:235]\n\t"
"ds_write_b128 v248, v[28:31] offset:3072\n\t"
"v_mfma_f32_16x16x32_bf16 v[236:239], v[220:223], v[124:127], v[236:239]\n\t"
"ds_read_b128 v[212:215], v246 offset:22528\n\t"
"v_mfma_f32_16x16x32_bf16 v[236:239], v[216:219], v[116:119], v[236:239]\n\t"
"ds_write_b128 v248, v[36:39] offset:4096\n\t"
"s_waitcnt vmcnt(2) lgkmcnt(9)\n\t"
"v_mfma_f32_16x16x32_bf16 v[144:147], v[192:195], v[128:131], v[144:147]\n\t"
"ds_read_b128 v[216:219], v246 offset:3072\n\t"
"v_mfma_f32_16x16x32_bf16 v[144:147], v[196:199], v[136:139], v[144:147]\n\t"
"ds_write_b128 v248, v[44:47] offset:5120\n\t"
"v_mfma_f32_16x16x32_bf16 v[148:151], v[196:199], v[140:143], v[148:151]\n\t"
"ds_read_b128 v[220:223], v246 offset:23552\n\t"
"v_mfma_f32_16x16x32_bf16 v[148:151], v[192:195], v[132:135], v[148:151]\n\t"
"ds_write_b128 v248, v[52:55] offset:6144\n\t"
"s_waitcnt lgkmcnt(9)\n\t"
"v_mfma_f32_16x16x32_bf16 v[152:155], v[200:203], v[128:131], v[152:155]\n\t"
"ds_read_b128 v[192:195], v246 offset:4096\n\t"
"v_mfma_f32_16x16x32_bf16 v[152:155], v[204:207], v[136:139], v[152:155]\n\t"
"ds_write_b128 v248, v[60:63] offset:7168\n\t"
"v_mfma_f32_16x16x32_bf16 v[156:159], v[204:207], v[140:143], v[156:159]\n\t"
"ds_read_b128 v[196:199], v246 offset:24576\n\t"
"v_mfma_f32_16x16x32_bf16 v[156:159], v[200:203], v[132:135], v[156:159]\n\t"
"ds_write_b128 v248, v[68:71] offset:8192\n\t"
"s_waitcnt lgkmcnt(9)\n\t"
"v_mfma_f32_16x16x32_bf16 v[160:163], v[208:211], v[128:131], v[160:163]\n\t"
"ds_read_b128 v[200:203], v246 offset:5120\n\t"
"v_mfma_f32_16x16x32_bf16 v[160:163], v[212:215], v[136:139], v[160:163]\n\t"
"ds_write_b128 v248, v[76:79] offset:9216\n\t"
"v_mfma_f32_16x16x32_bf16 v[164:167], v[212:215], v[140:143], v[164:167]\n\t"
"ds_read_b128 v[204:207], v246 offset:25600\n\t"
"v_mfma_f32_16x16x32_bf16 v[164:167], v[208:211], v[132:135], v[164:167]\n\t"
"s_waitcnt lgkmcnt(8)\n\t"
"v_mfma_f32_16x16x32_bf16 v[168:171], v[216:219], v[128:131], v[168:171]\n\t"
"ds_read_b128 v[208:211], v246 offset:6144\n\t"
"v_mfma_f32_16x16x32_bf16 v[168:171], v[220:223], v[136:139], v[168:171]\n\t"
"v_mfma_f32_16x16x32_bf16 v[172:175], v[220:223], v[140:143], v[172:175]\n\t"
"ds_read_b128 v[212:215], v246 offset:26624\n\t"
"v_mfma_f32_16x16x32_bf16 v[172:175], v[216:219], v[132:135], v[172:175]\n\t"
"s_waitcnt lgkmcnt(0)\n\t"
"s_barrier\n\t"
"v_mfma_f32_16x16x32_bf16 v[176:179], v[192:195], v[128:131], v[176:179]\n\t"
"ds_read_b128 v[216:219], v246 offset:7168\n\t"
"ds_read_b128 v[4:7], v249 offset:0\n\t"
"v_mfma_f32_16x16x32_bf16 v[176:179], v[196:199], v[136:139], v[176:179]\n\t"
"ds_read_b128 v[12:15], v249 offset:1024\n\t"
"v_mfma_f32_16x16x32_bf16 v[180:183], v[196:199], v[140:143], v[180:183]\n\t"
"ds_read_b128 v[220:223], v246 offset:27648\n\t"
"ds_read_b128 v[20:23], v249 offset:2048\n\t"
"v_mfma_f32_16x16x32_bf16 v[180:183], v[192:195], v[132:135], v[180:183]\n\t"
"ds_read_b128 v[28:31], v249 offset:3072\n\t"
"v_mfma_f32_16x16x32_bf16 v[184:187], v[200:203], v[128:131], v[184:187]\n\t"
"ds_read_b128 v[192:195], v246 offset:8192\n\t"
"ds_read_b128 v[36:39], v249 offset:4096\n\t"
"v_mfma_f32_16x16x32_bf16 v[184:187], v[204:207], v[136:139], v[184:187]\n\t"
"ds_read_b128 v[44:47], v249 offset:5120\n\t"
"v_mfma_f32_16x16x32_bf16 v[188:191], v[204:207], v[140:143], v[188:191]\n\t"
"ds_read_b128 v[196:199], v246 offset:28672\n\t"
"ds_read_b128 v[52:55], v249 offset:6144\n\t"
"v_mfma_f32_16x16x32_bf16 v[188:191], v[200:203], v[132:135], v[188:191]\n\t"
"ds_read_b128 v[60:63], v249 offset:7168\n\t"
"v_mfma_f32_16x16x32_bf16 v[80:83], v[208:211], v[128:131], v[80:83]\n\t"
"ds_read_b128 v[200:203], v246 offset:9216\n\t"
"v_mfma_f32_16x16x32_bf16 v[80:83], v[212:215], v[136:139], v[80:83]\n\t"
"ds_read_b128 v[68:71], v249 offset:8192\n\t"
"v_mfma_f32_16x16x32_bf16 v[84:87], v[212:215], v[140:143], v[84:87]\n\t"
"ds_read_b128 v[76:79], v249 offset:9216\n\t"
"v_mfma_f32_16x16x32_bf16 v[84:87], v[208:211], v[132:135], v[84:87]\n\t"
"ds_read_b128 v[204:207], v246 offset:29696\n\t"
"s_waitcnt lgkmcnt(12)\n\t"
"v_mfma_f32_16x16x32_bf16 v[88:91], v[216:219], v[128:131], v[88:91]\n\t"
"v_add_f32 v0, v0, v4\n\t"
"v_add_f32 v1, v1, v5\n\t"
"v_add_f32 v2, v2, v6\n\t"
"v_add_f32 v3, v3, v7\n\t"
"v_add_f32 v8, v8, v12\n\t"
"v_add_f32 v9, v9, v13\n\t"
"v_add_f32 v10, v10, v14\n\t"
"v_mfma_f32_16x16x32_bf16 v[88:91], v[220:223], v[136:139], v[88:91]\n\t"
"v_add_f32 v11, v11, v15\n\t"
"v_cvt_pk_bf16_f32 v4, v0, v1\n\t"
"v_cvt_pk_bf16_f32 v5, v2, v3\n\t"
"v_cvt_pk_bf16_f32 v6, v8, v9\n\t"
"v_cvt_pk_bf16_f32 v7, v10, v11\n\t"
"s_add_u32 s87, s16, 163840\n\t"
"buffer_store_dwordx4 v[4:7], v240, s[56:59], s87 offen offset:0 sc1\n\t"
"v_mfma_f32_16x16x32_bf16 v[92:95], v[220:223], v[140:143], v[92:95]\n\t"
"s_waitcnt lgkmcnt(10)\n\t"
"v_add_f32 v16, v16, v20\n\t"
"v_add_f32 v17, v17, v21\n\t"
"v_add_f32 v18, v18, v22\n\t"
"v_add_f32 v19, v19, v23\n\t"
"v_add_f32 v24, v24, v28\n\t"
"v_add_f32 v25, v25, v29\n\t"
"v_add_f32 v26, v26, v30\n\t"
"v_mfma_f32_16x16x32_bf16 v[92:95], v[216:219], v[132:135], v[92:95]\n\t"
"v_add_f32 v27, v27, v31\n\t"
"v_cvt_pk_bf16_f32 v20, v16, v17\n\t"
"v_cvt_pk_bf16_f32 v21, v18, v19\n\t"
"v_cvt_pk_bf16_f32 v22, v24, v25\n\t"
"v_cvt_pk_bf16_f32 v23, v26, v27\n\t"
"s_add_u32 s87, s16, 164864\n\t"
"buffer_store_dwordx4 v[20:23], v240, s[56:59], s87 offen offset:0 sc1\n\t"
"s_waitcnt lgkmcnt(6)\n\t"
"v_mfma_f32_16x16x32_bf16 v[224:227], v[192:195], v[128:131], v[224:227]\n\t"
"v_add_f32 v32, v32, v36\n\t"
"v_add_f32 v33, v33, v37\n\t"
"v_add_f32 v34, v34, v38\n\t"
"v_add_f32 v35, v35, v39\n\t"
"v_add_f32 v40, v40, v44\n\t"
"v_add_f32 v41, v41, v45\n\t"
"v_add_f32 v42, v42, v46\n\t"
"v_mfma_f32_16x16x32_bf16 v[224:227], v[196:199], v[136:139], v[224:227]\n\t"
"v_add_f32 v43, v43, v47\n\t"
"v_cvt_pk_bf16_f32 v36, v32, v33\n\t"
"v_cvt_pk_bf16_f32 v37, v34, v35\n\t"
"v_cvt_pk_bf16_f32 v38, v40, v41\n\t"
"v_cvt_pk_bf16_f32 v39, v42, v43\n\t"
"s_add_u32 s87, s16, 165888\n\t"
"buffer_store_dwordx4 v[36:39], v240, s[56:59], s87 offen offset:0 sc1\n\t"
"v_mfma_f32_16x16x32_bf16 v[228:231], v[196:199], v[140:143], v[228:231]\n\t"
"s_waitcnt lgkmcnt(4)\n\t"
"v_add_f32 v48, v48, v52\n\t"
"v_add_f32 v49, v49, v53\n\t"
"v_add_f32 v50, v50, v54\n\t"
"v_add_f32 v51, v51, v55\n\t"
"v_add_f32 v56, v56, v60\n\t"
"v_add_f32 v57, v57, v61\n\t"
"v_add_f32 v58, v58, v62\n\t"
"v_mfma_f32_16x16x32_bf16 v[228:231], v[192:195], v[132:135], v[228:231]\n\t"
"v_add_f32 v59, v59, v63\n\t"
"v_cvt_pk_bf16_f32 v52, v48, v49\n\t"
"v_cvt_pk_bf16_f32 v53, v50, v51\n\t"
"v_cvt_pk_bf16_f32 v54, v56, v57\n\t"
"v_cvt_pk_bf16_f32 v55, v58, v59\n\t"
"s_add_u32 s87, s16, 166912\n\t"
"buffer_store_dwordx4 v[52:55], v240, s[56:59], s87 offen offset:0 sc1\n\t"
"s_waitcnt lgkmcnt(0)\n\t"
"v_mfma_f32_16x16x32_bf16 v[232:235], v[200:203], v[128:131], v[232:235]\n\t"
"v_add_f32 v64, v64, v68\n\t"
"v_add_f32 v65, v65, v69\n\t"
"v_add_f32 v66, v66, v70\n\t"
"v_mfma_f32_16x16x32_bf16 v[232:235], v[204:207], v[136:139], v[232:235]\n\t"
"v_add_f32 v67, v67, v71\n\t"
"v_add_f32 v72, v72, v76\n\t"
"v_add_f32 v73, v73, v77\n\t"
"v_add_f32 v74, v74, v78\n\t"
"v_mfma_f32_16x16x32_bf16 v[236:239], v[204:207], v[140:143], v[236:239]\n\t"
"v_add_f32 v75, v75, v79\n\t"
"v_cvt_pk_bf16_f32 v68, v64, v65\n\t"
"v_cvt_pk_bf16_f32 v69, v66, v67\n\t"
"v_cvt_pk_bf16_f32 v70, v72, v73\n\t"
"v_mfma_f32_16x16x32_bf16 v[236:239], v[200:203], v[132:135], v[236:239]\n\t"
"v_cvt_pk_bf16_f32 v71, v74, v75\n\t"
"s_add_u32 s87, s16, 167936\n\t"
"buffer_store_dwordx4 v[68:71], v240, s[56:59], s87 offen offset:0 sc1\n\t"
"ds_write_b128 v249, v[148:151] offset:0\n\t"
"ds_write_b128 v249, v[156:159] offset:1024\n\t"
"ds_write_b128 v249, v[164:167] offset:2048\n\t"
"ds_write_b128 v249, v[172:175] offset:3072\n\t"
"ds_write_b128 v249, v[180:183] offset:4096\n\t"
"ds_write_b128 v249, v[188:191] offset:5120\n\t"
"ds_write_b128 v249, v[84:87] offset:6144\n\t"
"ds_write_b128 v249, v[92:95] offset:7168\n\t"
"ds_write_b128 v249, v[228:231] offset:8192\n\t"
"s_nop 3\n\t"
"ds_write_b128 v249, v[236:239] offset:9216\n\t"
"s_waitcnt lgkmcnt(0)\n\t"
"s_barrier\n\t"
"ds_read_b128 v[148:151], v248 offset:0\n\t"
"ds_read_b128 v[156:159], v248 offset:1024\n\t"
"ds_read_b128 v[164:167], v248 offset:2048\n\t"
"ds_read_b128 v[172:175], v248 offset:3072\n\t"
"ds_read_b128 v[180:183], v248 offset:4096\n\t"
"ds_read_b128 v[188:191], v248 offset:5120\n\t"
"ds_read_b128 v[84:87], v248 offset:6144\n\t"
"ds_read_b128 v[92:95], v248 offset:7168\n\t"
"ds_read_b128 v[228:231], v248 offset:8192\n\t"
"ds_read_b128 v[236:239], v248 offset:9216\n\t"
"s_cmp_eq_u32 s17, 0\n\t"
"s_cbranch_scc1 LOWN_%=\n\t"
"s_waitcnt lgkmcnt(8)\n\t"
"v_add_f32 v144, v144, v148\n\t"
"v_add_f32 v145, v145, v149\n\t"
"v_add_f32 v146, v146, v150\n\t"
"v_add_f32 v147, v147, v151\n\t"
"v_add_f32 v152, v152, v156\n\t"
"v_add_f32 v153, v153, v157\n\t"
"v_add_f32 v154, v154, v158\n\t"
"v_add_f32 v155, v155, v159\n\t"
"v_cvt_pk_bf16_f32 v148, v144, v145\n\t"
"v_cvt_pk_bf16_f32 v149, v146, v147\n\t"
"v_cvt_pk_bf16_f32 v150, v152, v153\n\t"
"v_cvt_pk_bf16_f32 v151, v154, v155\n\t"
"s_add_u32 s87, s16, 0\n\t"
"buffer_store_dwordx4 v[148:151], v240, s[56:59], s87 offen offset:0 sc1\n\t"
"s_waitcnt lgkmcnt(6)\n\t"
"v_add_f32 v160, v160, v164\n\t"
"v_add_f32 v161, v161, v165\n\t"
"v_add_f32 v162, v162, v166\n\t"
"v_add_f32 v163, v163, v167\n\t"
"v_add_f32 v168, v168, v172\n\t"
"v_add_f32 v169, v169, v173\n\t"
"v_add_f32 v170, v170, v174\n\t"
"v_add_f32 v171, v171, v175\n\t"
"v_cvt_pk_bf16_f32 v164, v160, v161\n\t"
"v_cvt_pk_bf16_f32 v165, v162, v163\n\t"
"v_cvt_pk_bf16_f32 v166, v168, v169\n\t"
"v_cvt_pk_bf16_f32 v167, v170, v171\n\t"
"s_add_u32 s87, s16, 1024\n\t"
"buffer_store_dwordx4 v[164:167], v240, s[56:59], s87 offen offset:0 sc1\n\t"
"s_waitcnt lgkmcnt(4)\n\t"
"v_add_f32 v176, v176, v180\n\t"
"v_add_f32 v177, v177, v181\n\t"
"v_add_f32 v178, v178, v182\n\t"
"v_add_f32 v179, v179, v183\n\t"
"v_add_f32 v184, v184, v188\n\t"
"v_add_f32 v185, v185, v189\n\t"
"v_add_f32 v186, v186, v190\n\t"
"v_add_f32 v187, v187, v191\n\t"
"v_cvt_pk_bf16_f32 v180, v176, v177\n\t"
"v_cvt_pk_bf16_f32 v181, v178, v179\n\t"
"v_cvt_pk_bf16_f32 v182, v184, v185\n\t"
"v_cvt_pk_bf16_f32 v183, v186, v187\n\t"
"s_add_u32 s87, s16, 2048\n\t"
"buffer_store_dwordx4 v[180:183], v240, s[56:59], s87 offen offset:0 sc1\n\t"
"s_waitcnt lgkmcnt(2)\n\t"
"v_add_f32 v80, v80, v84\n\t"
"v_add_f32 v81, v81, v85\n\t"
"v_add_f32 v82, v82, v86\n\t"
"v_add_f32 v83, v83, v87\n\t"
"v_add_f32 v88, v88, v92\n\t"
"v_add_f32 v89, v89, v93\n\t"
"v_add_f32 v90, v90, v94\n\t"
"v_add_f32 v91, v91, v95\n\t"
"v_cvt_pk_bf16_f32 v84, v80, v81\n\t"
"v_cvt_pk_bf16_f32 v85, v82, v83\n\t"
"v_cvt_pk_bf16_f32 v86, v88, v89\n\t"
"v_cvt_pk_bf16_f32 v87, v90, v91\n\t"
"s_add_u32 s87, s16, 3072\n\t"
"buffer_store_dwordx4 v[84:87], v240, s[56:59], s87 offen offset:0 sc1\n\t"
"s_waitcnt lgkmcnt(0)\n\t"
"v_add_f32 v224, v224, v228\n\t"
"v_add_f32 v225, v225, v229\n\t"
"v_add_f32 v226, v226, v230\n\t"
"v_add_f32 v227, v227, v231\n\t"
"v_add_f32 v232, v232, v236\n\t"
"v_add_f32 v233, v233, v237\n\t"
"v_add_f32 v234, v234, v238\n\t"
"v_add_f32 v235, v235, v239\n\t"
"v_cvt_pk_bf16_f32 v228, v224, v225\n\t"
"v_cvt_pk_bf16_f32 v229, v226, v227\n\t"
"v_cvt_pk_bf16_f32 v230, v232, v233\n\t"
"v_cvt_pk_bf16_f32 v231, v234, v235\n\t"
"s_add_u32 s87, s16, 4096\n\t"
"buffer_store_dwordx4 v[228:231], v240, s[56:59], s87 offen offset:0 sc1\n\t"
"s_waitcnt vmcnt(0)\n\t"
"s_mov_b64 exec, 1\n\t"
"buffer_atomic_add v241, off, s[56:59], s18\n\t"
"s_mov_b64 exec, -1\n\t"
"s_branch LPUB_%=\n\t"
"LOWN_%=:\n\t"
"v_add_u32 v252, 81920, v253\n\t"
"ds_write_b128 v252, v[0:3] offset:0\n\t"
"ds_write_b128 v252, v[8:11] offset:1024\n\t"
"ds_write_b128 v252, v[16:19] offset:2048\n\t"
"ds_write_b128 v252, v[24:27] offset:3072\n\t"
"ds_write_b128 v252, v[32:35] offset:4096\n\t"
"ds_write_b128 v252, v[40:43] offset:5120\n\t"
"ds_write_b128 v252, v[48:51] offset:6144\n\t"
"ds_write_b128 v252, v[56:59] offset:7168\n\t"
"ds_write_b128 v252, v[64:67] offset:8192\n\t"
"ds_write_b128 v252, v[72:75] offset:9216\n\t"
"s_waitcnt lgkmcnt(15)\n\t"
"v_add_f32 v144, v144, v148\n\t"
"v_add_f32 v145, v145, v149\n\t"
"v_add_f32 v146, v146, v150\n\t"
"v_add_f32 v147, v147, v151\n\t"
"v_add_f32 v152, v152, v156\n\t"
"v_add_f32 v153, v153, v157\n\t"
"v_add_f32 v154, v154, v158\n\t"
"v_add_f32 v155, v155, v159\n\t"
"ds_write_b128 v252, v[144:147] offset:20480\n\t"
"ds_write_b128 v252, v[152:155] offset:21504\n\t"
"s_waitcnt lgkmcnt(15)\n\t"
"v_add_f32 v160, v160, v164\n\t"
"v_add_f32 v161, v161, v165\n\t"
"v_add_f32 v162, v162, v166\n\t"
"v_add_f32 v163, v163, v167\n\t"
"v_add_f32 v168, v168, v172\n\t"
"v_add_f32 v169, v169, v173\n\t"
"v_add_f32 v170, v170, v174\n\t"
"v_add_f32 v171, v171, v175\n\t"
"ds_write_b128 v252, v[160:163] offset:22528\n\t"
"ds_write_b128 v252, v[168:171] offset:23552\n\t"
"s_waitcnt lgkmcnt(15)\n\t"
"v_add_f32 v176, v176, v180\n\t"
"v_add_f32 v177, v177, v181\n\t"
"v_add_f32 v178, v178, v182\n\t"
"v_add_f32 v179, v179, v183\n\t"
"v_add_f32 v184, v184, v188\n\t"
"v_add_f32 v185, v185, v189\n\t"
"v_add_f32 v186, v186, v190\n\t"
"v_add_f32 v187, v187, v191\n\t"
"ds_write_b128 v252, v[176:179] offset:24576\n\t"
"ds_write_b128 v252, v[184:187] offset:25600\n\t"
"s_waitcnt lgkmcnt(15)\n\t"
"v_add_f32 v80, v80, v84\n\t"
"v_add_f32 v81, v81, v85\n\t"
"v_add_f32 v82, v82, v86\n\t"
"v_add_f32 v83, v83, v87\n\t"
"v_add_f32 v88, v88, v92\n\t"
"v_add_f32 v89, v89, v93\n\t"
"v_add_f32 v90, v90, v94\n\t"
"v_add_f32 v91, v91, v95\n\t"
"ds_write_b128 v252, v[80:83] offset:26624\n\t"
"ds_write_b128 v252, v[88:91] offset:27648\n\t"
"s_waitcnt lgkmcnt(15)\n\t"
"v_add_f32 v224, v224, v228\n\t"
"v_add_f32 v225, v225, v229\n\t"
"v_add_f32 v226, v226, v230\n\t"
"v_add_f32 v227, v227, v231\n\t"
"v_add_f32 v232, v232, v236\n\t"
"v_add_f32 v233, v233, v237\n\t"
"v_add_f32 v234, v234, v238\n\t"
"v_add_f32 v235, v235, v239\n\t"
"ds_write_b128 v252, v[224:227] offset:28672\n\t"
"ds_write_b128 v252, v[232:235] offset:29696\n\t"
"s_mov_b32 s83, 0x20000\n\t"
"s_branch LPCF_%=\n\t"
"LPC_%=:\n\t"
"s_sleep 2\n\t"
"LPCF_%=:\n\t"
"s_sub_u32 s83, s83, 1\n\t"
"s_cbranch_scc1 LPCX_%=\n\t"
"buffer_load_dword v242, off, s[56:59], s19 sc1\n\t"
"s_waitcnt vmcnt(0)\n\t"
"v_cmp_gt_u32 vcc, 6, v242\n\t"
"s_cbranch_vccnz LPC_%=\n\t"
"LPCX_%=:\n\t"
"LPUB_%=:\n\t"
"s_waitcnt lgkmcnt(0)\n\t"
"s_barrier\n\t"
"s_add_u32 s87, s24, s26\n\t"
"s_add_u32 s87, s87, 163840\n\t"
"buffer_load_dwordx4 v[0:3], v240, s[56:59], s87 offen offset:0 sc1\n\t"
"s_add_u32 s87, s24, s27\n\t"
"s_add_u32 s87, s87, 163840\n\t"
"buffer_load_dwordx4 v[4:7], v240, s[56:59], s87 offen offset:0 sc1\n\t"
"s_add_u32 s87, s24, s28\n\t"
"s_add_u32 s87, s87, 163840\n\t"
"buffer_load_dwordx4 v[8:11], v240, s[56:59], s87 offen offset:0 sc1\n\t"
"s_add_u32 s87, s24, s26\n\t"
"buffer_load_dwordx4 v[12:15], v240, s[56:59], s87 offen offset:0 sc1\n\t"
"s_add_u32 s87, s24, s27\n\t"
"buffer_load_dwordx4 v[16:19], v240, s[56:59], s87 offen offset:0 sc1\n\t"
"s_add_u32 s87, s24, s28\n\t"
"buffer_load_dwordx4 v[20:23], v240, s[56:59], s87 offen offset:0 sc1\n\t"
"ds_read_b128 v[48:51], v243 offset:0\n\t"
"ds_read_b128 v[52:55], v243 offset:1024\n\t"
"ds_read_b128 v[56:59], v243 offset:20480\n\t"
"ds_read_b128 v[60:63], v243 offset:21504\n\t"
"s_cmp_eq_u32 s11, 0\n\t"
"s_cbranch_scc1 LNO2_%=\n\t"
"s_add_u32 s87, s25, s26\n\t"
"s_add_u32 s87, s87, 163840\n\t"
"buffer_load_dwordx4 v[24:27], v240, s[56:59], s87 offen offset:0 sc1\n\t"
"s_add_u32 s87, s25, s27\n\t"
"s_add_u32 s87, s87, 163840\n\t"
"buffer_load_dwordx4 v[28:31], v240, s[56:59], s87 offen offset:0 sc1\n\t"
"s_add_u32 s87, s25, s28\n\t"
"s_add_u32 s87, s87, 163840\n\t"
"buffer_load_dwordx4 v[32:35], v240, s[56:59], s87 offen offset:0 sc1\n\t"
"s_add_u32 s87, s25, s26\n\t"
"buffer_load_dwordx4 v[36:39], v240, s[56:59], s87 offen offset:0 sc1\n\t"
"s_add_u32 s87, s25, s27\n\t"
"buffer_load_dwordx4 v[40:43], v240, s[56:59], s87 offen offset:0 sc1\n\t"
"s_add_u32 s87, s25, s28\n\t"
"buffer_load_dwordx4 v[44:47], v240, s[56:59], s87 offen offset:0 sc1\n\t"
"ds_read_b128 v[64:67], v244 offset:0\n\t"
"ds_read_b128 v[68:71], v244 offset:1024\n\t"
"ds_read_b128 v[72:75], v244 offset:20480\n\t"
"ds_read_b128 v[76:79], v244 offset:21504\n\t"
"LNO2_%=:\n\t"
"s_waitcnt vmcnt(3) lgkmcnt(2)\n\t"
"v_lshlrev_b32 v144, 16, v0\n\t"
"v_and_b32 v145, 0xffff0000, v0\n\t"
"v_lshlrev_b32 v146, 16, v1\n\t"
"v_and_b32 v147, 0xffff0000, v1\n\t"
"v_lshlrev_b32 v148, 16, v2\n\t"
"v_and_b32 v149, 0xffff0000, v2\n\t"
"v_lshlrev_b32 v150, 16, v3\n\t"
"v_and_b32 v151, 0xffff0000, v3\n\t"
"v_add_f32 v48, v48, v144\n\t"
"v_add_f32 v49, v49, v145\n\t"
"v_add_f32 v50, v50, v146\n\t"
"v_add_f32 v51, v51, v147\n\t"
"v_add_f32 v52, v52, v148\n\t"
"v_add_f32 v53, v53, v149\n\t"
"v_add_f32 v54, v54, v150\n\t"
"v_add_f32 v55, v55, v151\n\t"
"v_lshlrev_b32 v144, 16, v4\n\t"
"v_and_b32 v145, 0xffff0000, v4\n\t"
"v_lshlrev_b32 v146, 16, v5\n\t"
"v_and_b32 v147, 0xffff0000, v5\n\t"
"v_lshlrev_b32 v148, 16, v6\n\t"
"v_and_b32 v149, 0xffff0000, v6\n\t"
"v_lshlrev_b32 v150, 16, v7\n\t"
"v_and_b32 v151, 0xffff0000, v7\n\t"
"v_add_f32 v48, v48, v144\n\t"
"v_add_f32 v49, v49, v145\n\t"
"v_add_f32 v50, v50, v146\n\t"
"v_add_f32 v51, v51, v147\n\t"
"v_add_f32 v52, v52, v148\n\t"
"v_add_f32 v53, v53, v149\n\t"
"v_add_f32 v54, v54, v150\n\t"
"v_add_f32 v55, v55, v151\n\t"
"v_lshlrev_b32 v144, 16, v8\n\t"
"v_and_b32 v145, 0xffff0000, v8\n\t"
"v_lshlrev_b32 v146, 16, v9\n\t"
"v_and_b32 v147, 0xffff0000, v9\n\t"
"v_lshlrev_b32 v148, 16, v10\n\t"
"v_and_b32 v149, 0xffff0000, v10\n\t"
"v_lshlrev_b32 v150, 16, v11\n\t"
"v_and_b32 v151, 0xffff0000, v11\n\t"
"v_add_f32 v48, v48, v144\n\t"
"v_add_f32 v49, v49, v145\n\t"
"v_add_f32 v50, v50, v146\n\t"
"v_add_f32 v51, v51, v147\n\t"
"v_add_f32 v52, v52, v148\n\t"
"v_add_f32 v53, v53, v149\n\t"
"v_add_f32 v54, v54, v150\n\t"
"v_add_f32 v55, v55, v151\n\t"
"v_mul_f32 v144, 0xbfb8aa3b, v48\n\t"
"v_mul_f32 v145, 0xbfb8aa3b, v49\n\t"
"v_mul_f32 v146, 0xbfb8aa3b, v50\n\t"
"v_mul_f32 v147, 0xbfb8aa3b, v51\n\t"
"v_mul_f32 v148, 0xbfb8aa3b, v52\n\t"
"v_mul_f32 v149, 0xbfb8aa3b, v53\n\t"
"v_mul_f32 v150, 0xbfb8aa3b, v54\n\t"
"v_mul_f32 v151, 0xbfb8aa3b, v55\n\t"
"v_exp_f32 v144, v144\n\t"
"v_exp_f32 v145, v145\n\t"
"v_exp_f32 v146, v146\n\t"
"v_exp_f32 v147, v147\n\t"
"v_exp_f32 v148, v148\n\t"
"v_exp_f32 v149, v149\n\t"
"v_exp_f32 v150, v150\n\t"
"v_exp_f32 v151, v151\n\t"
"v_add_f32 v144, 1.0, v144\n\t"
"v_add_f32 v145, 1.0, v145\n\t"
"v_add_f32 v146, 1.0, v146\n\t"
"v_add_f32 v147, 1.0, v147\n\t"
"v_add_f32 v148, 1.0, v148\n\t"
"v_add_f32 v149, 1.0, v149\n\t"
"v_add_f32 v150, 1.0, v150\n\t"
"v_add_f32 v151, 1.0, v151\n\t"
"v_rcp_f32 v144, v144\n\t"
"v_rcp_f32 v145, v145\n\t"
"v_rcp_f32 v146, v146\n\t"
"v_rcp_f32 v147, v147\n\t"
"v_rcp_f32 v148, v148\n\t"
"v_rcp_f32 v149, v149\n\t"
"v_rcp_f32 v150, v150\n\t"
"v_rcp_f32 v151, v151\n\t"
"v_mul_f32 v48, v48, v144\n\t"
"v_mul_f32 v49, v49, v145\n\t"
"v_mul_f32 v50, v50, v146\n\t"
"v_mul_f32 v51, v51, v147\n\t"
"v_mul_f32 v52, v52, v148\n\t"
"v_mul_f32 v53, v53, v149\n\t"
"v_mul_f32 v54, v54, v150\n\t"
"v_mul_f32 v55, v55, v151\n\t"
"s_waitcnt vmcnt(0) lgkmcnt(0)\n\t"
"v_lshlrev_b32 v144, 16, v12\n\t"
"v_and_b32 v145, 0xffff0000, v12\n\t"
"v_lshlrev_b32 v146, 16, v13\n\t"
"v_and_b32 v147, 0xffff0000, v13\n\t"
"v_lshlrev_b32 v148, 16, v14\n\t"
"v_and_b32 v149, 0xffff0000, v14\n\t"
"v_lshlrev_b32 v150, 16, v15\n\t"
"v_and_b32 v151, 0xffff0000, v15\n\t"
"v_add_f32 v56, v56, v144\n\t"
"v_add_f32 v57, v57, v145\n\t"
"v_add_f32 v58, v58, v146\n\t"
"v_add_f32 v59, v59, v147\n\t"
"v_add_f32 v60, v60, v148\n\t"
"v_add_f32 v61, v61, v149\n\t"
"v_add_f32 v62, v62, v150\n\t"
"v_add_f32 v63, v63, v151\n\t"
"v_lshlrev_b32 v144, 16, v16\n\t"
"v_and_b32 v145, 0xffff0000, v16\n\t"
"v_lshlrev_b32 v146, 16, v17\n\t"
"v_and_b32 v147, 0xffff0000, v17\n\t"
"v_lshlrev_b32 v148, 16, v18\n\t"
"v_and_b32 v149, 0xffff0000, v18\n\t"
"v_lshlrev_b32 v150, 16, v19\n\t"
"v_and_b32 v151, 0xffff0000, v19\n\t"
"v_add_f32 v56, v56, v144\n\t"
"v_add_f32 v57, v57, v145\n\t"
"v_add_f32 v58, v58, v146\n\t"
"v_add_f32 v59, v59, v147\n\t"
"v_add_f32 v60, v60, v148\n\t"
"v_add_f32 v61, v61, v149\n\t"
"v_add_f32 v62, v62, v150\n\t"
"v_add_f32 v63, v63, v151\n\t"
"v_lshlrev_b32 v144, 16, v20\n\t"
"v_and_b32 v145, 0xffff0000, v20\n\t"
"v_lshlrev_b32 v146, 16, v21\n\t"
"v_and_b32 v147, 0xffff0000, v21\n\t"
"v_lshlrev_b32 v148, 16, v22\n\t"
"v_and_b32 v149, 0xffff0000, v22\n\t"
"v_lshlrev_b32 v150, 16, v23\n\t"
"v_and_b32 v151, 0xffff0000, v23\n\t"
"v_add_f32 v56, v56, v144\n\t"
"v_add_f32 v57, v57, v145\n\t"
"v_add_f32 v58, v58, v146\n\t"
"v_add_f32 v59, v59, v147\n\t"
"v_add_f32 v60, v60, v148\n\t"
"v_add_f32 v61, v61, v149\n\t"
"v_add_f32 v62, v62, v150\n\t"
"v_add_f32 v63, v63, v151\n\t"
"v_lshlrev_b32 v144, 16, v96\n\t"
"v_and_b32 v145, 0xffff0000, v96\n\t"
"v_lshlrev_b32 v146, 16, v97\n\t"
"v_and_b32 v147, 0xffff0000, v97\n\t"
"v_lshlrev_b32 v148, 16, v98\n\t"
"v_and_b32 v149, 0xffff0000, v98\n\t"
"v_lshlrev_b32 v150, 16, v99\n\t"
"v_and_b32 v151, 0xffff0000, v99\n\t"
"v_fma_f32 v144, v56, v48, v144\n\t"
"v_fma_f32 v145, v57, v49, v145\n\t"
"v_fma_f32 v146, v58, v50, v146\n\t"
"v_fma_f32 v147, v59, v51, v147\n\t"
"v_fma_f32 v148, v60, v52, v148\n\t"
"v_fma_f32 v149, v61, v53, v149\n\t"
"v_fma_f32 v150, v62, v54, v150\n\t"
"v_fma_f32 v151, v63, v55, v151\n\t"
"v_cvt_pk_bf16_f32 v160, v144, v145\n\t"
"v_cvt_pk_bf16_f32 v161, v146, v147\n\t"
"v_cvt_pk_bf16_f32 v162, v148, v149\n\t"
"v_cvt_pk_bf16_f32 v163, v150, v151\n\t"
"buffer_store_dwordx4 v[160:163], v250, s[52:55], 0 offen offset:0\n\t"
"s_cmp_eq_u32 s11, 0\n\t"
"s_cbranch_scc1 LEND_%=\n\t"
"s_waitcnt vmcnt(4) lgkmcnt(2)\n\t"
"v_lshlrev_b32 v144, 16, v24\n\t"
"v_and_b32 v145, 0xffff0000, v24\n\t"
"v_lshlrev_b32 v146, 16, v25\n\t"
"v_and_b32 v147, 0xffff0000, v25\n\t"
"v_lshlrev_b32 v148, 16, v26\n\t"
"v_and_b32 v149, 0xffff0000, v26\n\t"
"v_lshlrev_b32 v150, 16, v27\n\t"
"v_and_b32 v151, 0xffff0000, v27\n\t"
"v_add_f32 v64, v64, v144\n\t"
"v_add_f32 v65, v65, v145\n\t"
"v_add_f32 v66, v66, v146\n\t"
"v_add_f32 v67, v67, v147\n\t"
"v_add_f32 v68, v68, v148\n\t"
"v_add_f32 v69, v69, v149\n\t"
"v_add_f32 v70, v70, v150\n\t"
"v_add_f32 v71, v71, v151\n\t"
"v_lshlrev_b32 v144, 16, v28\n\t"
"v_and_b32 v145, 0xffff0000, v28\n\t"
"v_lshlrev_b32 v146, 16, v29\n\t"
"v_and_b32 v147, 0xffff0000, v29\n\t"
"v_lshlrev_b32 v148, 16, v30\n\t"
"v_and_b32 v149, 0xffff0000, v30\n\t"
"v_lshlrev_b32 v150, 16, v31\n\t"
"v_and_b32 v151, 0xffff0000, v31\n\t"
"v_add_f32 v64, v64, v144\n\t"
"v_add_f32 v65, v65, v145\n\t"
"v_add_f32 v66, v66, v146\n\t"
"v_add_f32 v67, v67, v147\n\t"
"v_add_f32 v68, v68, v148\n\t"
"v_add_f32 v69, v69, v149\n\t"
"v_add_f32 v70, v70, v150\n\t"
"v_add_f32 v71, v71, v151\n\t"
"v_lshlrev_b32 v144, 16, v32\n\t"
"v_and_b32 v145, 0xffff0000, v32\n\t"
"v_lshlrev_b32 v146, 16, v33\n\t"
"v_and_b32 v147, 0xffff0000, v33\n\t"
"v_lshlrev_b32 v148, 16, v34\n\t"
"v_and_b32 v149, 0xffff0000, v34\n\t"
"v_lshlrev_b32 v150, 16, v35\n\t"
"v_and_b32 v151, 0xffff0000, v35\n\t"
"v_add_f32 v64, v64, v144\n\t"
"v_add_f32 v65, v65, v145\n\t"
"v_add_f32 v66, v66, v146\n\t"
"v_add_f32 v67, v67, v147\n\t"
"v_add_f32 v68, v68, v148\n\t"
"v_add_f32 v69, v69, v149\n\t"
"v_add_f32 v70, v70, v150\n\t"
"v_add_f32 v71, v71, v151\n\t"
"v_mul_f32 v144, 0xbfb8aa3b, v64\n\t"
"v_mul_f32 v145, 0xbfb8aa3b, v65\n\t"
"v_mul_f32 v146, 0xbfb8aa3b, v66\n\t"
"v_mul_f32 v147, 0xbfb8aa3b, v67\n\t"
"v_mul_f32 v148, 0xbfb8aa3b, v68\n\t"
"v_mul_f32 v149, 0xbfb8aa3b, v69\n\t"
"v_mul_f32 v150, 0xbfb8aa3b, v70\n\t"
"v_mul_f32 v151, 0xbfb8aa3b, v71\n\t"
"v_exp_f32 v144, v144\n\t"
"v_exp_f32 v145, v145\n\t"
"v_exp_f32 v146, v146\n\t"
"v_exp_f32 v147, v147\n\t"
"v_exp_f32 v148, v148\n\t"
"v_exp_f32 v149, v149\n\t"
"v_exp_f32 v150, v150\n\t"
"v_exp_f32 v151, v151\n\t"
"v_add_f32 v144, 1.0, v144\n\t"
"v_add_f32 v145, 1.0, v145\n\t"
"v_add_f32 v146, 1.0, v146\n\t"
"v_add_f32 v147, 1.0, v147\n\t"
"v_add_f32 v148, 1.0, v148\n\t"
"v_add_f32 v149, 1.0, v149\n\t"
"v_add_f32 v150, 1.0, v150\n\t"
"v_add_f32 v151, 1.0, v151\n\t"
"v_rcp_f32 v144, v144\n\t"
"v_rcp_f32 v145, v145\n\t"
"v_rcp_f32 v146, v146\n\t"
"v_rcp_f32 v147, v147\n\t"
"v_rcp_f32 v148, v148\n\t"
"v_rcp_f32 v149, v149\n\t"
"v_rcp_f32 v150, v150\n\t"
"v_rcp_f32 v151, v151\n\t"
"v_mul_f32 v64, v64, v144\n\t"
"v_mul_f32 v65, v65, v145\n\t"
"v_mul_f32 v66, v66, v146\n\t"
"v_mul_f32 v67, v67, v147\n\t"
"v_mul_f32 v68, v68, v148\n\t"
"v_mul_f32 v69, v69, v149\n\t"
"v_mul_f32 v70, v70, v150\n\t"
"v_mul_f32 v71, v71, v151\n\t"
"s_waitcnt vmcnt(1) lgkmcnt(0)\n\t"
"v_lshlrev_b32 v144, 16, v36\n\t"
"v_and_b32 v145, 0xffff0000, v36\n\t"
"v_lshlrev_b32 v146, 16, v37\n\t"
"v_and_b32 v147, 0xffff0000, v37\n\t"
"v_lshlrev_b32 v148, 16, v38\n\t"
"v_and_b32 v149, 0xffff0000, v38\n\t"
"v_lshlrev_b32 v150, 16, v39\n\t"
"v_and_b32 v151, 0xffff0000, v39\n\t"
"v_add_f32 v72, v72, v144\n\t"
"v_add_f32 v73, v73, v145\n\t"
"v_add_f32 v74, v74, v146\n\t"
"v_add_f32 v75, v75, v147\n\t"
"v_add_f32 v76, v76, v148\n\t"
"v_add_f32 v77, v77, v149\n\t"
"v_add_f32 v78, v78, v150\n\t"
"v_add_f32 v79, v79, v151\n\t"
"v_lshlrev_b32 v144, 16, v40\n\t"
"v_and_b32 v145, 0xffff0000, v40\n\t"
"v_lshlrev_b32 v146, 16, v41\n\t"
"v_and_b32 v147, 0xffff0000, v41\n\t"
"v_lshlrev_b32 v148, 16, v42\n\t"
"v_and_b32 v149, 0xffff0000, v42\n\t"
"v_lshlrev_b32 v150, 16, v43\n\t"
"v_and_b32 v151, 0xffff0000, v43\n\t"
"v_add_f32 v72, v72, v144\n\t"
"v_add_f32 v73, v73, v145\n\t"
"v_add_f32 v74, v74, v146\n\t"
"v_add_f32 v75, v75, v147\n\t"
"v_add_f32 v76, v76, v148\n\t"
"v_add_f32 v77, v77, v149\n\t"
"v_add_f32 v78, v78, v150\n\t"
"v_add_f32 v79, v79, v151\n\t"
"v_lshlrev_b32 v144, 16, v44\n\t"
"v_and_b32 v145, 0xffff0000, v44\n\t"
"v_lshlrev_b32 v146, 16, v45\n\t"
"v_and_b32 v147, 0xffff0000, v45\n\t"
"v_lshlrev_b32 v148, 16, v46\n\t"
"v_and_b32 v149, 0xffff0000, v46\n\t"
"v_lshlrev_b32 v150, 16, v47\n\t"
"v_and_b32 v151, 0xffff0000, v47\n\t"
"v_add_f32 v72, v72, v144\n\t"
"v_add_f32 v73, v73, v145\n\t"
"v_add_f32 v74, v74, v146\n\t"
"v_add_f32 v75, v75, v147\n\t"
"v_add_f32 v76, v76, v148\n\t"
"v_add_f32 v77, v77, v149\n\t"
"v_add_f32 v78, v78, v150\n\t"
"v_add_f32 v79, v79, v151\n\t"
"v_lshlrev_b32 v144, 16, v100\n\t"
"v_and_b32 v145, 0xffff0000, v100\n\t"
"v_lshlrev_b32 v146, 16, v101\n\t"
"v_and_b32 v147, 0xffff0000, v101\n\t"
"v_lshlrev_b32 v148, 16, v102\n\t"
"v_and_b32 v149, 0xffff0000, v102\n\t"
"v_lshlrev_b32 v150, 16, v103\n\t"
"v_and_b32 v151, 0xffff0000, v103\n\t"
"v_fma_f32 v144, v72, v64, v144\n\t"
"v_fma_f32 v145, v73, v65, v145\n\t"
"v_fma_f32 v146, v74, v66, v146\n\t"
"v_fma_f32 v147, v75, v67, v147\n\t"
"v_fma_f32 v148, v76, v68, v148\n\t"
"v_fma_f32 v149, v77, v69, v149\n\t"
"v_fma_f32 v150, v78, v70, v150\n\t"
"v_fma_f32 v151, v79, v71, v151\n\t"
"v_cvt_pk_bf16_f32 v164, v144, v145\n\t"
"v_cvt_pk_bf16_f32 v165, v146, v147\n\t"
"v_cvt_pk_bf16_f32 v166, v148, v149\n\t"
"v_cvt_pk_bf16_f32 v167, v150, v151\n\t"
"buffer_store_dwordx4 v[164:167], v251, s[52:55], 0 offen offset:0\n\t"
"s_branch LEND_%=\n\t"
"LRARE_%=:\n\t"
"v_max3_f32 v237, v144, v145, v146\n\t"
"v_max3_f32 v237, v237, v147, v152\n\t"
"v_max3_f32 v237, v237, v153, v154\n\t"
"v_max3_f32 v237, v237, v155, v160\n\t"
"v_max3_f32 v237, v237, v161, v162\n\t"
"v_max3_f32 v237, v237, v163, v168\n\t"
"v_max3_f32 v237, v237, v169, v170\n\t"
"v_max_f32 v237, v237, v171\n\t"
"v_mov_b32 v239, v237\n\t"
"s_nop 1\n\t"
"v_permlane16_swap_b32 v237, v239\n\t"
"s_nop 0\n\t"
"v_max_f32 v237, v237, v239\n\t"
"v_mov_b32 v239, v237\n\t"
"s_nop 1\n\t"
"v_permlane32_swap_b32 v237, v239\n\t"
"s_nop 0\n\t"
"v_max_f32 v237, v237, v239\n\t"
"v_max_f32 v237, 0, v237\n\t"
"v_sub_f32 v144, v144, v237\n\t"
"v_sub_f32 v145, v145, v237\n\t"
"v_sub_f32 v146, v146, v237\n\t"
"v_sub_f32 v147, v147, v237\n\t"
"v_sub_f32 v152, v152, v237\n\t"
"v_sub_f32 v153, v153, v237\n\t"
"v_sub_f32 v154, v154, v237\n\t"
"v_sub_f32 v155, v155, v237\n\t"
"v_sub_f32 v160, v160, v237\n\t"
"v_sub_f32 v161, v161, v237\n\t"
"v_sub_f32 v162, v162, v237\n\t"
"v_sub_f32 v163, v163, v237\n\t"
"v_sub_f32 v168, v168, v237\n\t"
"v_sub_f32 v169, v169, v237\n\t"
"v_sub_f32 v170, v170, v237\n\t"
"v_sub_f32 v171, v171, v237\n\t"
"v_sub_f32 v224, v224, v237\n\t"
"v_sub_f32 v225, v225, v237\n\t"
"v_sub_f32 v226, v226, v237\n\t"
"v_sub_f32 v227, v227, v237\n\t"
"v_exp_f32_e64 v237, -v237\n\t"
"s_nop 0\n\t"
"v_mul_f32 v234, v234, v237\n\t"
"v_max3_f32 v238, v148, v149, v150\n\t"
"v_max3_f32 v238, v238, v151, v156\n\t"
"v_max3_f32 v238, v238, v157, v158\n\t"
"v_max3_f32 v238, v238, v159, v164\n\t"
"v_max3_f32 v238, v238, v165, v166\n\t"
"v_max3_f32 v238, v238, v167, v172\n\t"
"v_max3_f32 v238, v238, v173, v174\n\t"
"v_max_f32 v238, v238, v175\n\t"
"v_mov_b32 v239, v238\n\t"
"s_nop 1\n\t"
"v_permlane16_swap_b32 v238, v239\n\t"
"s_nop 0\n\t"
"v_max_f32 v238, v238, v239\n\t"
"v_mov_b32 v239, v238\n\t"
"s_nop 1\n\t"
"v_permlane32_swap_b32 v238, v239\n\t"
"s_nop 0\n\t"
"v_max_f32 v238, v238, v239\n\t"
"v_max_f32 v238, 0, v238\n\t"
"v_sub_f32 v148, v148, v238\n\t"
"v_sub_f32 v149, v149, v238\n\t"
"v_sub_f32 v150, v150, v238\n\t"
"v_sub_f32 v151, v151, v238\n\t"
"v_sub_f32 v156, v156, v238\n\t"
"v_sub_f32 v157, v157, v238\n\t"
"v_sub_f32 v158, v158, v238\n\t"
"v_sub_f32 v159, v159, v238\n\t"
"v_sub_f32 v164, v164, v238\n\t"
"v_sub_f32 v165, v165, v238\n\t"
"v_sub_f32 v166, v166, v238\n\t"
"v_sub_f32 v167, v167, v238\n\t"
"v_sub_f32 v172, v172, v238\n\t"
"v_sub_f32 v173, v173, v238\n\t"
"v_sub_f32 v174, v174, v238\n\t"
"v_sub_f32 v175, v175, v238\n\t"
"v_sub_f32 v228, v228, v238\n\t"
"v_sub_f32 v229, v229, v238\n\t"
"v_sub_f32 v230, v230, v238\n\t"
"v_sub_f32 v231, v231, v238\n\t"
"v_exp_f32_e64 v238, -v238\n\t"
"s_nop 0\n\t"
"v_mul_f32 v235, v235, v238\n\t"
"v_mul_f32 v0, v0, v237\n\t"
"v_mul_f32 v1, v1, v237\n\t"
"v_mul_f32 v2, v2, v237\n\t"
"v_mul_f32 v3, v3, v237\n\t"
"v_mul_f32 v4, v4, v238\n\t"
"v_mul_f32 v5, v5, v238\n\t"
"v_mul_f32 v6, v6, v238\n\t"
"v_mul_f32 v7, v7, v238\n\t"
"v_mul_f32 v8, v8, v237\n\t"
"v_mul_f32 v9, v9, v237\n\t"
"v_mul_f32 v10, v10, v237\n\t"
"v_mul_f32 v11, v11, v237\n\t"
"v_mul_f32 v12, v12, v238\n\t"
"v_mul_f32 v13, v13, v238\n\t"
"v_mul_f32 v14, v14, v238\n\t"
"v_mul_f32 v15, v15, v238\n\t"
"v_mul_f32 v16, v16, v237\n\t"
"v_mul_f32 v17, v17, v237\n\t"
"v_mul_f32 v18, v18, v237\n\t"
"v_mul_f32 v19, v19, v237\n\t"
"v_mul_f32 v20, v20, v238\n\t"
"v_mul_f32 v21, v21, v238\n\t"
"v_mul_f32 v22, v22, v238\n\t"
"v_mul_f32 v23, v23, v238\n\t"
"v_mul_f32 v24, v24, v237\n\t"
"v_mul_f32 v25, v25, v237\n\t"
"v_mul_f32 v26, v26, v237\n\t"
"v_mul_f32 v27, v27, v237\n\t"
"v_mul_f32 v28, v28, v238\n\t"
"v_mul_f32 v29, v29, v238\n\t"
"v_mul_f32 v30, v30, v238\n\t"
"v_mul_f32 v31, v31, v238\n\t"
"v_mul_f32 v32, v32, v237\n\t"
"v_mul_f32 v33, v33, v237\n\t"
"v_mul_f32 v34, v34, v237\n\t"
"v_mul_f32 v35, v35, v237\n\t"
"v_mul_f32 v36, v36, v238\n\t"
"v_mul_f32 v37, v37, v238\n\t"
"v_mul_f32 v38, v38, v238\n\t"
"v_mul_f32 v39, v39, v238\n\t"
"v_mul_f32 v40, v40, v237\n\t"
"v_mul_f32 v41, v41, v237\n\t"
"v_mul_f32 v42, v42, v237\n\t"
"v_mul_f32 v43, v43, v237\n\t"
"v_mul_f32 v44, v44, v238\n\t"
"v_mul_f32 v45, v45, v238\n\t"
"v_mul_f32 v46, v46, v238\n\t"
"v_mul_f32 v47, v47, v238\n\t"
"v_mul_f32 v48, v48, v237\n\t"
"v_mul_f32 v49, v49, v237\n\t"
"v_mul_f32 v50, v50, v237\n\t"
"v_mul_f32 v51, v51, v237\n\t"
"v_mul_f32 v52, v52, v238\n\t"
"v_mul_f32 v53, v53, v238\n\t"
"v_mul_f32 v54, v54, v238\n\t"
"v_mul_f32 v55, v55, v238\n\t"
"v_mul_f32 v56, v56, v237\n\t"
"v_mul_f32 v57, v57, v237\n\t"
"v_mul_f32 v58, v58, v237\n\t"
"v_mul_f32 v59, v59, v237\n\t"
"v_mul_f32 v60, v60, v238\n\t"
"v_mul_f32 v61, v61, v238\n\t"
"v_mul_f32 v62, v62, v238\n\t"
"v_mul_f32 v63, v63, v238\n\t"
"v_mul_f32 v64, v64, v237\n\t"
"v_mul_f32 v65, v65, v237\n\t"
"v_mul_f32 v66, v66, v237\n\t"
"v_mul_f32 v67, v67, v237\n\t"
"v_mul_f32 v68, v68, v238\n\t"
"v_mul_f32 v69, v69, v238\n\t"
"v_mul_f32 v70, v70, v238\n\t"
"v_mul_f32 v71, v71, v238\n\t"
"v_mul_f32 v72, v72, v237\n\t"
"v_mul_f32 v73, v73, v237\n\t"
"v_mul_f32 v74, v74, v237\n\t"
"v_mul_f32 v75, v75, v237\n\t"
"v_mul_f32 v76, v76, v238\n\t"
"v_mul_f32 v77, v77, v238\n\t"
"v_mul_f32 v78, v78, v238\n\t"
"v_mul_f32 v79, v79, v238\n\t"
"v_mul_f32 v80, v80, v237\n\t"
"v_mul_f32 v81, v81, v237\n\t"
"v_mul_f32 v82, v82, v237\n\t"
"v_mul_f32 v83, v83, v237\n\t"
"v_mul_f32 v84, v84, v238\n\t"
"v_mul_f32 v85, v85, v238\n\t"
"v_mul_f32 v86, v86, v238\n\t"
"v_mul_f32 v87, v87, v238\n\t"
"v_mul_f32 v88, v88, v237\n\t"
"v_mul_f32 v89, v89, v237\n\t"
"v_mul_f32 v90, v90, v237\n\t"
"v_mul_f32 v91, v91, v237\n\t"
"v_mul_f32 v92, v92, v238\n\t"
"v_mul_f32 v93, v93, v238\n\t"
"v_mul_f32 v94, v94, v238\n\t"
"v_mul_f32 v95, v95, v238\n\t"
"s_cmp_eq_u32 s79, 7\n\t"
"s_cbranch_scc1 LSML_%=\n\t"
"s_branch LSM_%=\n\t"
"LPW_%=:\n\t"
"s_mov_b32 s83, 0x20000\n\t"
"s_branch LPSWF_%=\n\t"
"LPSW_%=:\n\t"
"s_sleep 2\n\t"
"LPSWF_%=:\n\t"
"s_sub_u32 s83, s83, 1\n\t"
"s_cbranch_scc1 LPSWX_%=\n\t"
"buffer_load_dword v242, v248, s[56:59], s22 offen offset:0 sc1\n\t"
"s_waitcnt vmcnt(0)\n\t"
"v_cmp_eq_u32 vcc, 0, v242\n\t"
"s_cbranch_vccnz LPSW_%=\n\t"
"LPSWX_%=:\n\t"
"s_branch LPWR_%=\n\t"
"LPH_%=:\n\t"
"s_mov_b32 s83, 0x20000\n\t"
"s_branch LPSHF_%=\n\t"
"LPSH_%=:\n\t"
"s_sleep 2\n\t"
"LPSHF_%=:\n\t"
"s_sub_u32 s83, s83, 1\n\t"
"s_cbranch_scc1 LPSHX_%=\n\t"
"buffer_load_dword v245, v243, s[56:59], s23 offen offset:0 sc1\n\t"
"s_waitcnt vmcnt(0)\n\t"
"v_cmp_eq_u32 vcc, 0, v245\n\t"
"s_cbranch_vccnz LPSH_%=\n\t"
"LPSHX_%=:\n\t"
"s_branch LPHR_%=\n\t"
"LEND_%=:\n\t"

    : "+{v[240:241]}"(v240), "+{v247}"(v247), "+{v254}"(v254), "+{s[60:63]}"(s60), "+{s[64:65]}"(s64), "+{s[4:5]}"(pX), "+{s[6:7]}"(pOut), "+{s34}"(s34),
      "+{s[36:39]}"(s36), "+{s[48:49]}"(pK), "+{s[52:53]}"(pV), "+{s[92:93]}"(pO)
    : "{s[56:59]}"(srdA), "{s[40:43]}"(srdQ), "{s[96:99]}"(srdT)
    : "memory", "scc", "vcc", "v0", "v1", "v2", "v3", "v4", "v5", "v6", "v7", "v8", "v9", "v10", "v11", "v12", "v13", "v14", "v15", "v16", "v17", "v18", "v19", "v20", "v21", "v22", "v23", "v24", "v25", "v26", "v27", "v28", "v29", "v30", "v31", "v32", "v33", "v34", "v35", "v36", "v37", "v38", "v39", "v40", "v41", "v42", "v43", "v44", "v45", "v46", "v47", "v48", "v49", "v50", "v51", "v52", "v53", "v54", "v55", "v56", "v57", "v58", "v59", "v60", "v61", "v62", "v63", "v64", "v65", "v66", "v67", "v68", "v69", "v70", "v71", "v72", "v73", "v74", "v75", "v76", "v77", "v78", "v79", "v80", "v81", "v82", "v83", "v84", "v85", "v86", "v87", "v88", "v89", "v90", "v91", "v92", "v93", "v94", "v95", "v96", "v97", "v98", "v99", "v100", "v101", "v102", "v103", "v104", "v105", "v106", "v107", "v108", "v109", "v110", "v111", "v112", "v113", "v114", "v115", "v116", "v117", "v118", "v119", "v120", "v121", "v122", "v123", "v124", "v125", "v126", "v127", "v128", "v129", "v130", "v131", "v132", "v133", "v134", "v135", "v136", "v137", "v138", "v139", "v140", "v141", "v142", "v143", "v144", "v145", "v146", "v147", "v148", "v149", "v150", "v151", "v152", "v153", "v154", "v155", "v156", "v157", "v158", "v159", "v160", "v161", "v162", "v163", "v164", "v165", "v166", "v167", "v168", "v169", "v170", "v171", "v172", "v173", "v174", "v175", "v176", "v177", "v178", "v179", "v180", "v181", "v182", "v183", "v184", "v185", "v186", "v187", "v188", "v189", "v190", "v191", "v192", "v193", "v194", "v195", "v196", "v197", "v198", "v199", "v200", "v201", "v202", "v203", "v204", "v205", "v206", "v207", "v208", "v209", "v210", "v211", "v212", "v213", "v214", "v215", "v216", "v217", "v218", "v219", "v220", "v221", "v222", "v223", "v224", "v225", "v226", "v227", "v228", "v229", "v230", "v231", "v232", "v233", "v234", "v235", "v236", "v237", "v238", "v239", "v242", "v243", "v244", "v245", "v246", "v248", "v249", "v250", "v251", "v252", "v253", "v255", "s8", "s9", "s10", "s11", "s12", "s13", "s14", "s15", "s16", "s17", "s18", "s19", "s20", "s21", "s22", "s23", "s24", "s25", "s26", "s27", "s28", "s29", "s30", "s31", "s32", "s33", "s35", "s44", "s45", "s46", "s47", "s50", "s51", "s54", "s55", "s66", "s67", "s68", "s69", "s70", "s71", "s72", "s73", "s74", "s75", "s76", "s77", "s78", "s79", "s80", "s81", "s82", "s83", "s84", "s85", "s86", "s87", "s88", "s89", "s90", "s91", "s94", "s95"
  );
}

extern "C" void kernel_launch(void* const* d_in, const int* in_sizes, int n_in, void* d_out, int out_size, void* d_ws, size_t ws_size, hipStream_t stream) {
  const u16* x = (const u16*)d_in[0]; const u16* kc = (const u16*)d_in[1]; const u16* vc = (const u16*)d_in[2];
  const u16* wq = (const u16*)d_in[3]; const u16* wo = (const u16*)d_in[4];
  const u16* wg = (const u16*)d_in[6]; const u16* wu = (const u16*)d_in[7]; const u16* wd = (const u16*)d_in[8];
  char* ws = (char*)d_ws;
  u16* xf = (u16*)(ws + WS_XF); u16* guf = (u16*)(ws + WS_GU);
  (void)hipFuncSetAttribute((const void*)k_main, hipFuncAttributeMaxDynamicSharedMemorySize, LDS_A);
  k_pre<<<160 + 256, 512, 0, stream>>>(x, wg, wu, (u32x4*)xf, (u32x4*)guf, (unsigned*)(ws + WS_CNT));
  k_main<<<256, 512, LDS_A, stream>>>(ws, wq, kc, vc, wo, wd, x, (u16*)d_out);
}
```
